```python
import torch

def _softmax(x, axis=-1):
    return torch.softmax(x, dim=axis)
def _gelu(x):
    return torch.nn.functional.gelu(x, approximate="tanh")
def _silu(x):
    return torch.nn.functional.silu(x)
def _sigmoid(x):
    return torch.sigmoid(x)
def _layernorm(x, axis=-1, eps=1e-5):
    m = x.mean(dim=axis, keepdim=True)
    v = x.var(dim=axis, keepdim=True, unbiased=False)
    return (x - m) / torch.sqrt(v + eps)
def _scatter_add(tgt, idx, src, axis=0):
    return tgt.clone().index_add_(axis, idx, src)
def _segment_sum(src, seg, n):
    out = torch.zeros((n,) + src.shape[1:], dtype=src.dtype)
    return out.index_add_(0, seg, src)
def _pad(x, pw):
    flat = tuple(v for pair in reversed(pw) for v in pair)
    return torch.nn.functional.pad(x, flat)

kernel_name = 'parallel_sum__grouped_kv_mqa__glu_gated_reglu__prompt'

def setup_inputs():

    _w = (lambda a: (lambda t: t.to(torch.bfloat16) if t.is_floating_point() else t)(torch.as_tensor(a)))
    def _A_setup():
        B, S, D, H = 1, 2048, 1024, 32
        Dh = D // H
        return {
            "x": _w((lambda *s: torch.randn(*s, dtype=torch.bfloat16) * 0.05)(B, S, D)),
            "wq": _w((lambda *s: torch.randn(*s, dtype=torch.bfloat16) * 0.05)(D, H * Dh)),
            "wk": _w((lambda *s: torch.randn(*s, dtype=torch.bfloat16) * 0.05)(D, Dh)),
            "wv": _w((lambda *s: torch.randn(*s, dtype=torch.bfloat16) * 0.05)(D, Dh)),
            "wo": _w((lambda *s: torch.randn(*s, dtype=torch.bfloat16) * 0.05)(H * Dh, D)),
        }
    _A_in = _A_setup()

    def _B_setup():
        B, S, D, F = 1, 2048, 1024, 3584
        return {
            "x": _w((lambda *s: torch.randn(*s, dtype=torch.bfloat16) * 0.05)(B, S, D)),
            "W_gate": _w((lambda *s: torch.randn(*s, dtype=torch.bfloat16) * 0.8)(D, F) * (2.0 / D) ** 0.5),
            "W_up": _w((lambda *s: torch.randn(*s, dtype=torch.bfloat16) * 0.8)(D, F) * (2.0 / D) ** 0.5),
            "W_down": _w((lambda *s: torch.randn(*s, dtype=torch.bfloat16) * 0.8)(F, D) * (1.0 / F) ** 0.5),
        }
    _B_in = _B_setup()

    return {
        "A_x": _A_in["x"],
        "A_wq": _A_in["wq"],
        "A_wk": _A_in["wk"],
        "A_wv": _A_in["wv"],
        "A_wo": _A_in["wo"],
        "B_x": _B_in["x"],
        "B_W_gate": _B_in["W_gate"],
        "B_W_up": _B_in["W_up"],
        "B_W_down": _B_in["W_down"],
    }

def reference(A_x, A_wq, A_wk, A_wv, A_wo, B_x, B_W_gate, B_W_up, B_W_down):
    def _A_ref(x, wq, wk, wv, wo):
        B, S, D = x.shape
        H = 32
        Dh = D // H
        q = (lambda x, *d: x.permute(*d))((x @ wq).reshape(B, S, H, Dh), 0, 2, 1, 3)
        k = x @ wk
        v = x @ wv
        scores = torch.einsum("bhqd,bkd->bhqk", q, k) * (Dh ** -0.5)
        p = _softmax(scores, axis=-1)
        o = torch.einsum("bhqk,bkd->bhqd", p, v)
        return (lambda x, *d: x.permute(*d))(o, 0, 2, 1, 3).reshape(B, S, H * Dh) @ wo

    def _B_ref(x, W_gate, W_up, W_down):
        gate = torch.einsum("bsd,df->bsf", x, W_gate)
        up = torch.einsum("bsd,df->bsf", x, W_up)
        gate = gate * (lambda x, dt: x.to(dt))(gate > 0, gate.dtype)
        h = gate * up
        return torch.einsum("bsf,fd->bsd", h, W_down)

    x = A_x
    xn = _layernorm(x, axis=-1)
    return x + _A_ref(xn, A_wq, A_wk, A_wv, A_wo) + _B_ref(xn, B_W_gate, B_W_up, B_W_down)
```

```cpp
#include <hip/hip_runtime.h>
#include <hip/hip_bf16.h>
#include <stdint.h>
typedef unsigned short u16;
__device__ __forceinline__ float bf2f(u16 v){ return __uint_as_float(((unsigned)v)<<16); }
__device__ __forceinline__ u16 f2bf(float f){ unsigned u=__float_as_uint(f); u += 0x7fffu + ((u>>16)&1u); return (u16)(u>>16); }
constexpr int S=2048, D=1024, H=32, DH=32, F=3584;

__global__ void ln_k(const u16* x, u16* xn){
  int row=blockIdx.x; int t=threadIdx.x; __shared__ float red[256];
  float v[4]; float s=0; for(int i=0;i<4;i++){ v[i]=bf2f(x[row*D+t*4+i]); s+=v[i]; }
  red[t]=s; __syncthreads(); for(int o=128;o>0;o>>=1){ if(t<o) red[t]+=red[t+o]; __syncthreads(); }
  float m=red[0]/D; __syncthreads();
  float q=0; for(int i=0;i<4;i++){ float d=v[i]-m; q+=d*d; }
  red[t]=q; __syncthreads(); for(int o=128;o>0;o>>=1){ if(t<o) red[t]+=red[t+o]; __syncthreads(); }
  float r=1.0f/sqrtf(red[0]/D+1e-5f);
  for(int i=0;i<4;i++) xn[row*D+t*4+i]=f2bf((v[i]-m)*r);
}
__global__ void gemm_k(const u16* A, int lda, const u16* B, int ldb, float* C, int ldc, int M, int N, int K, int accum){
  __shared__ float As[16][65], Bs[16][65];
  int tx=threadIdx.x&15, ty=threadIdx.x>>4; int m0=blockIdx.y*64, n0=blockIdx.x*64;
  float acc[4][4]={};
  for(int k0=0;k0<K;k0+=16){
    for(int i=threadIdx.x;i<1024;i+=256){ int r=i>>4,c=i&15; As[c][r]=bf2f(A[(m0+r)*lda+k0+c]); int kr=i>>6, nc=i&63; Bs[kr][nc]=(n0+nc<N)?bf2f(B[(k0+kr)*ldb+n0+nc]):0.f; }
    __syncthreads();
    for(int k=0;k<16;k++){ float a[4],b[4]; for(int i=0;i<4;i++){a[i]=As[k][ty*4+i]; b[i]=Bs[k][tx*4+i];} for(int i=0;i<4;i++)for(int j=0;j<4;j++) acc[i][j]+=a[i]*b[j]; }
    __syncthreads();
  }
  for(int i=0;i<4;i++)for(int j=0;j<4;j++){ int n=n0+tx*4+j; if(n<N){ float* p=&C[(m0+ty*4+i)*ldc+n]; *p = accum? *p+acc[i][j] : acc[i][j]; } }
}
__global__ void cvt_k(const float* a, u16* o, int n){ int i=blockIdx.x*256+threadIdx.x; if(i<n) o[i]=f2bf(a[i]); }
__global__ void glu_k(const float* g, const float* u, u16* h, int n){ int i=blockIdx.x*256+threadIdx.x; if(i<n){ float gg=bf2f(f2bf(g[i])), uu=bf2f(f2bf(u[i])); h[i]=f2bf((gg>0?gg:0.f)*uu);} }
__global__ void attn_k(const u16* q, const u16* k, const u16* v, u16* o){
  int idx=blockIdx.x*64+threadIdx.x; int tok=idx>>5, h=idx&31;
  __shared__ float ks[64][32], vs[64][32];
  float qq[32]; for(int d=0;d<32;d++) qq[d]=bf2f(q[tok*D+h*32+d])*0.17677669529663687f;
  float m=-1e30f,l=0,acc[32]; for(int d=0;d<32;d++) acc[d]=0;
  for(int j0=0;j0<S;j0+=64){
    __syncthreads();
    for(int i=threadIdx.x;i<2048;i+=64){ ks[i>>5][i&31]=bf2f(k[j0*32+i]); vs[i>>5][i&31]=bf2f(v[j0*32+i]); }
    __syncthreads();
    for(int j=0;j<64;j++){ float s=0; for(int d=0;d<32;d++) s+=qq[d]*ks[j][d]; float mn=fmaxf(m,s); float a=__expf(m-mn), p=__expf(s-mn); l=l*a+p; for(int d=0;d<32;d++) acc[d]=acc[d]*a+p*vs[j][d]; m=mn; }
  }
  for(int d=0;d<32;d++) o[tok*D+h*32+d]=f2bf(acc[d]/l);
}
__global__ void fin_k(const u16* x, const float* a, const float* b, u16* out, int n){ int i=blockIdx.x*256+threadIdx.x; if(i<n) out[i]=f2bf(bf2f(x[i])+a[i]+b[i]); }

extern "C" void kernel_launch(void* const* d_in, const int* in_sizes, int n_in, void* d_out, int out_size, void* d_ws, size_t ws_size, hipStream_t stream){
  const u16* x=(const u16*)d_in[0]; const u16* wq=(const u16*)d_in[1]; const u16* wk=(const u16*)d_in[2]; const u16* wv=(const u16*)d_in[3]; const u16* wo=(const u16*)d_in[4];
  const u16* wg=(const u16*)d_in[6]; const u16* wu=(const u16*)d_in[7]; const u16* wd=(const u16*)d_in[8];
  char* ws=(char*)d_ws;
  u16* xn=(u16*)ws; ws+=S*D*2; u16* q=(u16*)ws; ws+=S*D*2; u16* k=(u16*)ws; ws+=S*32*2; u16* v=(u16*)ws; ws+=S*32*2; u16* o=(u16*)ws; ws+=S*D*2; u16* h=(u16*)ws; ws+=S*F*2;
  float* t0=(float*)ws; ws+=S*F*4; float* t1=(float*)ws; ws+=S*F*4;
  ln_k<<<S,256,0,stream>>>(x,xn);
  gemm_k<<<dim3(D/64,S/64),256,0,stream>>>(xn,D,wq,D,t0,D,S,D,D,0); cvt_k<<<S*D/256,256,0,stream>>>(t0,q,S*D);
  gemm_k<<<dim3(1,S/64),256,0,stream>>>(xn,D,wk,32,t0,32,S,32,D,0); cvt_k<<<S*32/256,256,0,stream>>>(t0,k,S*32);
  gemm_k<<<dim3(1,S/64),256,0,stream>>>(xn,D,wv,32,t0,32,S,32,D,0); cvt_k<<<S*32/256,256,0,stream>>>(t0,v,S*32);
  gemm_k<<<dim3(F/64,S/64),256,0,stream>>>(xn,D,wg,F,t0,F,S,F,D,0);
  gemm_k<<<dim3(F/64,S/64),256,0,stream>>>(xn,D,wu,F,t1,F,S,F,D,0);
  glu_k<<<S*F/256,256,0,stream>>>(t0,t1,h,S*F);
  attn_k<<<S*H/64,64,0,stream>>>(q,k,v,o);
  gemm_k<<<dim3(D/64,S/64),256,0,stream>>>(o,D,wo,D,t0,D,S,D,D,0);
  gemm_k<<<dim3(D/64,S/64),256,0,stream>>>(h,F,wd,D,t1,D,S,D,F,0);
  fin_k<<<S*D/256,256,0,stream>>>(x,t0,t1,(u16*)d_out,S*D);
}
```

```cpp
#include <hip/hip_runtime.h>

typedef unsigned short u16;

__global__ __launch_bounds__(256, 1) void fused_block(const u16* x, const u16* wq, const u16* wk, const u16* wv, const u16* wo, const u16* wg, const u16* wu,
                                                      const u16* wd, char* ws, u16* out) {
  __shared__ __attribute__((aligned(1024))) char lds[163840];
  asm volatile(
"s_load_dwordx2 s[40:41], s[0:1], 0x0\n"
"s_load_dwordx8 s[64:71], s[0:1], 0x8\n"
"s_load_dwordx8 s[72:79], s[0:1], 0x28\n"
"s_load_dwordx2 s[44:45], s[0:1], 0x48\n"
"s_getpc_b64 s[84:85]\n"
"Lkpc0:\n"
"s_and_b32 s85, s85, 0xffff\n"
"s_mov_b32 s86, Lkend0-Lkpc0\n"
"s_mov_b32 s87, 0x20000\n"
"s_lshr_b32 s90, s2, 3\n"
"s_movk_i32 s91, 2048\n"
"s_mul_i32 s90, s90, s91\n"
"v_lshrrev_b32 v4, 4, v0\n"
"v_lshlrev_b32 v4, 7, v4\n"
"buffer_load_dword v224, v4, s[84:87], s90 offen\n"
"v_readfirstlane_b32 s48, v0\n"
"s_and_b32 s3, s2, 63\n"
"s_lshr_b32 s4, s2, 6\n"
"s_lshr_b32 s48, s48, 6\n"
"s_lshl_b32 s3, s3, 2\n"
"s_add_u32 s3, s3, s4\n"
"s_and_b32 s49, s3, 1\n"
"s_lshr_b32 s5, s3, 1\n"
"s_lshl_b32 s6, s5, 7\n"
"s_lshl_b32 s7, s48, 1\n"
"s_add_u32 s7, s7, s49\n"
"s_add_u32 s6, s6, s7\n"
"s_lshl_b32 s92, s6, 8\n"
"s_add_u32 s93, s92, 16384\n"
"s_xor_b32 s94, s92, 256\n"
"s_xor_b32 s95, s93, 256\n"
"s_lshl_b32 s89, s48, 13\n"
"s_add_u32 s89, s89, 32768\n"
"s_mov_b32 s42, 0x400000\n"
"s_mov_b32 s43, 0x20000\n"
"v_and_b32 v1, 63, v0\n"
"v_lshlrev_b32 v18, 4, v1\n"
"v_lshrrev_b32 v3, 3, v1\n"
"v_and_b32 v5, 6, v3\n"
"v_and_b32 v6, 7, v1\n"
"v_xor_b32 v5, v5, v6\n"
"v_lshlrev_b32 v5, 4, v5\n"
"v_lshl_add_u32 v3, v3, 11, v5\n"
"s_and_b32 s6, s48, 1\n"
"s_cmp_eq_u32 s6, 0\n"
"s_cselect_b32 s14, 7168, s91\n"
"s_cselect_b32 s15, 6, 1\n"
"s_lshl_b32 s54, s14, 5\n"
"s_lshr_b32 s6, s14, 5\n"
"s_mul_i32 s6, s6, s4\n"
"s_sub_u32 s6, 4096, s6\n"
"v_lshrrev_b32 v5, 4, v1\n"
"v_bfe_u32 v6, v1, 1, 3\n"
"v_min_u32 v6, s15, v6\n"
"v_lshl_add_u32 v6, v6, 7, s6\n"
"s_lshr_b32 s6, s90, 9\n"
"v_add_u32 v7, s6, v5\n"
"v_mad_u32_u24 v8, v7, s14, v6\n"
"v_lshrrev_b32 v4, 3, v0\n"
"v_lshlrev_b32 v4, 7, v4\n"
"s_lshr_b32 s6, s3, 5\n"
"s_lshr_b32 s15, s2, 7\n"
"s_lshl_b32 s15, s15, 3\n"
"s_add_u32 s6, s6, s15\n"
"s_lshl_b32 s15, s6, 12\n"
"s_mov_b32 m0, s89\n"
"s_waitcnt lgkmcnt(0)\n"
"s_and_b32 s41, s41, 0xffff\n"
"buffer_load_dwordx4 v3, s[40:43], s92 offen offset:0 lds\n"
"s_addk_i32 m0, 1024\n"
"s_mov_b32 s16, s78\n"
"buffer_load_dwordx4 v3, s[40:43], s93 offen offset:0 lds\n"
"s_addk_i32 m0, 896\n"
"s_and_b32 s17, s79, 0xffff\n"
"buffer_load_dwordx4 v3, s[40:43], s92 offen offset:128 lds\n"
"s_addk_i32 m0, 1024\n"
"s_mov_b32 s18, 41943040\n"
"buffer_load_dwordx4 v3, s[40:43], s93 offen offset:128 lds\n"
"s_addk_i32 m0, 1152\n"
"s_mov_b32 s19, s43\n"
"buffer_load_dwordx4 v3, s[40:43], s94 offen offset:0 lds\n"
"s_addk_i32 m0, 1024\n"
"s_and_b32 s6, s3, 31\n"
"buffer_load_dwordx4 v3, s[40:43], s95 offen offset:0 lds\n"
"s_addk_i32 m0, 896\n"
"s_mul_i32 s80, s6, 224\n"
"buffer_load_dwordx4 v3, s[40:43], s94 offen offset:128 lds\n"
"s_addk_i32 m0, 1024\n"
"s_lshl_b32 s81, s6, 6\n"
"buffer_load_dwordx4 v3, s[40:43], s95 offen offset:128 lds\n"
"s_mov_b32 s96, 5115904\n"
"buffer_load_dwordx2 v[228:229], off, s[16:19], s96 sc1\n"
"s_cmp_lt_u32 s48, 2\n"
"s_cselect_b32 s20, s72, s74\n"
"s_cselect_b32 s21, s73, s75\n"
"s_sub_u32 s22, 7344128, s80\n"
"s_add_u32 s20, s20, s80\n"
"s_addc_u32 s21, s21, 0\n"
"s_sub_u32 s20, s20, 4096\n"
"s_subb_u32 s21, s21, 0\n"
"s_sub_u32 s80, 2101248, s81\n"
"s_add_u32 s64, s64, s81\n"
"s_addc_u32 s65, s65, 0\n"
"s_sub_u32 s64, s64, 4096\n"
"s_subb_u32 s65, s65, 0\n"
"s_cmp_eq_u32 s14, s91\n"
"s_cselect_b32 s24, s64, s20\n"
"s_cselect_b32 s25, s65, s21\n"
"s_cselect_b32 s26, s80, s22\n"
"s_and_b32 s25, s25, 0xffff\n"
"s_mov_b32 s27, s43\n"
"s_cmp_eq_u32 s49, 0\n"
"s_cselect_b32 s8, s66, s68\n"
"s_cselect_b32 s9, s67, s69\n"
"s_and_b32 s9, s9, 0xffff\n"
"s_mov_b32 s10, 65536\n"
"s_mov_b32 s11, s43\n"
"s_lshl_b32 s14, s14, 7\n"
"s_and_b32 s21, s21, 0xffff\n"
"s_mov_b32 s23, s43\n"
"s_mov_b32 s32, s76\n"
"s_mov_b32 s33, s77\n"
"s_mov_b32 s36, s70\n"
"s_mov_b32 s37, s71\n"
"s_mov_b32 s61, s2\n"
"s_lshl_b32 s55, s48, 12\n"
"s_lshl_b32 s12, s48, 14\n"
"s_lshl_b32 s13, s7, 13\n"
"s_mov_b32 s53, 229376\n"
"s_lshr_b32 s6, s3, 5\n"
"s_lshl_b32 s6, s6, 3\n"
"s_add_u32 s6, s6, s7\n"
"s_lshl_b32 s6, s6, 6\n"
"s_and_b32 s5, s5, 15\n"
"s_add_u32 s6, s6, s5\n"
"s_lshl_b32 s50, s6, 10\n"
"s_lshl_b32 s6, s3, 2\n"
"s_add_u32 s52, s6, 4456448\n"
"s_lshl_b32 s6, s3, 1\n"
"s_lshr_b32 s5, s48, 1\n"
"s_add_u32 s6, s6, s5\n"
"s_lshl_b32 s6, s6, 5\n"
"s_and_b32 s5, s48, 1\n"
"s_lshl_b32 s5, s5, 3\n"
"s_add_u32 s6, s6, s5\n"
"s_add_u32 s51, s6, 4521984\n"
"v_and_b32 v6, 15, v1\n"
"v_and_b32 v7, 6, v1\n"
"v_xor_b32 v7, v7, v5\n"
"v_lshlrev_b32 v7, 4, v7\n"
"v_lshl_add_u32 v7, v6, 7, v7\n"
"v_add_u32 v2, s89, v7\n"
"v_lshlrev_b32 v6, 5, v6\n"
"v_add_u32 v21, 90112, v6\n"
"s_lshl_b32 s5, s48, 3\n"
"v_add_u32 v20, s5, v21\n"
"s_mov_b32 s82, 0\n"
"s_mov_b32 s83, 0\n"
"s_mov_b32 s88, 0x3f803f80\n"
"s_mov_b32 s91, 0xffff0000\n"
"v_mov_b32 v32, 0\n"
"v_mov_b32 v33, 0\n"
"v_mov_b32 v34, 0\n"
"v_mov_b32 v35, 0\n"
"v_mov_b32 v14, 0\n"
"v_mov_b32 v15, 0\n"
"v_mov_b32 v16, 0\n"
"v_mov_b32 v17, 0\n"
"v_xor_b32 v0, 64, v2\n"
"v_lshrrev_b32 v30, 4, v18\n"
"v_and_b32 v30, 32, v30\n"
"v_xor_b32 v30, v30, v18\n"
"s_and_b32 s84, s55, 4096\n"
"s_lshl_b32 s85, s49, 1\n"
"s_movk_i32 s86, 7168\n"
"v_add_u32 v24, s84, v18\n"
"v_lshrrev_b32 v24, 4, v24\n"
"v_mul_u32_u24 v25, 4682, v24\n"
"v_mfma_f32_32x32x16_bf16 v[128:143], v[14:17], v[14:17], 0\n"
"v_lshrrev_b32 v25, 16, v25\n"
"v_mul_u32_u24 v26, 14, v25\n"
"v_sub_u32 v26, v24, v26\n"
"v_add_u32 v26, s85, v26\n"
"v_subrev_u32 v27, 14, v26\n"
"v_min_u32 v26, v26, v27\n"
"v_mfma_f32_32x32x16_bf16 v[144:159], v[14:17], v[14:17], 0\n"
"v_and_b32 v27, 12, v25\n"
"v_add_u32 v27, v27, v25\n"
"v_lshrrev_b32 v25, 4, v25\n"
"v_mul_u32_u24 v25, 12, v25\n"
"v_sub_u32 v27, v27, v25\n"
"v_lshlrev_b32 v26, 4, v26\n"
"v_mfma_f32_32x32x16_bf16 v[160:175], v[14:17], v[14:17], 0\n"
"v_mad_u32_u24 v26, v27, s86, v26\n"
"v_add_u32 v10, 4096, v26\n"
"v_add_u32 v24, 64, v24\n"
"v_mul_u32_u24 v25, 4682, v24\n"
"v_lshrrev_b32 v25, 16, v25\n"
"v_mul_u32_u24 v26, 14, v25\n"
"v_mfma_f32_32x32x16_bf16 v[176:191], v[14:17], v[14:17], 0\n"
"v_sub_u32 v26, v24, v26\n"
"v_add_u32 v26, s85, v26\n"
"v_subrev_u32 v27, 14, v26\n"
"v_min_u32 v26, v26, v27\n"
"v_and_b32 v27, 12, v25\n"
"v_add_u32 v27, v27, v25\n"
"v_mfma_f32_32x32x16_bf16 a[192:207], v[14:17], v[14:17], 0\n"
"v_lshrrev_b32 v25, 4, v25\n"
"v_mul_u32_u24 v25, 12, v25\n"
"v_sub_u32 v27, v27, v25\n"
"v_lshlrev_b32 v26, 4, v26\n"
"v_mad_u32_u24 v26, v27, s86, v26\n"
"v_add_u32 v11, 3072, v26\n"
"v_mfma_f32_32x32x16_bf16 a[208:223], v[14:17], v[14:17], 0\n"
"v_add_u32 v24, 64, v24\n"
"v_mul_u32_u24 v25, 4682, v24\n"
"v_lshrrev_b32 v25, 16, v25\n"
"v_mul_u32_u24 v26, 14, v25\n"
"v_sub_u32 v26, v24, v26\n"
"v_add_u32 v26, s85, v26\n"
"v_subrev_u32 v27, 14, v26\n"
"v_min_u32 v26, v26, v27\n"
"v_and_b32 v27, 12, v25\n"
"v_add_u32 v27, v27, v25\n"
"v_lshrrev_b32 v25, 4, v25\n"
"v_mul_u32_u24 v25, 12, v25\n"
"v_sub_u32 v27, v27, v25\n"
"v_lshlrev_b32 v26, 4, v26\n"
"v_mad_u32_u24 v26, v27, s86, v26\n"
"v_add_u32 v12, 2048, v26\n"
"v_add_u32 v24, 64, v24\n"
"v_mul_u32_u24 v25, 4682, v24\n"
"v_lshrrev_b32 v25, 16, v25\n"
"v_mul_u32_u24 v26, 14, v25\n"
"v_sub_u32 v26, v24, v26\n"
"v_add_u32 v26, s85, v26\n"
"v_subrev_u32 v27, 14, v26\n"
"v_min_u32 v26, v26, v27\n"
"v_and_b32 v27, 12, v25\n"
"v_add_u32 v27, v27, v25\n"
"v_lshrrev_b32 v25, 4, v25\n"
"v_mul_u32_u24 v25, 12, v25\n"
"v_sub_u32 v27, v27, v25\n"
"v_lshlrev_b32 v26, 4, v26\n"
"v_mad_u32_u24 v26, v27, s86, v26\n"
"v_add_u32 v13, 1024, v26\n"
"v_lshrrev_b32 v24, 8, v18\n"
"v_bfe_u32 v25, v18, 6, 2\n"
"v_lshl_add_u32 v25, v24, 3, v25\n"
"s_lshr_b32 s85, s48, 1\n"
"s_lshl_b32 s85, s85, 13\n"
"s_add_u32 s85, s85, 1024\n"
"v_and_b32 v26, 1, v24\n"
"v_lshlrev_b32 v27, 5, v26\n"
"v_and_b32 v9, 48, v18\n"
"v_xor_b32 v27, v27, v9\n"
"v_lshl_add_u32 v27, v25, 11, v27\n"
"v_add_u32 v27, s85, v27\n"
"v_cmp_eq_u32 vcc, s84, v14\n"
"v_lshrrev_b32 v9, 1, v9\n"
"v_lshl_add_u32 v9, v25, 6, v9\n"
"v_cndmask_b32 v13, v27, v13, vcc\n"
"s_add_u32 s85, s12, 98304\n"
"v_lshl_add_u32 v9, v26, 5, v9\n"
"v_add_u32 v19, s85, v9\n"
"s_sub_u32 s84, s52, 4456448\n"
"s_lshr_b32 s85, s84, 4\n"
"s_bfe_u32 s84, s84, 0x10003\n"
"s_sub_u32 s87, s85, 23\n"
"s_cmp_lt_u32 s87, 25\n"
"s_cselect_b32 s87, 6, 0\n"
"s_mul_i32 s87, s87, s49\n"
"s_add_u32 s85, s85, s49\n"
"s_add_u32 s85, s85, 63\n"
"s_and_b32 s85, s85, 63\n"
"s_lshl_b32 s85, s85, 12\n"
"s_lshl_b32 s86, s49, 1\n"
"s_add_u32 s86, s86, 8\n"
"s_lshl_b32 s84, s84, s86\n"
"s_add_u32 s85, s85, s84\n"
"s_lshl_b32 s84, s49, 11\n"
"s_add_u32 s85, s85, s84\n"
"s_add_u32 s85, s85, 4194304\n"
"s_lshl_b32 s84, s48, 28\n"
"s_add_u32 s85, s85, s84\n"
"s_mul_i32 s84, s49, 7\n"
"s_sub_u32 s84, 10, s84\n"
"s_add_u32 s84, s84, s87\n"
"s_sub_u32 s87, 9, s87\n"
"v_lshlrev_b32 v26, s84, v26\n"
"v_lshrrev_b32 v24, 1, v24\n"
"v_lshlrev_b32 v24, s87, v24\n"
"v_and_b32 v25, 0xf0, v18\n"
"v_add_u32 v24, v24, v26\n"
"v_add_u32 v24, v24, v25\n"
"v_add_u32 v22, s85, v24\n"
"s_mul_i32 s84, s49, 248\n"
"s_add_u32 s84, s84, 8\n"
"v_add_u32 v23, s84, v22\n"
"s_sub_u32 s84, s52, 4456448\n"
"s_lshl_b32 s60, s84, 1\n"
"s_and_b32 s56, s51, 0xfffff800\n"
"s_add_u32 s57, s60, 4587520\n"
"s_lshr_b32 s85, s84, 7\n"
"s_lshl_b32 s86, s85, 19\n"
"s_add_u32 s28, s16, s86\n"
"s_addc_u32 s29, s17, 0\n"
"s_mov_b32 s30, 524288\n"
"s_mov_b32 s31, 0x20000\n"
"s_lshl_b32 s85, s85, 1\n"
"s_lshr_b32 s86, s48, 1\n"
"s_add_u32 s85, s85, s86\n"
"s_mul_i32 s85, s85, 1179648\n"
"s_and_b32 s86, s55, 4096\n"
"s_add_u32 s85, s85, s86\n"
"s_add_u32 s85, s85, 5242880\n"
"s_lshr_b32 s86, s84, 7\n"
"s_lshl_b32 s86, s86, 2\n"
"s_add_u32 s86, s86, s48\n"
"s_lshl_b32 s86, s86, 8\n"
"s_and_b32 s87, s60, 255\n"
"s_add_u32 s86, s86, s87\n"
"s_lshl_b32 s86, s86, 0\n"
"s_add_u32 s63, s86, 4591616\n"
"s_and_b32 s84, s84, 127\n"
"s_lshl_b32 s86, s84, 11\n"
"s_add_u32 s76, s85, s86\n"
"s_lshr_b32 s84, s84, 3\n"
"s_mul_i32 s84, s84, 57344\n"
"s_add_u32 s85, s85, s84\n"
"s_lshl_b32 s84, s49, 15\n"
"s_add_u32 s84, s84, 262144\n"
"s_add_u32 s58, s85, s84\n"
"s_lshl_b32 s84, s49, 3\n"
"s_add_u32 s84, s84, 286720\n"
"s_add_u32 s59, s85, s84\n"
"s_and_b32 s84, s61, 3\n"
"s_bfe_u32 s85, s61, 0x20003\n"
"buffer_load_dword v225, v8, s[24:27], 0 offen\n"
"buffer_load_dword v226, v4, s[8:11], s15 offen\n"
"buffer_load_dword v227, v8, s[24:27], s14 offen\n"
"s_and_b32 s86, s61, 4\n"
"s_add_u32 s85, s85, s86\n"
"s_lshl_b32 s86, s85, 8\n"
"s_add_u32 s64, s86, 4589568\n"
"s_lshl_b32 s87, s84, 6\n"
"s_add_u32 s86, s86, s87\n"
"s_lshr_b32 s86, s86, 7\n"
"s_lshl_b32 s87, s86, 1\n"
"s_lshr_b32 s90, s48, 1\n"
"s_add_u32 s87, s87, s90\n"
"s_lshl_b32 s87, s87, 8\n"
"s_add_u32 s65, s87, 4591616\n"
"s_lshr_b32 s87, s55, 1\n"
"s_add_u32 s87, s87, 5242880\n"
"s_mul_i32 s90, s86, 1179648\n"
"s_add_u32 s68, s90, s87\n"
"s_xor_b32 s90, s86, 1\n"
"s_mul_i32 s90, s90, 1179648\n"
"s_add_u32 s90, s90, s87\n"
"s_mul_i32 s87, s84, 196608\n"
"s_add_u32 s87, s87, 262144\n"
"s_add_u32 s66, s68, s87\n"
"s_add_u32 s67, s90, s87\n"
"s_lshr_b32 s87, s61, 5\n"
"s_and_b32 s90, s61, 1\n"
"s_lshl_b32 s90, s90, 7\n"
"s_lshl_b32 s69, s87, 8\n"
"s_add_u32 s69, s69, s90\n"
"s_xor_b32 s70, s69, 128\n"
"s_lshl_b32 s86, s86, 18\n"
"s_add_u32 s86, s86, s69\n"
"s_lshl_b32 s62, s48, 16\n"
"s_add_u32 s62, s62, s86\n"
"s_lshr_b32 s86, s61, 3\n"
"s_lshl_b32 s86, s86, 2\n"
"s_add_u32 s86, s86, s48\n"
"s_lshl_b32 s86, s86, 14\n"
"s_add_u32 s86, s86, s90\n"
"s_add_u32 s78, s86, 4096\n"
"s_lshl_b32 s85, s85, 3\n"
"s_add_u32 s85, s85, s87\n"
"s_lshl_b32 s85, s85, 2\n"
"s_add_u32 s85, s85, s84\n"
"s_lshl_b32 s86, s85, 3\n"
"s_lshr_b32 s87, s55, 1\n"
"s_add_u32 s86, s86, s87\n"
"s_lshl_b32 s86, s86, 0\n"
"s_add_u32 s75, s86, 4722688\n"
"s_add_u32 s86, s55, 25165824\n"
"s_mul_i32 s87, s85, 49152\n"
"s_add_u32 s74, s87, s86\n"
"s_xor_b32 s87, s85, 1\n"
"s_mul_i32 s87, s87, 49152\n"
"s_add_u32 s71, s87, s86\n"
"s_add_u32 s86, s86, 16384\n"
"s_xor_b32 s87, s85, 2\n"
"s_mul_i32 s87, s87, 49152\n"
"s_add_u32 s72, s87, s86\n"
"s_add_u32 s86, s86, 16384\n"
"s_xor_b32 s87, s85, 3\n"
"s_mul_i32 s87, s87, 49152\n"
"s_add_u32 s73, s87, s86\n"
"s_mul_i32 s77, s84, 1572864\n"
"s_mov_b32 s34, 7344128\n"
"s_sub_u32 s32, s32, 4096\n"
"s_subb_u32 s33, s33, 0\n"
"s_sub_u32 s36, s36, 4096\n"
"s_subb_u32 s37, s37, 0\n"
"s_and_b32 s33, s33, 0xffff\n"
"s_mov_b32 s35, 0x20000\n"
"s_and_b32 s37, s37, 0xffff\n"
"s_mov_b32 s39, 0x20000\n"
"s_and_b32 s45, s45, 0xffff\n"
"s_mov_b32 s47, 0x20000\n"
"s_mov_b32 s38, 2101248\n"
"s_mov_b32 s46, 4194304\n"
"v_add_u32 v6, s55, v18\n"
"v_lshrrev_b32 v24, 8, v18\n"
"v_bfe_u32 v25, v18, 1, 5\n"
"v_lshrrev_b32 v26, 6, v18\n"
"v_mul_u32_u24 v27, 224, v26\n"
"v_add_u32 v3, v27, v25\n"
"v_and_b32 v1, 1, v24\n"
"v_lshl_add_u32 v27, v26, 6, v25\n"
"v_lshl_add_u32 v4, v1, 5, v27\n"
"v_xor_b32 v5, 32, v4\n"
"v_and_b32 v26, 7, v26\n"
"v_and_b32 v27, 6, v26\n"
"v_lshlrev_b32 v27, 4, v27\n"
"v_xor_b32 v27, v27, v25\n"
"v_lshl_add_u32 v27, v26, 7, v27\n"
"v_lshrrev_b32 v9, 1, v24\n"
"v_lshl_add_u32 v27, v9, 12, v27\n"
"v_accvgpr_write_b32 a245, v27\n"
"v_and_b32 v25, 0x70, v18\n"
"v_lshlrev_b32 v27, 5, v24\n"
"v_xor_b32 v27, v27, v25\n"
"v_lshrrev_b32 v26, 7, v18\n"
"v_lshl_add_u32 v27, v26, 11, v27\n"
"v_mov_b32 v26, s55\n"
"v_lshl_add_u32 v27, v26, 2, v27\n"
"v_add_u32 v27, 4096, v27\n"
"v_accvgpr_write_b32 a244, v27\n"
"v_cmp_eq_u32 vcc, 0, v25\n"
"v_mov_b32 v26, 0x3f803f80\n"
"s_nop 0\n"
"v_cndmask_b32 v9, 0, v26, vcc\n"
"v_cmp_eq_u32 vcc, 64, v25\n"
"v_accvgpr_write_b32 a240, v9\n"
"v_accvgpr_write_b32 a241, v9\n"
"v_cndmask_b32 v9, 0, v26, vcc\n"
"v_accvgpr_write_b32 a242, v9\n"
"v_accvgpr_write_b32 a243, v9\n"
"v_and_b32 v25, 0x60, v18\n"
"v_cmp_eq_u32 vcc, 0, v25\n"
"v_bfe_u32 v27, v18, 4, 1\n"
"v_mul_u32_u24 v27, 0xaaaa, v27\n"
"v_cndmask_b32 v9, 0, v26, vcc\n"
"v_cmp_eq_u32 vcc, 64, v25\n"
"v_accvgpr_write_b32 a252, v9\n"
"v_accvgpr_write_b32 a253, v9\n"
"v_cndmask_b32 v9, 0, v26, vcc\n"
"v_add_u32 v27, 0x4444, v27\n"
"v_accvgpr_write_b32 a254, v9\n"
"v_accvgpr_write_b32 a255, v9\n"
"v_accvgpr_write_b32 a250, v27\n"
"v_and_b32 v25, 0xf0, v18\n"
"v_lshrrev_b32 v9, 1, v24\n"
"v_lshl_add_u32 v27, v1, 10, v25\n"
"v_lshl_add_u32 v27, v9, 8, v27\n"
"v_accvgpr_write_b32 a248, v27\n"
"v_lshlrev_b32 v27, 7, v25\n"
"v_add_u32 v27, s62, v27\n"
"v_lshl_add_u32 v26, v24, 4, v27\n"
"v_accvgpr_write_b32 a246, v26\n"
"v_lshl_add_u32 v27, v1, 1, v9\n"
"v_lshlrev_b32 v27, 4, v27\n"
"v_and_b32 v26, 0x70, v18\n"
"v_xor_b32 v27, v27, v26\n"
"v_lshl_add_u32 v27, v25, 3, v27\n"
"v_add_u32 v27, s55, v27\n"
"v_add_u32 v27, 98304, v27\n"
"v_accvgpr_write_b32 a249, v27\n"
"v_lshrrev_b32 v27, 7, v18\n"
"v_lshlrev_b32 v9, 4, v27\n"
"v_xor_b32 v26, v26, v9\n"
"v_lshl_add_u32 v27, v27, 11, v26\n"
"v_add_u32 v27, s62, v27\n"
"v_accvgpr_write_b32 a247, v27\n"
"v_and_b32 v26, 16, v18\n"
"v_mov_b32 v27, 0x3f80\n"
"v_lshlrev_b32 v1, v26, v27\n"
"v_lshrrev_b32 v25, 5, v25\n"
"v_lshlrev_b32 v24, 1, v24\n"
"v_sub_u32 v25, v25, v24\n"
"v_sub_u32 v24, v25, v24\n"
"s_waitcnt vmcnt(4)\n"
"ds_read_b128 v[192:195], v2 offset:0\n"
"ds_read_b128 v[196:199], v0 offset:0\n"
"ds_read_b128 v[200:203], v2 offset:2048\n"
"ds_read_b128 v[204:207], v0 offset:2048\n"
"ds_read_b128 v[208:211], v2 offset:4096\n"
"ds_read_b128 v[212:215], v0 offset:4096\n"
"ds_read_b128 v[216:219], v2 offset:6144\n"
"ds_read_b128 v[220:223], v0 offset:6144\n"
"s_add_u32 m0, s12, 98304\n"
"s_xor_b32 s89, s13, 0\n"
"buffer_load_dwordx4 v30, s[8:11], s89 offen offset:0 lds\n"
"buffer_load_dwordx4 v30, s[8:11], s89 offen offset:1024 lds\n"
"buffer_load_dwordx4 v30, s[8:11], s89 offen offset:2048 lds\n"
"buffer_load_dwordx4 v30, s[8:11], s89 offen offset:3072 lds\n"
"s_waitcnt lgkmcnt(0)\n"
"v_dot2c_f32_bf16 v32, s88, v192\n"
"v_dot2c_f32_bf16 v33, v192, v192\n"
"v_dot2c_f32_bf16 v34, s88, v193\n"
"v_dot2c_f32_bf16 v35, v193, v193\n"
"v_dot2c_f32_bf16 v32, s88, v194\n"
"v_dot2c_f32_bf16 v33, v194, v194\n"
"v_dot2c_f32_bf16 v34, s88, v195\n"
"v_dot2c_f32_bf16 v35, v195, v195\n"
"v_dot2c_f32_bf16 v32, s88, v196\n"
"v_dot2c_f32_bf16 v33, v196, v196\n"
"v_dot2c_f32_bf16 v34, s88, v197\n"
"v_dot2c_f32_bf16 v35, v197, v197\n"
"v_dot2c_f32_bf16 v32, s88, v198\n"
"v_dot2c_f32_bf16 v33, v198, v198\n"
"v_dot2c_f32_bf16 v34, s88, v199\n"
"v_dot2c_f32_bf16 v35, v199, v199\n"
"v_dot2c_f32_bf16 v32, s88, v200\n"
"v_dot2c_f32_bf16 v33, v200, v200\n"
"v_dot2c_f32_bf16 v34, s88, v201\n"
"v_dot2c_f32_bf16 v35, v201, v201\n"
"v_dot2c_f32_bf16 v32, s88, v202\n"
"v_dot2c_f32_bf16 v33, v202, v202\n"
"v_dot2c_f32_bf16 v34, s88, v203\n"
"v_dot2c_f32_bf16 v35, v203, v203\n"
"v_dot2c_f32_bf16 v32, s88, v204\n"
"v_dot2c_f32_bf16 v33, v204, v204\n"
"v_dot2c_f32_bf16 v34, s88, v205\n"
"v_dot2c_f32_bf16 v35, v205, v205\n"
"v_dot2c_f32_bf16 v32, s88, v206\n"
"v_dot2c_f32_bf16 v33, v206, v206\n"
"v_dot2c_f32_bf16 v34, s88, v207\n"
"v_dot2c_f32_bf16 v35, v207, v207\n"
"v_dot2c_f32_bf16 v32, s88, v208\n"
"v_dot2c_f32_bf16 v33, v208, v208\n"
"v_dot2c_f32_bf16 v34, s88, v209\n"
"v_dot2c_f32_bf16 v35, v209, v209\n"
"v_dot2c_f32_bf16 v32, s88, v210\n"
"v_dot2c_f32_bf16 v33, v210, v210\n"
"v_dot2c_f32_bf16 v34, s88, v211\n"
"v_dot2c_f32_bf16 v35, v211, v211\n"
"v_dot2c_f32_bf16 v32, s88, v212\n"
"v_dot2c_f32_bf16 v33, v212, v212\n"
"v_dot2c_f32_bf16 v34, s88, v213\n"
"v_dot2c_f32_bf16 v35, v213, v213\n"
"v_dot2c_f32_bf16 v32, s88, v214\n"
"v_dot2c_f32_bf16 v33, v214, v214\n"
"v_dot2c_f32_bf16 v34, s88, v215\n"
"v_dot2c_f32_bf16 v35, v215, v215\n"
"v_dot2c_f32_bf16 v32, s88, v216\n"
"v_dot2c_f32_bf16 v33, v216, v216\n"
"v_dot2c_f32_bf16 v34, s88, v217\n"
"v_dot2c_f32_bf16 v35, v217, v217\n"
"v_dot2c_f32_bf16 v32, s88, v218\n"
"v_dot2c_f32_bf16 v33, v218, v218\n"
"v_dot2c_f32_bf16 v34, s88, v219\n"
"v_dot2c_f32_bf16 v35, v219, v219\n"
"v_dot2c_f32_bf16 v32, s88, v220\n"
"v_dot2c_f32_bf16 v33, v220, v220\n"
"v_dot2c_f32_bf16 v34, s88, v221\n"
"v_dot2c_f32_bf16 v35, v221, v221\n"
"v_dot2c_f32_bf16 v32, s88, v222\n"
"v_dot2c_f32_bf16 v33, v222, v222\n"
"v_dot2c_f32_bf16 v34, s88, v223\n"
"v_dot2c_f32_bf16 v35, v223, v223\n"
"s_nop 2\n"
"v_pk_add_f32 v[32:33], v[32:33], v[34:35]\n"
"v_mov_b32 v34, v32\n"
"v_mov_b32 v35, v33\n"
"s_nop 1\n"
"v_permlane16_swap_b32 v32, v34\n"
"v_permlane16_swap_b32 v33, v35\n"
"v_pk_add_f32 v[32:33], v[32:33], v[34:35]\n"
"v_mov_b32 v34, v32\n"
"v_mov_b32 v35, v33\n"
"s_nop 1\n"
"v_permlane32_swap_b32 v32, v34\n"
"v_permlane32_swap_b32 v33, v35\n"
"v_pk_add_f32 v[32:33], v[32:33], v[34:35]\n"
"ds_write_b64 v20, v[32:33]\n"
"v_lshlrev_b32 v64, 16, v192\n"
"v_and_b32 v65, s91, v192\n"
"v_lshlrev_b32 v66, 16, v193\n"
"v_and_b32 v67, s91, v193\n"
"v_lshlrev_b32 v68, 16, v194\n"
"v_and_b32 v69, s91, v194\n"
"v_lshlrev_b32 v70, 16, v195\n"
"v_and_b32 v71, s91, v195\n"
"v_lshlrev_b32 v72, 16, v196\n"
"v_and_b32 v73, s91, v196\n"
"v_lshlrev_b32 v74, 16, v197\n"
"v_and_b32 v75, s91, v197\n"
"v_lshlrev_b32 v76, 16, v198\n"
"v_and_b32 v77, s91, v198\n"
"v_lshlrev_b32 v78, 16, v199\n"
"v_and_b32 v79, s91, v199\n"
"s_waitcnt lgkmcnt(0)\n"
"s_barrier\n"
"ds_read_b128 v[36:39], v21\n"
"ds_read_b128 v[40:43], v21 offset:16\n"
"v_lshlrev_b32 v80, 16, v200\n"
"v_and_b32 v81, s91, v200\n"
"v_lshlrev_b32 v82, 16, v201\n"
"v_and_b32 v83, s91, v201\n"
"v_lshlrev_b32 v84, 16, v202\n"
"v_and_b32 v85, s91, v202\n"
"v_lshlrev_b32 v86, 16, v203\n"
"v_and_b32 v87, s91, v203\n"
"v_lshlrev_b32 v88, 16, v204\n"
"v_and_b32 v89, s91, v204\n"
"v_lshlrev_b32 v90, 16, v205\n"
"v_and_b32 v91, s91, v205\n"
"v_lshlrev_b32 v92, 16, v206\n"
"v_and_b32 v93, s91, v206\n"
"v_lshlrev_b32 v94, 16, v207\n"
"v_and_b32 v95, s91, v207\n"
"s_waitcnt lgkmcnt(0)\n"
"v_pk_add_f32 v[36:37], v[36:37], v[38:39]\n"
"v_pk_add_f32 v[40:41], v[40:41], v[42:43]\n"
"v_pk_add_f32 v[36:37], v[36:37], v[40:41]\n"
"v_mul_f32 v36, 0x3a800000, v36\n"
"v_mul_f32 v37, 0x3a800000, v37\n"
"v_fma_f32 v37, -v36, v36, v37\n"
"v_max_f32 v37, 0, v37\n"
"v_cvt_pk_bf16_f32 v37, v36, v37\n"
"v_lshlrev_b32 v36, 16, v37\n"
"v_and_b32 v37, s91, v37\n"
"v_add_f32 v37, 0x3727c5ac, v37\n"
"v_cvt_pk_bf16_f32 v37, 0, v37\n"
"v_sqrt_f32 v37, v37\n"
"s_nop 0\n"
"v_cvt_pk_bf16_f32 v37, 0, v37\n"
"v_rcp_f32 v38, v37\n"
"s_nop 0\n"
"v_mul_f32 v40, v36, v38\n"
"v_xor_b32 v40, 0x80000000, v40\n"
"v_pk_fma_f32 v[64:65], v[64:65], v[38:39], v[40:41] op_sel_hi:[1,0,0]\n"
"v_pk_fma_f32 v[66:67], v[66:67], v[38:39], v[40:41] op_sel_hi:[1,0,0]\n"
"v_pk_fma_f32 v[68:69], v[68:69], v[38:39], v[40:41] op_sel_hi:[1,0,0]\n"
"v_pk_fma_f32 v[70:71], v[70:71], v[38:39], v[40:41] op_sel_hi:[1,0,0]\n"
"v_cvt_pk_bf16_f32 v192, v64, v65\n"
"v_cvt_pk_bf16_f32 v193, v66, v67\n"
"v_cvt_pk_bf16_f32 v194, v68, v69\n"
"v_cvt_pk_bf16_f32 v195, v70, v71\n"
"buffer_store_dwordx4 v[192:195], v18, s[16:19], s50 offen sc1\n"
"s_add_u32 s88, s50, 16384\n"
"v_pk_fma_f32 v[72:73], v[72:73], v[38:39], v[40:41] op_sel_hi:[1,0,0]\n"
"v_pk_fma_f32 v[74:75], v[74:75], v[38:39], v[40:41] op_sel_hi:[1,0,0]\n"
"v_pk_fma_f32 v[76:77], v[76:77], v[38:39], v[40:41] op_sel_hi:[1,0,0]\n"
"v_pk_fma_f32 v[78:79], v[78:79], v[38:39], v[40:41] op_sel_hi:[1,0,0]\n"
"v_cvt_pk_bf16_f32 v196, v72, v73\n"
"v_cvt_pk_bf16_f32 v197, v74, v75\n"
"v_cvt_pk_bf16_f32 v198, v76, v77\n"
"v_cvt_pk_bf16_f32 v199, v78, v79\n"
"buffer_store_dwordx4 v[196:199], v18, s[16:19], s88 offen sc1\n"
"s_add_u32 s88, s50, 32768\n"
"v_pk_fma_f32 v[80:81], v[80:81], v[38:39], v[40:41] op_sel_hi:[1,0,0]\n"
"v_pk_fma_f32 v[82:83], v[82:83], v[38:39], v[40:41] op_sel_hi:[1,0,0]\n"
"v_pk_fma_f32 v[84:85], v[84:85], v[38:39], v[40:41] op_sel_hi:[1,0,0]\n"
"v_pk_fma_f32 v[86:87], v[86:87], v[38:39], v[40:41] op_sel_hi:[1,0,0]\n"
"v_cvt_pk_bf16_f32 v200, v80, v81\n"
"v_cvt_pk_bf16_f32 v201, v82, v83\n"
"v_cvt_pk_bf16_f32 v202, v84, v85\n"
"v_cvt_pk_bf16_f32 v203, v86, v87\n"
"buffer_store_dwordx4 v[200:203], v18, s[16:19], s88 offen sc1\n"
"s_add_u32 s88, s50, 49152\n"
"v_pk_fma_f32 v[88:89], v[88:89], v[38:39], v[40:41] op_sel_hi:[1,0,0]\n"
"v_pk_fma_f32 v[90:91], v[90:91], v[38:39], v[40:41] op_sel_hi:[1,0,0]\n"
"v_pk_fma_f32 v[92:93], v[92:93], v[38:39], v[40:41] op_sel_hi:[1,0,0]\n"
"v_pk_fma_f32 v[94:95], v[94:95], v[38:39], v[40:41] op_sel_hi:[1,0,0]\n"
"v_cvt_pk_bf16_f32 v204, v88, v89\n"
"v_cvt_pk_bf16_f32 v205, v90, v91\n"
"v_cvt_pk_bf16_f32 v206, v92, v93\n"
"v_cvt_pk_bf16_f32 v207, v94, v95\n"
"buffer_store_dwordx4 v[204:207], v18, s[16:19], s88 offen sc1\n"
"s_waitcnt vmcnt(11)\n"
"v_readfirstlane_b32 s80, v228\n"
"v_readfirstlane_b32 s81, v229\n"
"s_mul_i32 s88, s81, 0x7f4a7c15\n"
"s_mul_i32 s89, s80, 0x9e3779b9\n"
"s_mul_hi_u32 s90, s80, 0x7f4a7c15\n"
"s_mul_i32 s80, s80, 0x7f4a7c15\n"
"s_add_u32 s88, s88, s89\n"
"s_add_u32 s88, s88, s90\n"
"s_add_u32 s80, s80, 0xd9b4e019\n"
"s_addc_u32 s81, s88, 0x632be59b\n"
"s_add_u32 m0, s12, 102400\n"
"s_xor_b32 s89, s13, 4096\n"
"buffer_load_dwordx4 v30, s[8:11], s89 offen offset:0 lds\n"
"v_lshlrev_b32 v96, 16, v208\n"
"v_and_b32 v97, s91, v208\n"
"v_lshlrev_b32 v98, 16, v209\n"
"v_and_b32 v99, s91, v209\n"
"v_lshlrev_b32 v100, 16, v210\n"
"v_and_b32 v101, s91, v210\n"
"v_lshlrev_b32 v102, 16, v211\n"
"v_and_b32 v103, s91, v211\n"
"buffer_load_dwordx4 v30, s[8:11], s89 offen offset:1024 lds\n"
"v_pk_fma_f32 v[96:97], v[96:97], v[38:39], v[40:41] op_sel_hi:[1,0,0]\n"
"v_pk_fma_f32 v[98:99], v[98:99], v[38:39], v[40:41] op_sel_hi:[1,0,0]\n"
"v_pk_fma_f32 v[100:101], v[100:101], v[38:39], v[40:41] op_sel_hi:[1,0,0]\n"
"v_pk_fma_f32 v[102:103], v[102:103], v[38:39], v[40:41] op_sel_hi:[1,0,0]\n"
"v_cvt_pk_bf16_f32 v208, v96, v97\n"
"v_cvt_pk_bf16_f32 v209, v98, v99\n"
"v_cvt_pk_bf16_f32 v210, v100, v101\n"
"v_cvt_pk_bf16_f32 v211, v102, v103\n"
"buffer_load_dwordx4 v30, s[8:11], s89 offen offset:2048 lds\n"
"v_lshlrev_b32 v104, 16, v212\n"
"v_and_b32 v105, s91, v212\n"
"v_lshlrev_b32 v106, 16, v213\n"
"v_and_b32 v107, s91, v213\n"
"v_lshlrev_b32 v108, 16, v214\n"
"v_and_b32 v109, s91, v214\n"
"v_lshlrev_b32 v110, 16, v215\n"
"v_and_b32 v111, s91, v215\n"
"buffer_load_dwordx4 v30, s[8:11], s89 offen offset:3072 lds\n"
"v_pk_fma_f32 v[104:105], v[104:105], v[38:39], v[40:41] op_sel_hi:[1,0,0]\n"
"v_pk_fma_f32 v[106:107], v[106:107], v[38:39], v[40:41] op_sel_hi:[1,0,0]\n"
"v_pk_fma_f32 v[108:109], v[108:109], v[38:39], v[40:41] op_sel_hi:[1,0,0]\n"
"v_pk_fma_f32 v[110:111], v[110:111], v[38:39], v[40:41] op_sel_hi:[1,0,0]\n"
"v_cvt_pk_bf16_f32 v212, v104, v105\n"
"v_cvt_pk_bf16_f32 v213, v106, v107\n"
"v_cvt_pk_bf16_f32 v214, v108, v109\n"
"v_cvt_pk_bf16_f32 v215, v110, v111\n"
"s_add_u32 m0, s12, 106496\n"
"s_xor_b32 s89, s13, 8192\n"
"buffer_load_dwordx4 v30, s[8:11], s89 offen offset:0 lds\n"
"v_lshlrev_b32 v112, 16, v216\n"
"v_and_b32 v113, s91, v216\n"
"v_lshlrev_b32 v114, 16, v217\n"
"v_and_b32 v115, s91, v217\n"
"v_lshlrev_b32 v116, 16, v218\n"
"v_and_b32 v117, s91, v218\n"
"v_lshlrev_b32 v118, 16, v219\n"
"v_and_b32 v119, s91, v219\n"
"buffer_load_dwordx4 v30, s[8:11], s89 offen offset:1024 lds\n"
"v_pk_fma_f32 v[112:113], v[112:113], v[38:39], v[40:41] op_sel_hi:[1,0,0]\n"
"v_pk_fma_f32 v[114:115], v[114:115], v[38:39], v[40:41] op_sel_hi:[1,0,0]\n"
"v_pk_fma_f32 v[116:117], v[116:117], v[38:39], v[40:41] op_sel_hi:[1,0,0]\n"
"v_pk_fma_f32 v[118:119], v[118:119], v[38:39], v[40:41] op_sel_hi:[1,0,0]\n"
"v_cvt_pk_bf16_f32 v216, v112, v113\n"
"v_cvt_pk_bf16_f32 v217, v114, v115\n"
"v_cvt_pk_bf16_f32 v218, v116, v117\n"
"v_cvt_pk_bf16_f32 v219, v118, v119\n"
"buffer_load_dwordx4 v30, s[8:11], s89 offen offset:2048 lds\n"
"v_lshlrev_b32 v120, 16, v220\n"
"v_and_b32 v121, s91, v220\n"
"v_lshlrev_b32 v122, 16, v221\n"
"v_and_b32 v123, s91, v221\n"
"v_lshlrev_b32 v124, 16, v222\n"
"v_and_b32 v125, s91, v222\n"
"v_lshlrev_b32 v126, 16, v223\n"
"v_and_b32 v127, s91, v223\n"
"buffer_load_dwordx4 v30, s[8:11], s89 offen offset:3072 lds\n"
"v_pk_fma_f32 v[120:121], v[120:121], v[38:39], v[40:41] op_sel_hi:[1,0,0]\n"
"v_pk_fma_f32 v[122:123], v[122:123], v[38:39], v[40:41] op_sel_hi:[1,0,0]\n"
"v_pk_fma_f32 v[124:125], v[124:125], v[38:39], v[40:41] op_sel_hi:[1,0,0]\n"
"v_pk_fma_f32 v[126:127], v[126:127], v[38:39], v[40:41] op_sel_hi:[1,0,0]\n"
"v_cvt_pk_bf16_f32 v220, v120, v121\n"
"v_cvt_pk_bf16_f32 v221, v122, v123\n"
"v_cvt_pk_bf16_f32 v222, v124, v125\n"
"v_cvt_pk_bf16_f32 v223, v126, v127\n"
"v_mfma_f32_32x32x16_bf16 v[32:47], v[14:17], v[14:17], 0\n"
"v_accvgpr_write_b32 a226, 0\n"
"v_accvgpr_write_b32 a227, 0\n"
"v_accvgpr_write_b32 a228, 0\n"
"v_accvgpr_write_b32 a229, 0\n"
"v_cmp_eq_u32 vcc, 0, v25\n"
"s_nop 1\n"
"v_mfma_f32_32x32x16_bf16 v[48:63], v[14:17], v[14:17], 0\n"
"v_cndmask_b32 v26, 0, v1, vcc\n"
"v_cmp_eq_u32 vcc, 1, v25\n"
"v_accvgpr_write_b32 a224, v26\n"
"v_accvgpr_write_b32 a230, v26\n"
"s_nop 0\n"
"v_cndmask_b32 v26, 0, v1, vcc\n"
"v_mfma_f32_32x32x16_bf16 v[64:79], v[14:17], v[14:17], 0\n"
"v_cmp_eq_u32 vcc, 0, v24\n"
"v_accvgpr_write_b32 a225, v26\n"
"v_accvgpr_write_b32 a231, v26\n"
"s_nop 0\n"
"v_cndmask_b32 v26, 0, v1, vcc\n"
"v_cmp_eq_u32 vcc, 1, v24\n"
"v_mfma_f32_32x32x16_bf16 v[80:95], v[14:17], v[14:17], 0\n"
"v_accvgpr_write_b32 a232, v26\n"
"s_nop 0\n"
"v_cndmask_b32 v26, 0, v1, vcc\n"
"v_cmp_eq_u32 vcc, 2, v24\n"
"v_accvgpr_write_b32 a233, v26\n"
"s_nop 0\n"
"v_mfma_f32_32x32x16_bf16 v[96:111], v[14:17], v[14:17], 0\n"
"v_cndmask_b32 v26, 0, v1, vcc\n"
"v_cmp_eq_u32 vcc, 3, v24\n"
"v_accvgpr_write_b32 a234, v26\n"
"s_nop 0\n"
"v_cndmask_b32 v26, 0, v1, vcc\n"
"v_cmp_eq_u32 vcc, -8, v24\n"
"v_mfma_f32_32x32x16_bf16 v[112:127], v[14:17], v[14:17], 0\n"
"v_accvgpr_write_b32 a235, v26\n"
"s_nop 0\n"
"v_cndmask_b32 v26, 0, v1, vcc\n"
"v_cmp_eq_u32 vcc, -7, v24\n"
"v_accvgpr_write_b32 a236, v26\n"
"s_nop 0\n"
"s_waitcnt vmcnt(8)\n"
"v_mov_b32 v28, s80\n"
"v_mov_b32 v29, s81\n"
"s_mov_b64 exec, 1\n"
"buffer_store_dwordx2 v[28:29], off, s[16:19], s51 sc1\n"
"s_mov_b64 exec, -1\n"
"s_add_u32 m0, s12, 110592\n"
"s_xor_b32 s89, s13, 12288\n"
"buffer_load_dwordx4 v30, s[8:11], s89 offen offset:0 lds\n"
"v_cndmask_b32 v26, 0, v1, vcc\n"
"v_cmp_eq_u32 vcc, -6, v24\n"
"v_accvgpr_write_b32 a237, v26\n"
"s_nop 0\n"
"v_cndmask_b32 v26, 0, v1, vcc\n"
"v_cmp_eq_u32 vcc, -5, v24\n"
"v_accvgpr_write_b32 a238, v26\n"
"s_nop 0\n"
"buffer_load_dwordx4 v30, s[8:11], s89 offen offset:1024 lds\n"
"v_cndmask_b32 v26, 0, v1, vcc\n"
"v_accvgpr_write_b32 a239, v26\n"
"buffer_load_dwordx4 v30, s[8:11], s89 offen offset:2048 lds\n"
"buffer_load_dwordx4 v30, s[8:11], s89 offen offset:3072 lds\n"
"s_add_u32 m0, s55, 0\n"
"s_nop 0\n"
"buffer_load_dwordx4 v10, s[20:23], s82 offen offset:0 lds\n"
"buffer_load_dwordx4 v11, s[20:23], s82 offen offset:1024 lds\n"
"buffer_load_dwordx4 v12, s[20:23], s82 offen offset:2048 lds\n"
"buffer_load_dwordx4 v13, s[24:27], s83 offen offset:3072 lds\n"
"s_add_u32 s82, s82, s53\n"
"s_add_u32 s83, s83, s54\n"
"s_add_u32 m0, s55, 16384\n"
"s_nop 0\n"
"buffer_load_dwordx4 v10, s[20:23], s82 offen offset:0 lds\n"
"buffer_load_dwordx4 v11, s[20:23], s82 offen offset:1024 lds\n"
"buffer_load_dwordx4 v12, s[20:23], s82 offen offset:2048 lds\n"
"buffer_load_dwordx4 v13, s[24:27], s83 offen offset:3072 lds\n"
"s_add_u32 s82, s82, s53\n"
"s_add_u32 s83, s83, s54\n"
"s_waitcnt vmcnt(13)\n"
"v_xor_b32 v1, 32, v19\n"
"ds_read_b64_tr_b16 a[0:1], v19 offset:0\n"
"ds_read_b64_tr_b16 a[2:3], v19 offset:256\n"
"ds_read_b64_tr_b16 a[4:5], v1 offset:0\n"
"ds_read_b64_tr_b16 a[6:7], v1 offset:256\n"
"ds_read_b64_tr_b16 a[8:9], v19 offset:2048\n"
"ds_read_b64_tr_b16 a[10:11], v19 offset:2304\n"
"ds_read_b64_tr_b16 a[12:13], v1 offset:2048\n"
"ds_read_b64_tr_b16 a[14:15], v1 offset:2304\n"
"ds_read_b64_tr_b16 a[16:17], v19 offset:4096\n"
"ds_read_b64_tr_b16 a[18:19], v19 offset:4352\n"
"ds_read_b64_tr_b16 a[20:21], v1 offset:4096\n"
"ds_read_b64_tr_b16 a[22:23], v1 offset:4352\n"
"s_cmp_eq_u32 s49, 0\n"
"s_waitcnt lgkmcnt(8)\n"
"s_cbranch_scc1 Lk0_%=\n"
"v_mfma_f32_16x16x32_bf16 a[64:67], v[192:195], a[0:3], 0\n"
"ds_read_b64_tr_b16 a[24:25], v19 offset:6144\n"
"ds_read_b64_tr_b16 a[26:27], v19 offset:6400\n"
"v_mfma_f32_16x16x32_bf16 a[68:71], v[192:195], a[4:7], 0\n"
"ds_read_b64_tr_b16 a[28:29], v1 offset:6144\n"
"ds_read_b64_tr_b16 a[30:31], v1 offset:6400\n"
"s_waitcnt lgkmcnt(8)\n"
"v_mfma_f32_16x16x32_bf16 a[64:67], v[196:199], a[8:11], a[64:67]\n"
"ds_read_b64_tr_b16 a[32:33], v19 offset:8192\n"
"ds_read_b64_tr_b16 a[34:35], v19 offset:8448\n"
"v_mfma_f32_16x16x32_bf16 a[68:71], v[196:199], a[12:15], a[68:71]\n"
"ds_read_b64_tr_b16 a[36:37], v1 offset:8192\n"
"ds_read_b64_tr_b16 a[38:39], v1 offset:8448\n"
"s_waitcnt lgkmcnt(8)\n"
"v_mfma_f32_16x16x32_bf16 a[64:67], v[200:203], a[16:19], a[64:67]\n"
"ds_read_b64_tr_b16 a[40:41], v19 offset:10240\n"
"ds_read_b64_tr_b16 a[42:43], v19 offset:10496\n"
"v_mfma_f32_16x16x32_bf16 a[68:71], v[200:203], a[20:23], a[68:71]\n"
"ds_read_b64_tr_b16 a[44:45], v1 offset:10240\n"
"ds_read_b64_tr_b16 a[46:47], v1 offset:10496\n"
"s_waitcnt lgkmcnt(8)\n"
"v_mfma_f32_16x16x32_bf16 a[64:67], v[204:207], a[24:27], a[64:67]\n"
"s_waitcnt vmcnt(8)\n"
"ds_read_b64_tr_b16 a[48:49], v19 offset:12288\n"
"ds_read_b64_tr_b16 a[50:51], v19 offset:12544\n"
"v_mfma_f32_16x16x32_bf16 a[68:71], v[204:207], a[28:31], a[68:71]\n"
"ds_read_b64_tr_b16 a[52:53], v1 offset:12288\n"
"ds_read_b64_tr_b16 a[54:55], v1 offset:12544\n"
"s_waitcnt lgkmcnt(8)\n"
"v_mfma_f32_16x16x32_bf16 a[64:67], v[208:211], a[32:35], a[64:67]\n"
"ds_read_b64_tr_b16 a[56:57], v19 offset:14336\n"
"ds_read_b64_tr_b16 a[58:59], v19 offset:14592\n"
"v_mfma_f32_16x16x32_bf16 a[68:71], v[208:211], a[36:39], a[68:71]\n"
"ds_read_b64_tr_b16 a[60:61], v1 offset:14336\n"
"ds_read_b64_tr_b16 a[62:63], v1 offset:14592\n"
"s_waitcnt lgkmcnt(8)\n"
"v_mfma_f32_16x16x32_bf16 a[64:67], v[212:215], a[40:43], a[64:67]\n"
"v_mfma_f32_16x16x32_bf16 a[68:71], v[212:215], a[44:47], a[68:71]\n"
"s_waitcnt lgkmcnt(4)\n"
"v_mfma_f32_16x16x32_bf16 a[64:67], v[216:219], a[48:51], a[64:67]\n"
"v_mfma_f32_16x16x32_bf16 a[68:71], v[216:219], a[52:55], a[68:71]\n"
"s_waitcnt lgkmcnt(0)\n"
"v_mfma_f32_16x16x32_bf16 a[64:67], v[220:223], a[56:59], a[64:67]\n"
"v_mfma_f32_16x16x32_bf16 a[68:71], v[220:223], a[60:63], a[68:71]\n"
"s_branch Lk1_%=\n"
"Lk0_%=:\n"
"v_mfma_f32_16x16x32_bf16 a[64:67], a[0:3], v[192:195], 0\n"
"ds_read_b64_tr_b16 a[24:25], v19 offset:6144\n"
"ds_read_b64_tr_b16 a[26:27], v19 offset:6400\n"
"v_mfma_f32_16x16x32_bf16 a[68:71], a[4:7], v[192:195], 0\n"
"ds_read_b64_tr_b16 a[28:29], v1 offset:6144\n"
"ds_read_b64_tr_b16 a[30:31], v1 offset:6400\n"
"s_waitcnt lgkmcnt(8)\n"
"v_mfma_f32_16x16x32_bf16 a[64:67], a[8:11], v[196:199], a[64:67]\n"
"ds_read_b64_tr_b16 a[32:33], v19 offset:8192\n"
"ds_read_b64_tr_b16 a[34:35], v19 offset:8448\n"
"v_mfma_f32_16x16x32_bf16 a[68:71], a[12:15], v[196:199], a[68:71]\n"
"ds_read_b64_tr_b16 a[36:37], v1 offset:8192\n"
"ds_read_b64_tr_b16 a[38:39], v1 offset:8448\n"
"s_waitcnt lgkmcnt(8)\n"
"v_mfma_f32_16x16x32_bf16 a[64:67], a[16:19], v[200:203], a[64:67]\n"
"ds_read_b64_tr_b16 a[40:41], v19 offset:10240\n"
"ds_read_b64_tr_b16 a[42:43], v19 offset:10496\n"
"v_mfma_f32_16x16x32_bf16 a[68:71], a[20:23], v[200:203], a[68:71]\n"
"ds_read_b64_tr_b16 a[44:45], v1 offset:10240\n"
"ds_read_b64_tr_b16 a[46:47], v1 offset:10496\n"
"s_waitcnt lgkmcnt(8)\n"
"v_mfma_f32_16x16x32_bf16 a[64:67], a[24:27], v[204:207], a[64:67]\n"
"s_waitcnt vmcnt(8)\n"
"ds_read_b64_tr_b16 a[48:49], v19 offset:12288\n"
"ds_read_b64_tr_b16 a[50:51], v19 offset:12544\n"
"v_mfma_f32_16x16x32_bf16 a[68:71], a[28:31], v[204:207], a[68:71]\n"
"ds_read_b64_tr_b16 a[52:53], v1 offset:12288\n"
"ds_read_b64_tr_b16 a[54:55], v1 offset:12544\n"
"s_waitcnt lgkmcnt(8)\n"
"v_mfma_f32_16x16x32_bf16 a[64:67], a[32:35], v[208:211], a[64:67]\n"
"ds_read_b64_tr_b16 a[56:57], v19 offset:14336\n"
"ds_read_b64_tr_b16 a[58:59], v19 offset:14592\n"
"v_mfma_f32_16x16x32_bf16 a[68:71], a[36:39], v[208:211], a[68:71]\n"
"ds_read_b64_tr_b16 a[60:61], v1 offset:14336\n"
"ds_read_b64_tr_b16 a[62:63], v1 offset:14592\n"
"s_waitcnt lgkmcnt(8)\n"
"v_mfma_f32_16x16x32_bf16 a[64:67], a[40:43], v[212:215], a[64:67]\n"
"v_mfma_f32_16x16x32_bf16 a[68:71], a[44:47], v[212:215], a[68:71]\n"
"s_waitcnt lgkmcnt(4)\n"
"v_mfma_f32_16x16x32_bf16 a[64:67], a[48:51], v[216:219], a[64:67]\n"
"v_mfma_f32_16x16x32_bf16 a[68:71], a[52:55], v[216:219], a[68:71]\n"
"s_waitcnt lgkmcnt(0)\n"
"v_mfma_f32_16x16x32_bf16 a[64:67], a[56:59], v[220:223], a[64:67]\n"
"v_mfma_f32_16x16x32_bf16 a[68:71], a[60:63], v[220:223], a[68:71]\n"
"Lk1_%=:\n"
"s_lshr_b32 s88, s55, 1\n"
"s_add_u32 s88, s88, 81920\n"
"v_add_u32 v19, s88, v18\n"
"s_mov_b32 s84, 5\n"
"s_mov_b32 s86, 65536\n"
"s_lshl_b32 s88, s54, 2\n"
"v_add_u32 v8, s88, v8\n"
"s_cmp_eq_u32 s48, 0\n"
"s_cselect_b32 s52, s52, 0x7f000000\n"
"s_cselect_b32 s57, s57, 0x7f000000\n"
"ds_write_b128 v19, a[64:67]\n"
"ds_write_b128 v19, a[68:71] offset:1024\n"
"v_lshlrev_b32 v1, 1, v18\n"
"s_mov_b32 s91, 0\n"
"s_waitcnt vmcnt(0) lgkmcnt(0)\n"
"s_barrier\n"
"buffer_load_dwordx4 v[228:231], v1, s[16:19], s56 offen sc1\n"
"ds_read_b128 v[192:195], v19 offset:0\n"
"ds_read_b128 v[196:199], v19 offset:1024\n"
"ds_read_b128 v[200:203], v19 offset:2048\n"
"ds_read_b128 v[204:207], v19 offset:3072\n"
"ds_read_b128 v[208:211], v19 offset:4096\n"
"ds_read_b128 v[212:215], v19 offset:5120\n"
"ds_read_b128 v[216:219], v19 offset:6144\n"
"ds_read_b128 v[220:223], v19 offset:7168\n"
"ds_read_b64_tr_b16 a[56:57], v4 offset:7168\n"
"ds_read_b64_tr_b16 a[58:59], v4 offset:15360\n"
"ds_read_b64_tr_b16 a[60:61], v5 offset:7168\n"
"ds_read_b64_tr_b16 a[62:63], v5 offset:15360\n"
"ds_read_b64_tr_b16 a[0:1], v3 offset:0\n"
"ds_read_b64_tr_b16 a[2:3], v3 offset:3584\n"
"ds_read_b64_tr_b16 a[28:29], v3 offset:8192\n"
"ds_read_b64_tr_b16 a[30:31], v3 offset:11776\n"
"ds_read_b64_tr_b16 a[4:5], v3 offset:32\n"
"ds_read_b64_tr_b16 a[6:7], v3 offset:3616\n"
"ds_read_b64_tr_b16 a[32:33], v3 offset:8224\n"
"ds_read_b64_tr_b16 a[34:35], v3 offset:11808\n"
"ds_read_b64_tr_b16 a[8:9], v3 offset:64\n"
"ds_read_b64_tr_b16 a[10:11], v3 offset:3648\n"
"ds_read_b64_tr_b16 a[36:37], v3 offset:8256\n"
"ds_read_b64_tr_b16 a[38:39], v3 offset:11840\n"
"ds_read_b64_tr_b16 a[12:13], v3 offset:96\n"
"ds_read_b64_tr_b16 a[14:15], v3 offset:3680\n"
"ds_read_b64_tr_b16 a[40:41], v3 offset:8288\n"
"ds_read_b64_tr_b16 a[42:43], v3 offset:11872\n"
"ds_read_b64_tr_b16 a[16:17], v3 offset:128\n"
"ds_read_b64_tr_b16 a[18:19], v3 offset:3712\n"
"ds_read_b64_tr_b16 a[44:45], v3 offset:8320\n"
"ds_read_b64_tr_b16 a[46:47], v3 offset:11904\n"
"ds_read_b64_tr_b16 a[20:21], v3 offset:160\n"
"ds_read_b64_tr_b16 a[22:23], v3 offset:3744\n"
"ds_read_b64_tr_b16 a[48:49], v3 offset:8352\n"
"ds_read_b64_tr_b16 a[50:51], v3 offset:11936\n"
"ds_read_b64_tr_b16 a[24:25], v3 offset:192\n"
"ds_read_b64_tr_b16 a[26:27], v3 offset:3776\n"
"ds_read_b64_tr_b16 a[52:53], v3 offset:8384\n"
"ds_read_b64_tr_b16 a[54:55], v3 offset:11968\n"
"s_waitcnt vmcnt(0)\n"
"s_mov_b32 s94, 200000\n"
"Lf1c_%=:\n"
"v_cmp_ne_u64 vcc, s[80:81], v[228:229]\n"
"v_cmp_ne_u64 s[92:93], s[80:81], v[230:231]\n"
"s_or_b64 vcc, vcc, s[92:93]\n"
"s_cbranch_vccz Lf1ok_%=\n"
"buffer_load_dwordx4 v[228:231], v1, s[16:19], s56 offen sc1\n"
"s_waitcnt vmcnt(0)\n"
"s_sub_u32 s94, s94, 1\n"
"s_cbranch_scc0 Lf1c_%=\n"
"Lf1ok_%=:\n"
"buffer_load_dwordx4 a[128:131], v6, s[28:31], s91 offen offset:0 sc1\n"
"buffer_load_dwordx4 a[132:135], v6, s[28:31], s91 offen offset:1024 sc1\n"
"buffer_load_dwordx4 a[136:139], v6, s[28:31], s91 offen offset:2048 sc1\n"
"buffer_load_dwordx4 a[140:143], v6, s[28:31], s91 offen offset:3072 sc1\n"
"s_add_u32 s91, s91, 16384\n"
"buffer_load_dwordx4 a[144:147], v6, s[28:31], s91 offen offset:0 sc1\n"
"buffer_load_dwordx4 a[148:151], v6, s[28:31], s91 offen offset:1024 sc1\n"
"buffer_load_dwordx4 a[152:155], v6, s[28:31], s91 offen offset:2048 sc1\n"
"buffer_load_dwordx4 a[156:159], v6, s[28:31], s91 offen offset:3072 sc1\n"
"s_add_u32 s91, s91, 16384\n"
"s_lshl_b32 s88, s54, 2\n"
"buffer_load_dword v2, v8, s[24:27], s88 offen\n"
"s_waitcnt lgkmcnt(0)\n"
"s_cmp_lg_u32 s48, 0\n"
"s_cbranch_scc1 Lfz_%=\n"
"v_pk_add_f32 v[192:193], v[192:193], v[200:201]\n"
"v_pk_add_f32 v[208:209], v[208:209], v[216:217]\n"
"v_pk_add_f32 v[194:195], v[194:195], v[202:203]\n"
"v_pk_add_f32 v[210:211], v[210:211], v[218:219]\n"
"v_pk_add_f32 v[196:197], v[196:197], v[204:205]\n"
"v_pk_add_f32 v[212:213], v[212:213], v[220:221]\n"
"v_pk_add_f32 v[198:199], v[198:199], v[206:207]\n"
"v_pk_add_f32 v[214:215], v[214:215], v[222:223]\n"
"v_pk_add_f32 v[192:193], v[192:193], v[208:209]\n"
"v_pk_add_f32 v[194:195], v[194:195], v[210:211]\n"
"v_pk_add_f32 v[196:197], v[196:197], v[212:213]\n"
"v_pk_add_f32 v[198:199], v[198:199], v[214:215]\n"
"s_cmp_eq_u32 s49, 0\n"
"s_cselect_b32 s88, 0x3e8293ee, 1.0\n"
"s_mov_b32 s89, s88\n"
"v_pk_mul_f32 v[192:193], v[192:193], s[88:89]\n"
"v_pk_mul_f32 v[194:195], v[194:195], s[88:89]\n"
"v_pk_mul_f32 v[196:197], v[196:197], s[88:89]\n"
"v_pk_mul_f32 v[198:199], v[198:199], s[88:89]\n"
"v_cvt_pk_bf16_f32 v24, v192, v193\n"
"v_cvt_pk_bf16_f32 v25, v194, v195\n"
"v_cvt_pk_bf16_f32 v26, v196, v197\n"
"v_cvt_pk_bf16_f32 v27, v198, v199\n"
"v_mov_b32 v28, 0\n"
"v_dot2_f32_bf16 v28, v24, v24, v28\n"
"v_dot2_f32_bf16 v28, v25, v25, v28\n"
"v_dot2_f32_bf16 v28, v26, v26, v28\n"
"v_dot2_f32_bf16 v28, v27, v27, v28\n"
"s_nop 2\n"
"v_mov_b32 v29, v28\n"
"s_nop 1\n"
"v_permlane16_swap_b32 v28, v29\n"
"v_add_f32 v28, v28, v29\n"
"v_mov_b32 v29, v28\n"
"s_nop 1\n"
"v_permlane32_swap_b32 v28, v29\n"
"v_add_f32 v28, v28, v29\n"
"v_and_b32 v28, 0x7fffffff, v28\n"
"s_nop 1\n"
"v_max_u32_dpp v28, v28, v28 quad_perm:[1,0,3,2] row_mask:0xf bank_mask:0xf\n"
"s_nop 1\n"
"v_max_u32_dpp v28, v28, v28 quad_perm:[2,3,0,1] row_mask:0xf bank_mask:0xf\n"
"s_nop 1\n"
"v_max_u32_dpp v28, v28, v28 row_half_mirror row_mask:0xf bank_mask:0xf\n"
"s_nop 1\n"
"v_max_u32_dpp v28, v28, v28 row_mirror row_mask:0xf bank_mask:0xf\n"
"Lfz_%=:\n"
"v_mfma_f32_32x32x16_bf16 v[192:207], v[14:17], v[14:17], 0\n"
"v_mfma_f32_32x32x16_bf16 v[208:223], v[14:17], v[14:17], 0\n"
"v_mfma_f32_32x32x16_bf16 v[224:239], v[14:17], v[14:17], 0\n"
"v_mfma_f32_32x32x16_bf16 v[240:255], v[14:17], v[14:17], 0\n"
"s_waitcnt vmcnt(5)\n"
"v_mfma_f32_16x16x32_bf16 a[192:195], a[56:59], a[128:131], a[192:195]\n"
"ds_read_b64_tr_b16 a[120:121], v4 offset:23552\n"
"v_mfma_f32_16x16x32_bf16 a[208:211], a[60:63], a[128:131], a[208:211]\n"
"s_add_u32 m0, s55, 32768\n"
"ds_read_b64_tr_b16 a[122:123], v4 offset:31744\n"
"v_mfma_f32_16x16x32_bf16 v[32:35], a[0:3], a[128:131], v[32:35]\n"
"ds_read_b64_tr_b16 a[124:125], v5 offset:23552\n"
"v_mfma_f32_16x16x32_bf16 v[144:147], a[28:31], a[128:131], v[144:147]\n"
"buffer_load_dwordx4 v10, s[20:23], s82 offen offset:0 lds\n"
"v_mfma_f32_16x16x32_bf16 v[48:51], a[4:7], a[128:131], v[48:51]\n"
"ds_read_b64_tr_b16 a[126:127], v5 offset:31744\n"
"v_mfma_f32_16x16x32_bf16 v[160:163], a[32:35], a[128:131], v[160:163]\n"
"ds_read_b64_tr_b16 a[64:65], v3 offset:16384\n"
"v_mfma_f32_16x16x32_bf16 v[64:67], a[8:11], a[128:131], v[64:67]\n"
"ds_read_b64_tr_b16 a[66:67], v3 offset:19968\n"
"v_mfma_f32_16x16x32_bf16 v[176:179], a[36:39], a[128:131], v[176:179]\n"
"buffer_load_dwordx4 v11, s[20:23], s82 offen offset:1024 lds\n"
"v_mfma_f32_16x16x32_bf16 v[80:83], a[12:15], a[128:131], v[80:83]\n"
"ds_read_b64_tr_b16 a[92:93], v3 offset:24576\n"
"v_mfma_f32_16x16x32_bf16 v[192:195], a[40:43], a[128:131], v[192:195]\n"
"ds_read_b64_tr_b16 a[94:95], v3 offset:28160\n"
"v_mfma_f32_16x16x32_bf16 v[96:99], a[16:19], a[128:131], v[96:99]\n"
"ds_read_b64_tr_b16 a[68:69], v3 offset:16416\n"
"v_mfma_f32_16x16x32_bf16 v[208:211], a[44:47], a[128:131], v[208:211]\n"
"v_mfma_f32_16x16x32_bf16 v[112:115], a[20:23], a[128:131], v[112:115]\n"
"ds_read_b64_tr_b16 a[70:71], v3 offset:20000\n"
"v_mfma_f32_16x16x32_bf16 v[224:227], a[48:51], a[128:131], v[224:227]\n"
"ds_read_b64_tr_b16 a[96:97], v3 offset:24608\n"
"v_mfma_f32_16x16x32_bf16 v[128:131], a[24:27], a[128:131], v[128:131]\n"
"ds_read_b64_tr_b16 a[98:99], v3 offset:28192\n"
"v_mfma_f32_16x16x32_bf16 v[240:243], a[52:55], a[128:131], v[240:243]\n"
"buffer_load_dwordx4 v12, s[20:23], s82 offen offset:2048 lds\n"
"v_mfma_f32_16x16x32_bf16 v[244:247], a[52:55], a[132:135], v[244:247]\n"
"ds_read_b64_tr_b16 a[72:73], v3 offset:16448\n"
"v_mfma_f32_16x16x32_bf16 v[132:135], a[24:27], a[132:135], v[132:135]\n"
"ds_read_b64_tr_b16 a[74:75], v3 offset:20032\n"
"v_mfma_f32_16x16x32_bf16 v[228:231], a[48:51], a[132:135], v[228:231]\n"
"ds_read_b64_tr_b16 a[100:101], v3 offset:24640\n"
"v_mfma_f32_16x16x32_bf16 v[116:119], a[20:23], a[132:135], v[116:119]\n"
"buffer_load_dwordx4 v13, s[24:27], s83 offen offset:3072 lds\n"
"v_mfma_f32_16x16x32_bf16 v[212:215], a[44:47], a[132:135], v[212:215]\n"
"s_add_u32 s82, s82, s53\n"
"ds_read_b64_tr_b16 a[102:103], v3 offset:28224\n"
"v_mfma_f32_16x16x32_bf16 v[100:103], a[16:19], a[132:135], v[100:103]\n"
"s_add_u32 s83, s83, s54\n"
"ds_read_b64_tr_b16 a[76:77], v3 offset:16480\n"
"v_mfma_f32_16x16x32_bf16 v[196:199], a[40:43], a[132:135], v[196:199]\n"
"ds_read_b64_tr_b16 a[78:79], v3 offset:20064\n"
"v_mfma_f32_16x16x32_bf16 v[84:87], a[12:15], a[132:135], v[84:87]\n"
"v_mfma_f32_16x16x32_bf16 v[180:183], a[36:39], a[132:135], v[180:183]\n"
"ds_read_b64_tr_b16 a[104:105], v3 offset:24672\n"
"v_mfma_f32_16x16x32_bf16 v[68:71], a[8:11], a[132:135], v[68:71]\n"
"ds_read_b64_tr_b16 a[106:107], v3 offset:28256\n"
"v_mfma_f32_16x16x32_bf16 v[164:167], a[32:35], a[132:135], v[164:167]\n"
"ds_read_b64_tr_b16 a[80:81], v3 offset:16512\n"
"v_mfma_f32_16x16x32_bf16 v[52:55], a[4:7], a[132:135], v[52:55]\n"
"buffer_load_dwordx4 a[160:163], v6, s[28:31], s91 offen offset:0 sc1\n"
"v_mfma_f32_16x16x32_bf16 v[148:151], a[28:31], a[132:135], v[148:151]\n"
"ds_read_b64_tr_b16 a[82:83], v3 offset:20096\n"
"v_mfma_f32_16x16x32_bf16 v[36:39], a[0:3], a[132:135], v[36:39]\n"
"ds_read_b64_tr_b16 a[108:109], v3 offset:24704\n"
"v_mfma_f32_16x16x32_bf16 a[212:215], a[60:63], a[132:135], a[212:215]\n"
"ds_read_b64_tr_b16 a[110:111], v3 offset:28288\n"
"v_mfma_f32_16x16x32_bf16 a[196:199], a[56:59], a[132:135], a[196:199]\n"
"buffer_load_dwordx4 a[164:167], v6, s[28:31], s91 offen offset:1024 sc1\n"
"v_mfma_f32_16x16x32_bf16 a[200:203], a[56:59], a[136:139], a[200:203]\n"
"ds_read_b64_tr_b16 a[84:85], v3 offset:16544\n"
"v_mfma_f32_16x16x32_bf16 a[216:219], a[60:63], a[136:139], a[216:219]\n"
"ds_read_b64_tr_b16 a[86:87], v3 offset:20128\n"
"v_mfma_f32_16x16x32_bf16 v[40:43], a[0:3], a[136:139], v[40:43]\n"
"ds_read_b64_tr_b16 a[112:113], v3 offset:24736\n"
"v_mfma_f32_16x16x32_bf16 v[152:155], a[28:31], a[136:139], v[152:155]\n"
"v_mfma_f32_16x16x32_bf16 v[56:59], a[4:7], a[136:139], v[56:59]\n"
"ds_read_b64_tr_b16 a[114:115], v3 offset:28320\n"
"v_mfma_f32_16x16x32_bf16 v[168:171], a[32:35], a[136:139], v[168:171]\n"
"ds_read_b64_tr_b16 a[88:89], v3 offset:16576\n"
"v_mfma_f32_16x16x32_bf16 v[72:75], a[8:11], a[136:139], v[72:75]\n"
"ds_read_b64_tr_b16 a[90:91], v3 offset:20160\n"
"v_mfma_f32_16x16x32_bf16 v[184:187], a[36:39], a[136:139], v[184:187]\n"
"buffer_load_dwordx4 a[168:171], v6, s[28:31], s91 offen offset:2048 sc1\n"
"v_mfma_f32_16x16x32_bf16 v[88:91], a[12:15], a[136:139], v[88:91]\n"
"ds_read_b64_tr_b16 a[116:117], v3 offset:24768\n"
"v_mfma_f32_16x16x32_bf16 v[200:203], a[40:43], a[136:139], v[200:203]\n"
"ds_read_b64_tr_b16 a[118:119], v3 offset:28352\n"
"v_mfma_f32_16x16x32_bf16 v[104:107], a[16:19], a[136:139], v[104:107]\n"
"v_mfma_f32_16x16x32_bf16 v[216:219], a[44:47], a[136:139], v[216:219]\n"
"buffer_load_dwordx4 a[172:175], v6, s[28:31], s91 offen offset:3072 sc1\n"
"v_mfma_f32_16x16x32_bf16 v[120:123], a[20:23], a[136:139], v[120:123]\n"
"s_add_u32 s91, s91, 16384\n"
"v_mfma_f32_16x16x32_bf16 v[232:235], a[48:51], a[136:139], v[232:235]\n"
"v_mfma_f32_16x16x32_bf16 v[136:139], a[24:27], a[136:139], v[136:139]\n"
"v_mfma_f32_16x16x32_bf16 v[248:251], a[52:55], a[136:139], v[248:251]\n"
"v_mfma_f32_16x16x32_bf16 v[252:255], a[52:55], a[140:143], v[252:255]\n"
"v_mfma_f32_16x16x32_bf16 v[140:143], a[24:27], a[140:143], v[140:143]\n"
"s_add_u32 m0, s55, 49152\n"
"v_mfma_f32_16x16x32_bf16 v[236:239], a[48:51], a[140:143], v[236:239]\n"
"v_mfma_f32_16x16x32_bf16 v[124:127], a[20:23], a[140:143], v[124:127]\n"
"buffer_load_dwordx4 v10, s[20:23], s82 offen offset:0 lds\n"
"v_mfma_f32_16x16x32_bf16 v[220:223], a[44:47], a[140:143], v[220:223]\n"
"v_mfma_f32_16x16x32_bf16 v[108:111], a[16:19], a[140:143], v[108:111]\n"
"v_mfma_f32_16x16x32_bf16 v[204:207], a[40:43], a[140:143], v[204:207]\n"
"v_mfma_f32_16x16x32_bf16 v[92:95], a[12:15], a[140:143], v[92:95]\n"
"buffer_load_dwordx4 v11, s[20:23], s82 offen offset:1024 lds\n"
"v_mfma_f32_16x16x32_bf16 v[188:191], a[36:39], a[140:143], v[188:191]\n"
"v_mfma_f32_16x16x32_bf16 v[76:79], a[8:11], a[140:143], v[76:79]\n"
"v_mfma_f32_16x16x32_bf16 v[172:175], a[32:35], a[140:143], v[172:175]\n"
"v_mfma_f32_16x16x32_bf16 v[60:63], a[4:7], a[140:143], v[60:63]\n"
"v_mfma_f32_16x16x32_bf16 v[156:159], a[28:31], a[140:143], v[156:159]\n"
"s_waitcnt vmcnt(6) lgkmcnt(0)\n"
"s_barrier\n"
"v_mfma_f32_16x16x32_bf16 v[44:47], a[0:3], a[140:143], v[44:47]\n"
"v_mfma_f32_16x16x32_bf16 a[220:223], a[60:63], a[140:143], a[220:223]\n"
"v_mfma_f32_16x16x32_bf16 a[204:207], a[56:59], a[140:143], a[204:207]\n"
"buffer_load_dwordx4 v12, s[20:23], s82 offen offset:2048 lds\n"
"v_mfma_f32_16x16x32_bf16 a[192:195], a[120:123], a[144:147], a[192:195]\n"
"ds_read_b64_tr_b16 a[56:57], v4 offset:39936\n"
"v_mfma_f32_16x16x32_bf16 a[208:211], a[124:127], a[144:147], a[208:211]\n"
"ds_read_b64_tr_b16 a[58:59], v4 offset:48128\n"
"v_mfma_f32_16x16x32_bf16 v[32:35], a[64:67], a[144:147], v[32:35]\n"
"ds_read_b64_tr_b16 a[60:61], v5 offset:39936\n"
"v_mfma_f32_16x16x32_bf16 v[144:147], a[92:95], a[144:147], v[144:147]\n"
"buffer_load_dwordx4 v13, s[24:27], s83 offen offset:3072 lds\n"
"v_mfma_f32_16x16x32_bf16 v[48:51], a[68:71], a[144:147], v[48:51]\n"
"s_add_u32 s82, s82, s53\n"
"ds_read_b64_tr_b16 a[62:63], v5 offset:48128\n"
"v_mfma_f32_16x16x32_bf16 v[160:163], a[96:99], a[144:147], v[160:163]\n"
"s_add_u32 s83, s83, s54\n"
"ds_read_b64_tr_b16 a[0:1], v3 offset:32768\n"
"v_mfma_f32_16x16x32_bf16 v[64:67], a[72:75], a[144:147], v[64:67]\n"
"ds_read_b64_tr_b16 a[2:3], v3 offset:36352\n"
"v_mfma_f32_16x16x32_bf16 v[176:179], a[100:103], a[144:147], v[176:179]\n"
"v_mfma_f32_16x16x32_bf16 v[80:83], a[76:79], a[144:147], v[80:83]\n"
"ds_read_b64_tr_b16 a[28:29], v3 offset:40960\n"
"v_mfma_f32_16x16x32_bf16 v[192:195], a[104:107], a[144:147], v[192:195]\n"
"ds_read_b64_tr_b16 a[30:31], v3 offset:44544\n"
"v_mfma_f32_16x16x32_bf16 v[96:99], a[80:83], a[144:147], v[96:99]\n"
"ds_read_b64_tr_b16 a[4:5], v3 offset:32800\n"
"v_mfma_f32_16x16x32_bf16 v[208:211], a[108:111], a[144:147], v[208:211]\n"
"buffer_load_dwordx4 a[176:179], v6, s[28:31], s91 offen offset:0 sc1\n"
"v_mfma_f32_16x16x32_bf16 v[112:115], a[84:87], a[144:147], v[112:115]\n"
"ds_read_b64_tr_b16 a[6:7], v3 offset:36384\n"
"v_mfma_f32_16x16x32_bf16 v[224:227], a[112:115], a[144:147], v[224:227]\n"
"ds_read_b64_tr_b16 a[32:33], v3 offset:40992\n"
"v_mfma_f32_16x16x32_bf16 v[128:131], a[88:91], a[144:147], v[128:131]\n"
"ds_read_b64_tr_b16 a[34:35], v3 offset:44576\n"
"v_mfma_f32_16x16x32_bf16 v[240:243], a[116:119], a[144:147], v[240:243]\n"
"buffer_load_dwordx4 a[180:183], v6, s[28:31], s91 offen offset:1024 sc1\n"
"v_mfma_f32_16x16x32_bf16 v[244:247], a[116:119], a[148:151], v[244:247]\n"
"ds_read_b64_tr_b16 a[8:9], v3 offset:32832\n"
"v_mfma_f32_16x16x32_bf16 v[132:135], a[88:91], a[148:151], v[132:135]\n"
"ds_read_b64_tr_b16 a[10:11], v3 offset:36416\n"
"v_mfma_f32_16x16x32_bf16 v[228:231], a[112:115], a[148:151], v[228:231]\n"
"ds_read_b64_tr_b16 a[36:37], v3 offset:41024\n"
"v_mfma_f32_16x16x32_bf16 v[116:119], a[84:87], a[148:151], v[116:119]\n"
"v_mfma_f32_16x16x32_bf16 v[212:215], a[108:111], a[148:151], v[212:215]\n"
"ds_read_b64_tr_b16 a[38:39], v3 offset:44608\n"
"v_mfma_f32_16x16x32_bf16 v[100:103], a[80:83], a[148:151], v[100:103]\n"
"ds_read_b64_tr_b16 a[12:13], v3 offset:32864\n"
"v_mfma_f32_16x16x32_bf16 v[196:199], a[104:107], a[148:151], v[196:199]\n"
"ds_read_b64_tr_b16 a[14:15], v3 offset:36448\n"
"v_mfma_f32_16x16x32_bf16 v[84:87], a[76:79], a[148:151], v[84:87]\n"
"buffer_load_dwordx4 a[184:187], v6, s[28:31], s91 offen offset:2048 sc1\n"
"v_mfma_f32_16x16x32_bf16 v[180:183], a[100:103], a[148:151], v[180:183]\n"
"ds_read_b64_tr_b16 a[40:41], v3 offset:41056\n"
"v_mfma_f32_16x16x32_bf16 v[68:71], a[72:75], a[148:151], v[68:71]\n"
"ds_read_b64_tr_b16 a[42:43], v3 offset:44640\n"
"v_mfma_f32_16x16x32_bf16 v[164:167], a[96:99], a[148:151], v[164:167]\n"
"ds_read_b64_tr_b16 a[16:17], v3 offset:32896\n"
"v_mfma_f32_16x16x32_bf16 v[52:55], a[68:71], a[148:151], v[52:55]\n"
"buffer_load_dwordx4 a[188:191], v6, s[28:31], s91 offen offset:3072 sc1\n"
"v_mfma_f32_16x16x32_bf16 v[148:151], a[92:95], a[148:151], v[148:151]\n"
"s_add_u32 s91, s91, 16384\n"
"ds_read_b64_tr_b16 a[18:19], v3 offset:36480\n"
"v_mfma_f32_16x16x32_bf16 v[36:39], a[64:67], a[148:151], v[36:39]\n"
"ds_read_b64_tr_b16 a[44:45], v3 offset:41088\n"
"v_mfma_f32_16x16x32_bf16 a[212:215], a[124:127], a[148:151], a[212:215]\n"
"ds_read_b64_tr_b16 a[46:47], v3 offset:44672\n"
"v_mfma_f32_16x16x32_bf16 a[196:199], a[120:123], a[148:151], a[196:199]\n"
"v_mfma_f32_16x16x32_bf16 a[200:203], a[120:123], a[152:155], a[200:203]\n"
"ds_read_b64_tr_b16 a[20:21], v3 offset:32928\n"
"v_mfma_f32_16x16x32_bf16 a[216:219], a[124:127], a[152:155], a[216:219]\n"
"s_add_u32 m0, s55, 65536\n"
"ds_read_b64_tr_b16 a[22:23], v3 offset:36512\n"
"v_mfma_f32_16x16x32_bf16 v[40:43], a[64:67], a[152:155], v[40:43]\n"
"ds_read_b64_tr_b16 a[48:49], v3 offset:41120\n"
"v_mfma_f32_16x16x32_bf16 v[152:155], a[92:95], a[152:155], v[152:155]\n"
"buffer_load_dwordx4 v10, s[20:23], s82 offen offset:0 lds\n"
"v_mfma_f32_16x16x32_bf16 v[56:59], a[68:71], a[152:155], v[56:59]\n"
"ds_read_b64_tr_b16 a[50:51], v3 offset:44704\n"
"v_mfma_f32_16x16x32_bf16 v[168:171], a[96:99], a[152:155], v[168:171]\n"
"ds_read_b64_tr_b16 a[24:25], v3 offset:32960\n"
"v_mfma_f32_16x16x32_bf16 v[72:75], a[72:75], a[152:155], v[72:75]\n"
"ds_read_b64_tr_b16 a[26:27], v3 offset:36544\n"
"v_mfma_f32_16x16x32_bf16 v[184:187], a[100:103], a[152:155], v[184:187]\n"
"buffer_load_dwordx4 v11, s[20:23], s82 offen offset:1024 lds\n"
"v_mfma_f32_16x16x32_bf16 v[88:91], a[76:79], a[152:155], v[88:91]\n"
"ds_read_b64_tr_b16 a[52:53], v3 offset:41152\n"
"v_mfma_f32_16x16x32_bf16 v[200:203], a[104:107], a[152:155], v[200:203]\n"
"ds_read_b64_tr_b16 a[54:55], v3 offset:44736\n"
"v_mfma_f32_16x16x32_bf16 v[104:107], a[80:83], a[152:155], v[104:107]\n"
"v_mfma_f32_16x16x32_bf16 v[216:219], a[108:111], a[152:155], v[216:219]\n"
"v_mfma_f32_16x16x32_bf16 v[120:123], a[84:87], a[152:155], v[120:123]\n"
"v_mfma_f32_16x16x32_bf16 v[232:235], a[112:115], a[152:155], v[232:235]\n"
"v_mfma_f32_16x16x32_bf16 v[136:139], a[88:91], a[152:155], v[136:139]\n"
"v_mfma_f32_16x16x32_bf16 v[248:251], a[116:119], a[152:155], v[248:251]\n"
"buffer_load_dwordx4 v12, s[20:23], s82 offen offset:2048 lds\n"
"v_mfma_f32_16x16x32_bf16 v[252:255], a[116:119], a[156:159], v[252:255]\n"
"v_mfma_f32_16x16x32_bf16 v[140:143], a[88:91], a[156:159], v[140:143]\n"
"v_mfma_f32_16x16x32_bf16 v[236:239], a[112:115], a[156:159], v[236:239]\n"
"v_mfma_f32_16x16x32_bf16 v[124:127], a[84:87], a[156:159], v[124:127]\n"
"buffer_load_dwordx4 v13, s[24:27], s83 offen offset:3072 lds\n"
"v_mfma_f32_16x16x32_bf16 v[220:223], a[108:111], a[156:159], v[220:223]\n"
"s_add_u32 s82, s82, s53\n"
"v_mfma_f32_16x16x32_bf16 v[108:111], a[80:83], a[156:159], v[108:111]\n"
"s_add_u32 s83, s83, s54\n"
"v_mfma_f32_16x16x32_bf16 v[204:207], a[104:107], a[156:159], v[204:207]\n"
"v_mfma_f32_16x16x32_bf16 v[92:95], a[76:79], a[156:159], v[92:95]\n"
"v_mfma_f32_16x16x32_bf16 v[188:191], a[100:103], a[156:159], v[188:191]\n"
"v_mfma_f32_16x16x32_bf16 v[76:79], a[72:75], a[156:159], v[76:79]\n"
"v_mfma_f32_16x16x32_bf16 v[172:175], a[96:99], a[156:159], v[172:175]\n"
"v_mfma_f32_16x16x32_bf16 v[60:63], a[68:71], a[156:159], v[60:63]\n"
"buffer_load_dwordx4 a[128:131], v6, s[28:31], s91 offen offset:0 sc1\n"
"v_mfma_f32_16x16x32_bf16 v[156:159], a[92:95], a[156:159], v[156:159]\n"
"s_waitcnt vmcnt(9) lgkmcnt(0)\n"
"s_barrier\n"
"v_mfma_f32_16x16x32_bf16 v[44:47], a[64:67], a[156:159], v[44:47]\n"
"v_mfma_f32_16x16x32_bf16 a[220:223], a[124:127], a[156:159], a[220:223]\n"
"v_mfma_f32_16x16x32_bf16 a[204:207], a[120:123], a[156:159], a[204:207]\n"
"v_mfma_f32_16x16x32_bf16 a[192:195], a[56:59], a[160:163], a[192:195]\n"
"ds_read_b64_tr_b16 a[120:121], v4 offset:56320\n"
"v_mfma_f32_16x16x32_bf16 a[208:211], a[60:63], a[160:163], a[208:211]\n"
"ds_read_b64_tr_b16 a[122:123], v4 offset:64512\n"
"v_mfma_f32_16x16x32_bf16 v[32:35], a[0:3], a[160:163], v[32:35]\n"
"ds_read_b64_tr_b16 a[124:125], v5 offset:56320\n"
"v_mfma_f32_16x16x32_bf16 v[144:147], a[28:31], a[160:163], v[144:147]\n"
"buffer_load_dwordx4 a[132:135], v6, s[28:31], s91 offen offset:1024 sc1\n"
"v_mfma_f32_16x16x32_bf16 v[48:51], a[4:7], a[160:163], v[48:51]\n"
"ds_read_b64_tr_b16 a[126:127], v5 offset:64512\n"
"v_mfma_f32_16x16x32_bf16 v[160:163], a[32:35], a[160:163], v[160:163]\n"
"ds_read_b64_tr_b16 a[64:65], v3 offset:49152\n"
"v_mfma_f32_16x16x32_bf16 v[64:67], a[8:11], a[160:163], v[64:67]\n"
"ds_read_b64_tr_b16 a[66:67], v3 offset:52736\n"
"v_mfma_f32_16x16x32_bf16 v[176:179], a[36:39], a[160:163], v[176:179]\n"
"buffer_load_dwordx4 a[136:139], v6, s[28:31], s91 offen offset:2048 sc1\n"
"v_mfma_f32_16x16x32_bf16 v[80:83], a[12:15], a[160:163], v[80:83]\n"
"ds_read_b64_tr_b16 a[92:93], v3 offset:57344\n"
"v_mfma_f32_16x16x32_bf16 v[192:195], a[40:43], a[160:163], v[192:195]\n"
"ds_read_b64_tr_b16 a[94:95], v3 offset:60928\n"
"v_mfma_f32_16x16x32_bf16 v[96:99], a[16:19], a[160:163], v[96:99]\n"
"ds_read_b64_tr_b16 a[68:69], v3 offset:49184\n"
"v_mfma_f32_16x16x32_bf16 v[208:211], a[44:47], a[160:163], v[208:211]\n"
"v_mfma_f32_16x16x32_bf16 v[112:115], a[20:23], a[160:163], v[112:115]\n"
"ds_read_b64_tr_b16 a[70:71], v3 offset:52768\n"
"v_mfma_f32_16x16x32_bf16 v[224:227], a[48:51], a[160:163], v[224:227]\n"
"ds_read_b64_tr_b16 a[96:97], v3 offset:57376\n"
"v_mfma_f32_16x16x32_bf16 v[128:131], a[24:27], a[160:163], v[128:131]\n"
"ds_read_b64_tr_b16 a[98:99], v3 offset:60960\n"
"v_mfma_f32_16x16x32_bf16 v[240:243], a[52:55], a[160:163], v[240:243]\n"
"buffer_load_dwordx4 a[140:143], v6, s[28:31], s91 offen offset:3072 sc1\n"
"v_mfma_f32_16x16x32_bf16 v[244:247], a[52:55], a[164:167], v[244:247]\n"
"s_add_u32 s91, s91, 16384\n"
"ds_read_b64_tr_b16 a[72:73], v3 offset:49216\n"
"v_mfma_f32_16x16x32_bf16 v[132:135], a[24:27], a[164:167], v[132:135]\n"
"s_add_u32 m0, s55, 81920\n"
"ds_read_b64_tr_b16 a[74:75], v3 offset:52800\n"
"v_mfma_f32_16x16x32_bf16 v[228:231], a[48:51], a[164:167], v[228:231]\n"
"ds_read_b64_tr_b16 a[100:101], v3 offset:57408\n"
"v_mfma_f32_16x16x32_bf16 v[116:119], a[20:23], a[164:167], v[116:119]\n"
"buffer_load_dwordx4 v10, s[20:23], s82 offen offset:0 lds\n"
"v_mfma_f32_16x16x32_bf16 v[212:215], a[44:47], a[164:167], v[212:215]\n"
"ds_read_b64_tr_b16 a[102:103], v3 offset:60992\n"
"v_mfma_f32_16x16x32_bf16 v[100:103], a[16:19], a[164:167], v[100:103]\n"
"ds_read_b64_tr_b16 a[76:77], v3 offset:49248\n"
"v_mfma_f32_16x16x32_bf16 v[196:199], a[40:43], a[164:167], v[196:199]\n"
"ds_read_b64_tr_b16 a[78:79], v3 offset:52832\n"
"v_mfma_f32_16x16x32_bf16 v[84:87], a[12:15], a[164:167], v[84:87]\n"
"v_mfma_f32_16x16x32_bf16 v[180:183], a[36:39], a[164:167], v[180:183]\n"
"ds_read_b64_tr_b16 a[104:105], v3 offset:57440\n"
"v_mfma_f32_16x16x32_bf16 v[68:71], a[8:11], a[164:167], v[68:71]\n"
"ds_read_b64_tr_b16 a[106:107], v3 offset:61024\n"
"v_mfma_f32_16x16x32_bf16 v[164:167], a[32:35], a[164:167], v[164:167]\n"
"ds_read_b64_tr_b16 a[80:81], v3 offset:49280\n"
"v_mfma_f32_16x16x32_bf16 v[52:55], a[4:7], a[164:167], v[52:55]\n"
"buffer_load_dwordx4 v11, s[20:23], s82 offen offset:1024 lds\n"
"v_mfma_f32_16x16x32_bf16 v[148:151], a[28:31], a[164:167], v[148:151]\n"
"ds_read_b64_tr_b16 a[82:83], v3 offset:52864\n"
"v_mfma_f32_16x16x32_bf16 v[36:39], a[0:3], a[164:167], v[36:39]\n"
"ds_read_b64_tr_b16 a[108:109], v3 offset:57472\n"
"v_mfma_f32_16x16x32_bf16 a[212:215], a[60:63], a[164:167], a[212:215]\n"
"ds_read_b64_tr_b16 a[110:111], v3 offset:61056\n"
"v_mfma_f32_16x16x32_bf16 a[196:199], a[56:59], a[164:167], a[196:199]\n"
"buffer_load_dwordx4 v12, s[20:23], s82 offen offset:2048 lds\n"
"v_mfma_f32_16x16x32_bf16 a[200:203], a[56:59], a[168:171], a[200:203]\n"
"ds_read_b64_tr_b16 a[84:85], v3 offset:49312\n"
"v_mfma_f32_16x16x32_bf16 a[216:219], a[60:63], a[168:171], a[216:219]\n"
"ds_read_b64_tr_b16 a[86:87], v3 offset:52896\n"
"v_mfma_f32_16x16x32_bf16 v[40:43], a[0:3], a[168:171], v[40:43]\n"
"ds_read_b64_tr_b16 a[112:113], v3 offset:57504\n"
"v_mfma_f32_16x16x32_bf16 v[152:155], a[28:31], a[168:171], v[152:155]\n"
"v_mfma_f32_16x16x32_bf16 v[56:59], a[4:7], a[168:171], v[56:59]\n"
"ds_read_b64_tr_b16 a[114:115], v3 offset:61088\n"
"v_mfma_f32_16x16x32_bf16 v[168:171], a[32:35], a[168:171], v[168:171]\n"
"ds_read_b64_tr_b16 a[88:89], v3 offset:49344\n"
"v_mfma_f32_16x16x32_bf16 v[72:75], a[8:11], a[168:171], v[72:75]\n"
"ds_read_b64_tr_b16 a[90:91], v3 offset:52928\n"
"v_mfma_f32_16x16x32_bf16 v[184:187], a[36:39], a[168:171], v[184:187]\n"
"buffer_load_dwordx4 v13, s[24:27], s83 offen offset:3072 lds\n"
"v_mfma_f32_16x16x32_bf16 v[88:91], a[12:15], a[168:171], v[88:91]\n"
"s_add_u32 s82, s82, s53\n"
"ds_read_b64_tr_b16 a[116:117], v3 offset:57536\n"
"v_mfma_f32_16x16x32_bf16 v[200:203], a[40:43], a[168:171], v[200:203]\n"
"s_add_u32 s83, s83, s54\n"
"ds_read_b64_tr_b16 a[118:119], v3 offset:61120\n"
"v_mfma_f32_16x16x32_bf16 v[104:107], a[16:19], a[168:171], v[104:107]\n"
"v_mfma_f32_16x16x32_bf16 v[216:219], a[44:47], a[168:171], v[216:219]\n"
"buffer_load_dwordx4 a[144:147], v6, s[28:31], s91 offen offset:0 sc1\n"
"v_mfma_f32_16x16x32_bf16 v[120:123], a[20:23], a[168:171], v[120:123]\n"
"v_mfma_f32_16x16x32_bf16 v[232:235], a[48:51], a[168:171], v[232:235]\n"
"v_mfma_f32_16x16x32_bf16 v[136:139], a[24:27], a[168:171], v[136:139]\n"
"v_mfma_f32_16x16x32_bf16 v[248:251], a[52:55], a[168:171], v[248:251]\n"
"v_mfma_f32_16x16x32_bf16 v[252:255], a[52:55], a[172:175], v[252:255]\n"
"v_xor_b32 v3, 0x10000, v3\n"
"v_mfma_f32_16x16x32_bf16 v[140:143], a[24:27], a[172:175], v[140:143]\n"
"v_xor_b32 v4, 0x10000, v4\n"
"v_mfma_f32_16x16x32_bf16 v[236:239], a[48:51], a[172:175], v[236:239]\n"
"v_xor_b32 v5, 0x10000, v5\n"
"v_mfma_f32_16x16x32_bf16 v[124:127], a[20:23], a[172:175], v[124:127]\n"
"buffer_load_dwordx4 a[148:151], v6, s[28:31], s91 offen offset:1024 sc1\n"
"v_mfma_f32_16x16x32_bf16 v[220:223], a[44:47], a[172:175], v[220:223]\n"
"v_mfma_f32_16x16x32_bf16 v[108:111], a[16:19], a[172:175], v[108:111]\n"
"v_mfma_f32_16x16x32_bf16 v[204:207], a[40:43], a[172:175], v[204:207]\n"
"v_mfma_f32_16x16x32_bf16 v[92:95], a[12:15], a[172:175], v[92:95]\n"
"buffer_load_dwordx4 a[152:155], v6, s[28:31], s91 offen offset:2048 sc1\n"
"v_mfma_f32_16x16x32_bf16 v[188:191], a[36:39], a[172:175], v[188:191]\n"
"v_mfma_f32_16x16x32_bf16 v[76:79], a[8:11], a[172:175], v[76:79]\n"
"v_mfma_f32_16x16x32_bf16 v[172:175], a[32:35], a[172:175], v[172:175]\n"
"v_mfma_f32_16x16x32_bf16 v[60:63], a[4:7], a[172:175], v[60:63]\n"
"v_mfma_f32_16x16x32_bf16 v[156:159], a[28:31], a[172:175], v[156:159]\n"
"s_waitcnt vmcnt(11) lgkmcnt(0)\n"
"s_barrier\n"
"v_mfma_f32_16x16x32_bf16 v[44:47], a[0:3], a[172:175], v[44:47]\n"
"v_mfma_f32_16x16x32_bf16 a[220:223], a[60:63], a[172:175], a[220:223]\n"
"v_mfma_f32_16x16x32_bf16 a[204:207], a[56:59], a[172:175], a[204:207]\n"
"buffer_load_dwordx4 a[156:159], v6, s[28:31], s91 offen offset:3072 sc1\n"
"v_mfma_f32_16x16x32_bf16 a[192:195], a[120:123], a[176:179], a[192:195]\n"
"s_add_u32 s91, s91, 16384\n"
"ds_read_b64_tr_b16 a[56:57], v4 offset:7168\n"
"v_mfma_f32_16x16x32_bf16 a[208:211], a[124:127], a[176:179], a[208:211]\n"
"s_add_u32 m0, s55, 98304\n"
"ds_read_b64_tr_b16 a[58:59], v4 offset:15360\n"
"v_mfma_f32_16x16x32_bf16 v[32:35], a[64:67], a[176:179], v[32:35]\n"
"ds_read_b64_tr_b16 a[60:61], v5 offset:7168\n"
"v_mfma_f32_16x16x32_bf16 v[144:147], a[92:95], a[176:179], v[144:147]\n"
"buffer_load_dwordx4 v10, s[20:23], s82 offen offset:0 lds\n"
"v_mfma_f32_16x16x32_bf16 v[48:51], a[68:71], a[176:179], v[48:51]\n"
"ds_read_b64_tr_b16 a[62:63], v5 offset:15360\n"
"v_mfma_f32_16x16x32_bf16 v[160:163], a[96:99], a[176:179], v[160:163]\n"
"ds_read_b64_tr_b16 a[0:1], v3 offset:0\n"
"v_mfma_f32_16x16x32_bf16 v[64:67], a[72:75], a[176:179], v[64:67]\n"
"ds_read_b64_tr_b16 a[2:3], v3 offset:3584\n"
"v_mfma_f32_16x16x32_bf16 v[176:179], a[100:103], a[176:179], v[176:179]\n"
"v_mfma_f32_16x16x32_bf16 v[80:83], a[76:79], a[176:179], v[80:83]\n"
"ds_read_b64_tr_b16 a[28:29], v3 offset:8192\n"
"v_mfma_f32_16x16x32_bf16 v[192:195], a[104:107], a[176:179], v[192:195]\n"
"ds_read_b64_tr_b16 a[30:31], v3 offset:11776\n"
"v_mfma_f32_16x16x32_bf16 v[96:99], a[80:83], a[176:179], v[96:99]\n"
"ds_read_b64_tr_b16 a[4:5], v3 offset:32\n"
"v_mfma_f32_16x16x32_bf16 v[208:211], a[108:111], a[176:179], v[208:211]\n"
"buffer_load_dwordx4 v11, s[20:23], s82 offen offset:1024 lds\n"
"v_mfma_f32_16x16x32_bf16 v[112:115], a[84:87], a[176:179], v[112:115]\n"
"ds_read_b64_tr_b16 a[6:7], v3 offset:3616\n"
"v_mfma_f32_16x16x32_bf16 v[224:227], a[112:115], a[176:179], v[224:227]\n"
"ds_read_b64_tr_b16 a[32:33], v3 offset:8224\n"
"v_mfma_f32_16x16x32_bf16 v[128:131], a[88:91], a[176:179], v[128:131]\n"
"ds_read_b64_tr_b16 a[34:35], v3 offset:11808\n"
"v_mfma_f32_16x16x32_bf16 v[240:243], a[116:119], a[176:179], v[240:243]\n"
"buffer_load_dwordx4 v12, s[20:23], s82 offen offset:2048 lds\n"
"v_mfma_f32_16x16x32_bf16 v[244:247], a[116:119], a[180:183], v[244:247]\n"
"ds_read_b64_tr_b16 a[8:9], v3 offset:64\n"
"v_mfma_f32_16x16x32_bf16 v[132:135], a[88:91], a[180:183], v[132:135]\n"
"ds_read_b64_tr_b16 a[10:11], v3 offset:3648\n"
"v_mfma_f32_16x16x32_bf16 v[228:231], a[112:115], a[180:183], v[228:231]\n"
"ds_read_b64_tr_b16 a[36:37], v3 offset:8256\n"
"v_mfma_f32_16x16x32_bf16 v[116:119], a[84:87], a[180:183], v[116:119]\n"
"v_mfma_f32_16x16x32_bf16 v[212:215], a[108:111], a[180:183], v[212:215]\n"
"ds_read_b64_tr_b16 a[38:39], v3 offset:11840\n"
"v_mfma_f32_16x16x32_bf16 v[100:103], a[80:83], a[180:183], v[100:103]\n"
"ds_read_b64_tr_b16 a[12:13], v3 offset:96\n"
"v_mfma_f32_16x16x32_bf16 v[196:199], a[104:107], a[180:183], v[196:199]\n"
"ds_read_b64_tr_b16 a[14:15], v3 offset:3680\n"
"v_mfma_f32_16x16x32_bf16 v[84:87], a[76:79], a[180:183], v[84:87]\n"
"buffer_load_dwordx4 v13, s[24:27], s83 offen offset:3072 lds\n"
"v_mfma_f32_16x16x32_bf16 v[180:183], a[100:103], a[180:183], v[180:183]\n"
"s_add_u32 s82, s82, s53\n"
"ds_read_b64_tr_b16 a[40:41], v3 offset:8288\n"
"v_mfma_f32_16x16x32_bf16 v[68:71], a[72:75], a[180:183], v[68:71]\n"
"s_add_u32 s83, s83, s54\n"
"ds_read_b64_tr_b16 a[42:43], v3 offset:11872\n"
"v_mfma_f32_16x16x32_bf16 v[164:167], a[96:99], a[180:183], v[164:167]\n"
"ds_read_b64_tr_b16 a[16:17], v3 offset:128\n"
"v_mfma_f32_16x16x32_bf16 v[52:55], a[68:71], a[180:183], v[52:55]\n"
"buffer_load_dwordx4 a[160:163], v6, s[28:31], s91 offen offset:0 sc1\n"
"v_mfma_f32_16x16x32_bf16 v[148:151], a[92:95], a[180:183], v[148:151]\n"
"ds_read_b64_tr_b16 a[18:19], v3 offset:3712\n"
"v_mfma_f32_16x16x32_bf16 v[36:39], a[64:67], a[180:183], v[36:39]\n"
"ds_read_b64_tr_b16 a[44:45], v3 offset:8320\n"
"v_mfma_f32_16x16x32_bf16 a[212:215], a[124:127], a[180:183], a[212:215]\n"
"ds_read_b64_tr_b16 a[46:47], v3 offset:11904\n"
"v_mfma_f32_16x16x32_bf16 a[196:199], a[120:123], a[180:183], a[196:199]\n"
"v_mfma_f32_16x16x32_bf16 a[200:203], a[120:123], a[184:187], a[200:203]\n"
"ds_read_b64_tr_b16 a[20:21], v3 offset:160\n"
"v_mfma_f32_16x16x32_bf16 a[216:219], a[124:127], a[184:187], a[216:219]\n"
"ds_read_b64_tr_b16 a[22:23], v3 offset:3744\n"
"v_mfma_f32_16x16x32_bf16 v[40:43], a[64:67], a[184:187], v[40:43]\n"
"ds_read_b64_tr_b16 a[48:49], v3 offset:8352\n"
"v_mfma_f32_16x16x32_bf16 v[152:155], a[92:95], a[184:187], v[152:155]\n"
"buffer_load_dwordx4 a[164:167], v6, s[28:31], s91 offen offset:1024 sc1\n"
"v_mfma_f32_16x16x32_bf16 v[56:59], a[68:71], a[184:187], v[56:59]\n"
"ds_read_b64_tr_b16 a[50:51], v3 offset:11936\n"
"v_mfma_f32_16x16x32_bf16 v[168:171], a[96:99], a[184:187], v[168:171]\n"
"ds_read_b64_tr_b16 a[24:25], v3 offset:192\n"
"v_mfma_f32_16x16x32_bf16 v[72:75], a[72:75], a[184:187], v[72:75]\n"
"ds_read_b64_tr_b16 a[26:27], v3 offset:3776\n"
"v_mfma_f32_16x16x32_bf16 v[184:187], a[100:103], a[184:187], v[184:187]\n"
"buffer_load_dwordx4 a[168:171], v6, s[28:31], s91 offen offset:2048 sc1\n"
"v_mfma_f32_16x16x32_bf16 v[88:91], a[76:79], a[184:187], v[88:91]\n"
"ds_read_b64_tr_b16 a[52:53], v3 offset:8384\n"
"v_mfma_f32_16x16x32_bf16 v[200:203], a[104:107], a[184:187], v[200:203]\n"
"ds_read_b64_tr_b16 a[54:55], v3 offset:11968\n"
"v_mfma_f32_16x16x32_bf16 v[104:107], a[80:83], a[184:187], v[104:107]\n"
"v_mfma_f32_16x16x32_bf16 v[216:219], a[108:111], a[184:187], v[216:219]\n"
"v_mfma_f32_16x16x32_bf16 v[120:123], a[84:87], a[184:187], v[120:123]\n"
"v_mfma_f32_16x16x32_bf16 v[232:235], a[112:115], a[184:187], v[232:235]\n"
"v_mfma_f32_16x16x32_bf16 v[136:139], a[88:91], a[184:187], v[136:139]\n"
"v_mfma_f32_16x16x32_bf16 v[248:251], a[116:119], a[184:187], v[248:251]\n"
"buffer_load_dwordx4 a[172:175], v6, s[28:31], s91 offen offset:3072 sc1\n"
"v_mfma_f32_16x16x32_bf16 v[252:255], a[116:119], a[188:191], v[252:255]\n"
"s_add_u32 s91, s91, 16384\n"
"v_mfma_f32_16x16x32_bf16 v[140:143], a[88:91], a[188:191], v[140:143]\n"
"v_mfma_f32_16x16x32_bf16 v[236:239], a[112:115], a[188:191], v[236:239]\n"
"v_mfma_f32_16x16x32_bf16 v[124:127], a[84:87], a[188:191], v[124:127]\n"
"v_mfma_f32_16x16x32_bf16 v[220:223], a[108:111], a[188:191], v[220:223]\n"
"v_mfma_f32_16x16x32_bf16 v[108:111], a[80:83], a[188:191], v[108:111]\n"
"s_add_u32 m0, s55, 114688\n"
"v_mfma_f32_16x16x32_bf16 v[204:207], a[104:107], a[188:191], v[204:207]\n"
"v_mfma_f32_16x16x32_bf16 v[92:95], a[76:79], a[188:191], v[92:95]\n"
"buffer_load_dwordx4 v10, s[20:23], s82 offen offset:0 lds\n"
"v_mfma_f32_16x16x32_bf16 v[188:191], a[100:103], a[188:191], v[188:191]\n"
"v_mfma_f32_16x16x32_bf16 v[76:79], a[72:75], a[188:191], v[76:79]\n"
"v_mfma_f32_16x16x32_bf16 v[172:175], a[96:99], a[188:191], v[172:175]\n"
"v_mfma_f32_16x16x32_bf16 v[60:63], a[68:71], a[188:191], v[60:63]\n"
"buffer_load_dwordx4 v11, s[20:23], s82 offen offset:1024 lds\n"
"v_mfma_f32_16x16x32_bf16 v[156:159], a[92:95], a[188:191], v[156:159]\n"
"s_waitcnt vmcnt(14) lgkmcnt(0)\n"
"s_barrier\n"
"v_mfma_f32_16x16x32_bf16 v[44:47], a[64:67], a[188:191], v[44:47]\n"
"v_mfma_f32_16x16x32_bf16 a[220:223], a[124:127], a[188:191], a[220:223]\n"
"v_mfma_f32_16x16x32_bf16 a[204:207], a[120:123], a[188:191], a[204:207]\n"
"v_mfma_f32_16x16x32_bf16 a[192:195], a[56:59], a[128:131], a[192:195]\n"
"ds_read_b64_tr_b16 a[120:121], v4 offset:23552\n"
"v_mfma_f32_16x16x32_bf16 a[208:211], a[60:63], a[128:131], a[208:211]\n"
"ds_read_b64_tr_b16 a[122:123], v4 offset:31744\n"
"v_mfma_f32_16x16x32_bf16 v[32:35], a[0:3], a[128:131], v[32:35]\n"
"ds_read_b64_tr_b16 a[124:125], v5 offset:23552\n"
"v_mfma_f32_16x16x32_bf16 v[144:147], a[28:31], a[128:131], v[144:147]\n"
"buffer_load_dwordx4 v12, s[20:23], s82 offen offset:2048 lds\n"
"v_mfma_f32_16x16x32_bf16 v[48:51], a[4:7], a[128:131], v[48:51]\n"
"ds_read_b64_tr_b16 a[126:127], v5 offset:31744\n"
"v_mfma_f32_16x16x32_bf16 v[160:163], a[32:35], a[128:131], v[160:163]\n"
"ds_read_b64_tr_b16 a[64:65], v3 offset:16384\n"
"v_mfma_f32_16x16x32_bf16 v[64:67], a[8:11], a[128:131], v[64:67]\n"
"ds_read_b64_tr_b16 a[66:67], v3 offset:19968\n"
"v_mfma_f32_16x16x32_bf16 v[176:179], a[36:39], a[128:131], v[176:179]\n"
"buffer_load_dwordx4 v13, s[24:27], s83 offen offset:3072 lds\n"
"v_mfma_f32_16x16x32_bf16 v[80:83], a[12:15], a[128:131], v[80:83]\n"
"s_add_u32 s82, s82, s53\n"
"ds_read_b64_tr_b16 a[92:93], v3 offset:24576\n"
"v_mfma_f32_16x16x32_bf16 v[192:195], a[40:43], a[128:131], v[192:195]\n"
"s_add_u32 s83, s83, s54\n"
"ds_read_b64_tr_b16 a[94:95], v3 offset:28160\n"
"v_mfma_f32_16x16x32_bf16 v[96:99], a[16:19], a[128:131], v[96:99]\n"
"ds_read_b64_tr_b16 a[68:69], v3 offset:16416\n"
"buffer_load_dword v2, v8, s[24:27], s83 offen\n"
"v_mfma_f32_16x16x32_bf16 v[208:211], a[44:47], a[128:131], v[208:211]\n"
"v_mfma_f32_16x16x32_bf16 v[112:115], a[20:23], a[128:131], v[112:115]\n"
"ds_read_b64_tr_b16 a[70:71], v3 offset:20000\n"
"v_mfma_f32_16x16x32_bf16 v[224:227], a[48:51], a[128:131], v[224:227]\n"
"ds_read_b64_tr_b16 a[96:97], v3 offset:24608\n"
"v_mfma_f32_16x16x32_bf16 v[128:131], a[24:27], a[128:131], v[128:131]\n"
"ds_read_b64_tr_b16 a[98:99], v3 offset:28192\n"
"v_mfma_f32_16x16x32_bf16 v[240:243], a[52:55], a[128:131], v[240:243]\n"
"buffer_load_dwordx4 a[176:179], v6, s[28:31], s91 offen offset:0 sc1\n"
"v_mfma_f32_16x16x32_bf16 v[244:247], a[52:55], a[132:135], v[244:247]\n"
"ds_read_b64_tr_b16 a[72:73], v3 offset:16448\n"
"v_mfma_f32_16x16x32_bf16 v[132:135], a[24:27], a[132:135], v[132:135]\n"
"ds_read_b64_tr_b16 a[74:75], v3 offset:20032\n"
"v_mfma_f32_16x16x32_bf16 v[228:231], a[48:51], a[132:135], v[228:231]\n"
"ds_read_b64_tr_b16 a[100:101], v3 offset:24640\n"
"v_mfma_f32_16x16x32_bf16 v[116:119], a[20:23], a[132:135], v[116:119]\n"
"buffer_load_dwordx4 a[180:183], v6, s[28:31], s91 offen offset:1024 sc1\n"
"v_mfma_f32_16x16x32_bf16 v[212:215], a[44:47], a[132:135], v[212:215]\n"
"ds_read_b64_tr_b16 a[102:103], v3 offset:28224\n"
"v_mfma_f32_16x16x32_bf16 v[100:103], a[16:19], a[132:135], v[100:103]\n"
"ds_read_b64_tr_b16 a[76:77], v3 offset:16480\n"
"v_mfma_f32_16x16x32_bf16 v[196:199], a[40:43], a[132:135], v[196:199]\n"
"ds_read_b64_tr_b16 a[78:79], v3 offset:20064\n"
"v_mfma_f32_16x16x32_bf16 v[84:87], a[12:15], a[132:135], v[84:87]\n"
"v_mfma_f32_16x16x32_bf16 v[180:183], a[36:39], a[132:135], v[180:183]\n"
"ds_read_b64_tr_b16 a[104:105], v3 offset:24672\n"
"v_mfma_f32_16x16x32_bf16 v[68:71], a[8:11], a[132:135], v[68:71]\n"
"ds_read_b64_tr_b16 a[106:107], v3 offset:28256\n"
"v_mfma_f32_16x16x32_bf16 v[164:167], a[32:35], a[132:135], v[164:167]\n"
"ds_read_b64_tr_b16 a[80:81], v3 offset:16512\n"
"v_mfma_f32_16x16x32_bf16 v[52:55], a[4:7], a[132:135], v[52:55]\n"
"buffer_load_dwordx4 a[184:187], v6, s[28:31], s91 offen offset:2048 sc1\n"
"v_mfma_f32_16x16x32_bf16 v[148:151], a[28:31], a[132:135], v[148:151]\n"
"ds_read_b64_tr_b16 a[82:83], v3 offset:20096\n"
"v_mfma_f32_16x16x32_bf16 v[36:39], a[0:3], a[132:135], v[36:39]\n"
"ds_read_b64_tr_b16 a[108:109], v3 offset:24704\n"
"v_mfma_f32_16x16x32_bf16 a[212:215], a[60:63], a[132:135], a[212:215]\n"
"ds_read_b64_tr_b16 a[110:111], v3 offset:28288\n"
"v_mfma_f32_16x16x32_bf16 a[196:199], a[56:59], a[132:135], a[196:199]\n"
"buffer_load_dwordx4 a[188:191], v6, s[28:31], s91 offen offset:3072 sc1\n"
"v_mfma_f32_16x16x32_bf16 a[200:203], a[56:59], a[136:139], a[200:203]\n"
"s_add_u32 s91, s91, 16384\n"
"ds_read_b64_tr_b16 a[84:85], v3 offset:16544\n"
"v_mfma_f32_16x16x32_bf16 a[216:219], a[60:63], a[136:139], a[216:219]\n"
"ds_read_b64_tr_b16 a[86:87], v3 offset:20128\n"
"v_mfma_f32_16x16x32_bf16 v[40:43], a[0:3], a[136:139], v[40:43]\n"
"ds_read_b64_tr_b16 a[112:113], v3 offset:24736\n"
"v_mfma_f32_16x16x32_bf16 v[152:155], a[28:31], a[136:139], v[152:155]\n"
"v_mfma_f32_16x16x32_bf16 v[56:59], a[4:7], a[136:139], v[56:59]\n"
"ds_read_b64_tr_b16 a[114:115], v3 offset:28320\n"
"v_mfma_f32_16x16x32_bf16 v[168:171], a[32:35], a[136:139], v[168:171]\n"
"s_add_u32 m0, s55, 0\n"
"ds_read_b64_tr_b16 a[88:89], v3 offset:16576\n"
"v_mfma_f32_16x16x32_bf16 v[72:75], a[8:11], a[136:139], v[72:75]\n"
"ds_read_b64_tr_b16 a[90:91], v3 offset:20160\n"
"v_mfma_f32_16x16x32_bf16 v[184:187], a[36:39], a[136:139], v[184:187]\n"
"buffer_load_dwordx4 v10, s[20:23], s82 offen offset:0 lds\n"
"v_mfma_f32_16x16x32_bf16 v[88:91], a[12:15], a[136:139], v[88:91]\n"
"ds_read_b64_tr_b16 a[116:117], v3 offset:24768\n"
"v_mfma_f32_16x16x32_bf16 v[200:203], a[40:43], a[136:139], v[200:203]\n"
"ds_read_b64_tr_b16 a[118:119], v3 offset:28352\n"
"v_mfma_f32_16x16x32_bf16 v[104:107], a[16:19], a[136:139], v[104:107]\n"
"v_mfma_f32_16x16x32_bf16 v[216:219], a[44:47], a[136:139], v[216:219]\n"
"buffer_load_dwordx4 v11, s[20:23], s82 offen offset:1024 lds\n"
"v_mfma_f32_16x16x32_bf16 v[120:123], a[20:23], a[136:139], v[120:123]\n"
"v_mfma_f32_16x16x32_bf16 v[232:235], a[48:51], a[136:139], v[232:235]\n"
"v_mfma_f32_16x16x32_bf16 v[136:139], a[24:27], a[136:139], v[136:139]\n"
"v_mfma_f32_16x16x32_bf16 v[248:251], a[52:55], a[136:139], v[248:251]\n"
"v_mfma_f32_16x16x32_bf16 v[252:255], a[52:55], a[140:143], v[252:255]\n"
"v_mfma_f32_16x16x32_bf16 v[140:143], a[24:27], a[140:143], v[140:143]\n"
"v_mfma_f32_16x16x32_bf16 v[236:239], a[48:51], a[140:143], v[236:239]\n"
"v_mfma_f32_16x16x32_bf16 v[124:127], a[20:23], a[140:143], v[124:127]\n"
"buffer_load_dwordx4 v12, s[20:23], s82 offen offset:2048 lds\n"
"v_mfma_f32_16x16x32_bf16 v[220:223], a[44:47], a[140:143], v[220:223]\n"
"v_mfma_f32_16x16x32_bf16 v[108:111], a[16:19], a[140:143], v[108:111]\n"
"v_mfma_f32_16x16x32_bf16 v[204:207], a[40:43], a[140:143], v[204:207]\n"
"v_mfma_f32_16x16x32_bf16 v[92:95], a[12:15], a[140:143], v[92:95]\n"
"buffer_load_dwordx4 v13, s[24:27], s83 offen offset:3072 lds\n"
"v_mfma_f32_16x16x32_bf16 v[188:191], a[36:39], a[140:143], v[188:191]\n"
"s_add_u32 s82, s82, s53\n"
"v_mfma_f32_16x16x32_bf16 v[76:79], a[8:11], a[140:143], v[76:79]\n"
"s_add_u32 s83, s83, s54\n"
"v_mfma_f32_16x16x32_bf16 v[172:175], a[32:35], a[140:143], v[172:175]\n"
"v_mfma_f32_16x16x32_bf16 v[60:63], a[4:7], a[140:143], v[60:63]\n"
"v_mfma_f32_16x16x32_bf16 v[156:159], a[28:31], a[140:143], v[156:159]\n"
"s_waitcnt vmcnt(17) lgkmcnt(0)\n"
"s_barrier\n"
"v_mfma_f32_16x16x32_bf16 v[44:47], a[0:3], a[140:143], v[44:47]\n"
"v_mfma_f32_16x16x32_bf16 a[220:223], a[60:63], a[140:143], a[220:223]\n"
"v_mfma_f32_16x16x32_bf16 a[204:207], a[56:59], a[140:143], a[204:207]\n"
"buffer_load_dwordx4 a[128:131], v6, s[28:31], s91 offen offset:0 sc1\n"
"v_mfma_f32_16x16x32_bf16 a[192:195], a[120:123], a[144:147], a[192:195]\n"
"ds_read_b64_tr_b16 a[56:57], v4 offset:39936\n"
"v_mfma_f32_16x16x32_bf16 a[208:211], a[124:127], a[144:147], a[208:211]\n"
"ds_read_b64_tr_b16 a[58:59], v4 offset:48128\n"
"v_mfma_f32_16x16x32_bf16 v[32:35], a[64:67], a[144:147], v[32:35]\n"
"ds_read_b64_tr_b16 a[60:61], v5 offset:39936\n"
"v_mfma_f32_16x16x32_bf16 v[144:147], a[92:95], a[144:147], v[144:147]\n"
"buffer_load_dwordx4 a[132:135], v6, s[28:31], s91 offen offset:1024 sc1\n"
"v_mfma_f32_16x16x32_bf16 v[48:51], a[68:71], a[144:147], v[48:51]\n"
"ds_read_b64_tr_b16 a[62:63], v5 offset:48128\n"
"v_mfma_f32_16x16x32_bf16 v[160:163], a[96:99], a[144:147], v[160:163]\n"
"ds_read_b64_tr_b16 a[0:1], v3 offset:32768\n"
"v_mfma_f32_16x16x32_bf16 v[64:67], a[72:75], a[144:147], v[64:67]\n"
"ds_read_b64_tr_b16 a[2:3], v3 offset:36352\n"
"v_mfma_f32_16x16x32_bf16 v[176:179], a[100:103], a[144:147], v[176:179]\n"
"v_mfma_f32_16x16x32_bf16 v[80:83], a[76:79], a[144:147], v[80:83]\n"
"ds_read_b64_tr_b16 a[28:29], v3 offset:40960\n"
"v_mfma_f32_16x16x32_bf16 v[192:195], a[104:107], a[144:147], v[192:195]\n"
"ds_read_b64_tr_b16 a[30:31], v3 offset:44544\n"
"v_mfma_f32_16x16x32_bf16 v[96:99], a[80:83], a[144:147], v[96:99]\n"
"ds_read_b64_tr_b16 a[4:5], v3 offset:32800\n"
"v_mfma_f32_16x16x32_bf16 v[208:211], a[108:111], a[144:147], v[208:211]\n"
"buffer_load_dwordx4 a[136:139], v6, s[28:31], s91 offen offset:2048 sc1\n"
"v_mfma_f32_16x16x32_bf16 v[112:115], a[84:87], a[144:147], v[112:115]\n"
"ds_read_b64_tr_b16 a[6:7], v3 offset:36384\n"
"v_mfma_f32_16x16x32_bf16 v[224:227], a[112:115], a[144:147], v[224:227]\n"
"ds_read_b64_tr_b16 a[32:33], v3 offset:40992\n"
"v_mfma_f32_16x16x32_bf16 v[128:131], a[88:91], a[144:147], v[128:131]\n"
"ds_read_b64_tr_b16 a[34:35], v3 offset:44576\n"
"v_mfma_f32_16x16x32_bf16 v[240:243], a[116:119], a[144:147], v[240:243]\n"
"buffer_load_dwordx4 a[140:143], v6, s[28:31], s91 offen offset:3072 sc1\n"
"v_mfma_f32_16x16x32_bf16 v[244:247], a[116:119], a[148:151], v[244:247]\n"
"s_add_u32 s91, s91, 16384\n"
"ds_read_b64_tr_b16 a[8:9], v3 offset:32832\n"
"v_mfma_f32_16x16x32_bf16 v[132:135], a[88:91], a[148:151], v[132:135]\n"
"ds_read_b64_tr_b16 a[10:11], v3 offset:36416\n"
"v_mfma_f32_16x16x32_bf16 v[228:231], a[112:115], a[148:151], v[228:231]\n"
"ds_read_b64_tr_b16 a[36:37], v3 offset:41024\n"
"v_mfma_f32_16x16x32_bf16 v[116:119], a[84:87], a[148:151], v[116:119]\n"
"v_mfma_f32_16x16x32_bf16 v[212:215], a[108:111], a[148:151], v[212:215]\n"
"ds_read_b64_tr_b16 a[38:39], v3 offset:44608\n"
"v_mfma_f32_16x16x32_bf16 v[100:103], a[80:83], a[148:151], v[100:103]\n"
"s_add_u32 m0, s55, 16384\n"
"ds_read_b64_tr_b16 a[12:13], v3 offset:32864\n"
"v_mfma_f32_16x16x32_bf16 v[196:199], a[104:107], a[148:151], v[196:199]\n"
"ds_read_b64_tr_b16 a[14:15], v3 offset:36448\n"
"v_mfma_f32_16x16x32_bf16 v[84:87], a[76:79], a[148:151], v[84:87]\n"
"buffer_load_dwordx4 v10, s[20:23], s82 offen offset:0 lds\n"
"v_mfma_f32_16x16x32_bf16 v[180:183], a[100:103], a[148:151], v[180:183]\n"
"ds_read_b64_tr_b16 a[40:41], v3 offset:41056\n"
"v_mfma_f32_16x16x32_bf16 v[68:71], a[72:75], a[148:151], v[68:71]\n"
"ds_read_b64_tr_b16 a[42:43], v3 offset:44640\n"
"v_mfma_f32_16x16x32_bf16 v[164:167], a[96:99], a[148:151], v[164:167]\n"
"ds_read_b64_tr_b16 a[16:17], v3 offset:32896\n"
"v_mfma_f32_16x16x32_bf16 v[52:55], a[68:71], a[148:151], v[52:55]\n"
"buffer_load_dwordx4 v11, s[20:23], s82 offen offset:1024 lds\n"
"v_mfma_f32_16x16x32_bf16 v[148:151], a[92:95], a[148:151], v[148:151]\n"
"ds_read_b64_tr_b16 a[18:19], v3 offset:36480\n"
"v_mfma_f32_16x16x32_bf16 v[36:39], a[64:67], a[148:151], v[36:39]\n"
"ds_read_b64_tr_b16 a[44:45], v3 offset:41088\n"
"v_mfma_f32_16x16x32_bf16 a[212:215], a[124:127], a[148:151], a[212:215]\n"
"ds_read_b64_tr_b16 a[46:47], v3 offset:44672\n"
"v_mfma_f32_16x16x32_bf16 a[196:199], a[120:123], a[148:151], a[196:199]\n"
"v_mfma_f32_16x16x32_bf16 a[200:203], a[120:123], a[152:155], a[200:203]\n"
"ds_read_b64_tr_b16 a[20:21], v3 offset:32928\n"
"v_mfma_f32_16x16x32_bf16 a[216:219], a[124:127], a[152:155], a[216:219]\n"
"ds_read_b64_tr_b16 a[22:23], v3 offset:36512\n"
"v_mfma_f32_16x16x32_bf16 v[40:43], a[64:67], a[152:155], v[40:43]\n"
"ds_read_b64_tr_b16 a[48:49], v3 offset:41120\n"
"v_mfma_f32_16x16x32_bf16 v[152:155], a[92:95], a[152:155], v[152:155]\n"
"buffer_load_dwordx4 v12, s[20:23], s82 offen offset:2048 lds\n"
"v_mfma_f32_16x16x32_bf16 v[56:59], a[68:71], a[152:155], v[56:59]\n"
"ds_read_b64_tr_b16 a[50:51], v3 offset:44704\n"
"v_mfma_f32_16x16x32_bf16 v[168:171], a[96:99], a[152:155], v[168:171]\n"
"ds_read_b64_tr_b16 a[24:25], v3 offset:32960\n"
"v_mfma_f32_16x16x32_bf16 v[72:75], a[72:75], a[152:155], v[72:75]\n"
"ds_read_b64_tr_b16 a[26:27], v3 offset:36544\n"
"v_mfma_f32_16x16x32_bf16 v[184:187], a[100:103], a[152:155], v[184:187]\n"
"buffer_load_dwordx4 v13, s[24:27], s83 offen offset:3072 lds\n"
"v_mfma_f32_16x16x32_bf16 v[88:91], a[76:79], a[152:155], v[88:91]\n"
"s_add_u32 s82, s82, s53\n"
"ds_read_b64_tr_b16 a[52:53], v3 offset:41152\n"
"v_mfma_f32_16x16x32_bf16 v[200:203], a[104:107], a[152:155], v[200:203]\n"
"s_add_u32 s83, s83, s54\n"
"ds_read_b64_tr_b16 a[54:55], v3 offset:44736\n"
"v_mfma_f32_16x16x32_bf16 v[104:107], a[80:83], a[152:155], v[104:107]\n"
"v_mfma_f32_16x16x32_bf16 v[216:219], a[108:111], a[152:155], v[216:219]\n"
"v_mfma_f32_16x16x32_bf16 v[120:123], a[84:87], a[152:155], v[120:123]\n"
"v_mfma_f32_16x16x32_bf16 v[232:235], a[112:115], a[152:155], v[232:235]\n"
"v_mfma_f32_16x16x32_bf16 v[136:139], a[88:91], a[152:155], v[136:139]\n"
"v_mfma_f32_16x16x32_bf16 v[248:251], a[116:119], a[152:155], v[248:251]\n"
"buffer_load_dwordx4 a[144:147], v6, s[28:31], s91 offen offset:0 sc1\n"
"v_mfma_f32_16x16x32_bf16 v[252:255], a[116:119], a[156:159], v[252:255]\n"
"v_mfma_f32_16x16x32_bf16 v[140:143], a[88:91], a[156:159], v[140:143]\n"
"v_mfma_f32_16x16x32_bf16 v[236:239], a[112:115], a[156:159], v[236:239]\n"
"v_mfma_f32_16x16x32_bf16 v[124:127], a[84:87], a[156:159], v[124:127]\n"
"v_mfma_f32_16x16x32_bf16 v[220:223], a[108:111], a[156:159], v[220:223]\n"
"v_mfma_f32_16x16x32_bf16 v[108:111], a[80:83], a[156:159], v[108:111]\n"
"buffer_store_dwordx2 v[24:25], v22, s[16:19], 0 offen sc1\n"
"v_mfma_f32_16x16x32_bf16 v[204:207], a[104:107], a[156:159], v[204:207]\n"
"v_mfma_f32_16x16x32_bf16 v[92:95], a[76:79], a[156:159], v[92:95]\n"
"buffer_load_dwordx4 a[148:151], v6, s[28:31], s91 offen offset:1024 sc1\n"
"v_mfma_f32_16x16x32_bf16 v[188:191], a[100:103], a[156:159], v[188:191]\n"
"v_mfma_f32_16x16x32_bf16 v[76:79], a[72:75], a[156:159], v[76:79]\n"
"buffer_store_dwordx2 v[26:27], v23, s[16:19], 0 offen sc1\n"
"v_mfma_f32_16x16x32_bf16 v[172:175], a[96:99], a[156:159], v[172:175]\n"
"v_mfma_f32_16x16x32_bf16 v[60:63], a[68:71], a[156:159], v[60:63]\n"
"buffer_load_dwordx4 a[152:155], v6, s[28:31], s91 offen offset:2048 sc1\n"
"v_mfma_f32_16x16x32_bf16 v[156:159], a[92:95], a[156:159], v[156:159]\n"
"s_waitcnt vmcnt(22) lgkmcnt(0)\n"
"s_barrier\n"
"v_mfma_f32_16x16x32_bf16 v[44:47], a[64:67], a[156:159], v[44:47]\n"
"s_mov_b64 exec, 1\n"
"buffer_store_dword v28, off, s[16:19], s52 sc1\n"
"s_mov_b64 exec, -1\n"
"v_mfma_f32_16x16x32_bf16 a[220:223], a[124:127], a[156:159], a[220:223]\n"
"v_mfma_f32_16x16x32_bf16 a[204:207], a[120:123], a[156:159], a[204:207]\n"
"v_mfma_f32_16x16x32_bf16 a[192:195], a[56:59], a[160:163], a[192:195]\n"
"ds_read_b64_tr_b16 a[120:121], v4 offset:56320\n"
"v_mfma_f32_16x16x32_bf16 a[208:211], a[60:63], a[160:163], a[208:211]\n"
"ds_read_b64_tr_b16 a[122:123], v4 offset:64512\n"
"v_mfma_f32_16x16x32_bf16 v[32:35], a[0:3], a[160:163], v[32:35]\n"
"ds_read_b64_tr_b16 a[124:125], v5 offset:56320\n"
"v_mfma_f32_16x16x32_bf16 v[144:147], a[28:31], a[160:163], v[144:147]\n"
"buffer_load_dwordx4 a[156:159], v6, s[28:31], s91 offen offset:3072 sc1\n"
"v_mfma_f32_16x16x32_bf16 v[48:51], a[4:7], a[160:163], v[48:51]\n"
"s_add_u32 s91, s91, 16384\n"
"ds_read_b64_tr_b16 a[126:127], v5 offset:64512\n"
"v_mfma_f32_16x16x32_bf16 v[160:163], a[32:35], a[160:163], v[160:163]\n"
"s_add_u32 m0, s55, 32768\n"
"ds_read_b64_tr_b16 a[64:65], v3 offset:49152\n"
"v_mfma_f32_16x16x32_bf16 v[64:67], a[8:11], a[160:163], v[64:67]\n"
"ds_read_b64_tr_b16 a[66:67], v3 offset:52736\n"
"v_mfma_f32_16x16x32_bf16 v[176:179], a[36:39], a[160:163], v[176:179]\n"
"buffer_load_dwordx4 v10, s[20:23], s82 offen offset:0 lds\n"
"v_mfma_f32_16x16x32_bf16 v[80:83], a[12:15], a[160:163], v[80:83]\n"
"ds_read_b64_tr_b16 a[92:93], v3 offset:57344\n"
"v_mfma_f32_16x16x32_bf16 v[192:195], a[40:43], a[160:163], v[192:195]\n"
"ds_read_b64_tr_b16 a[94:95], v3 offset:60928\n"
"v_mfma_f32_16x16x32_bf16 v[96:99], a[16:19], a[160:163], v[96:99]\n"
"ds_read_b64_tr_b16 a[68:69], v3 offset:49184\n"
"v_mfma_f32_16x16x32_bf16 v[208:211], a[44:47], a[160:163], v[208:211]\n"
"v_mfma_f32_16x16x32_bf16 v[112:115], a[20:23], a[160:163], v[112:115]\n"
"ds_read_b64_tr_b16 a[70:71], v3 offset:52768\n"
"v_mfma_f32_16x16x32_bf16 v[224:227], a[48:51], a[160:163], v[224:227]\n"
"ds_read_b64_tr_b16 a[96:97], v3 offset:57376\n"
"v_mfma_f32_16x16x32_bf16 v[128:131], a[24:27], a[160:163], v[128:131]\n"
"ds_read_b64_tr_b16 a[98:99], v3 offset:60960\n"
"v_mfma_f32_16x16x32_bf16 v[240:243], a[52:55], a[160:163], v[240:243]\n"
"buffer_load_dwordx4 v11, s[20:23], s82 offen offset:1024 lds\n"
"v_mfma_f32_16x16x32_bf16 v[244:247], a[52:55], a[164:167], v[244:247]\n"
"ds_read_b64_tr_b16 a[72:73], v3 offset:49216\n"
"v_mfma_f32_16x16x32_bf16 v[132:135], a[24:27], a[164:167], v[132:135]\n"
"ds_read_b64_tr_b16 a[74:75], v3 offset:52800\n"
"v_mfma_f32_16x16x32_bf16 v[228:231], a[48:51], a[164:167], v[228:231]\n"
"ds_read_b64_tr_b16 a[100:101], v3 offset:57408\n"
"v_mfma_f32_16x16x32_bf16 v[116:119], a[20:23], a[164:167], v[116:119]\n"
"buffer_load_dwordx4 v12, s[20:23], s82 offen offset:2048 lds\n"
"v_mfma_f32_16x16x32_bf16 v[212:215], a[44:47], a[164:167], v[212:215]\n"
"ds_read_b64_tr_b16 a[102:103], v3 offset:60992\n"
"v_mfma_f32_16x16x32_bf16 v[100:103], a[16:19], a[164:167], v[100:103]\n"
"ds_read_b64_tr_b16 a[76:77], v3 offset:49248\n"
"v_mfma_f32_16x16x32_bf16 v[196:199], a[40:43], a[164:167], v[196:199]\n"
"ds_read_b64_tr_b16 a[78:79], v3 offset:52832\n"
"v_mfma_f32_16x16x32_bf16 v[84:87], a[12:15], a[164:167], v[84:87]\n"
"v_mfma_f32_16x16x32_bf16 v[180:183], a[36:39], a[164:167], v[180:183]\n"
"ds_read_b64_tr_b16 a[104:105], v3 offset:57440\n"
"v_mfma_f32_16x16x32_bf16 v[68:71], a[8:11], a[164:167], v[68:71]\n"
"ds_read_b64_tr_b16 a[106:107], v3 offset:61024\n"
"v_mfma_f32_16x16x32_bf16 v[164:167], a[32:35], a[164:167], v[164:167]\n"
"ds_read_b64_tr_b16 a[80:81], v3 offset:49280\n"
"v_mfma_f32_16x16x32_bf16 v[52:55], a[4:7], a[164:167], v[52:55]\n"
"buffer_load_dwordx4 v13, s[24:27], s83 offen offset:3072 lds\n"
"v_mfma_f32_16x16x32_bf16 v[148:151], a[28:31], a[164:167], v[148:151]\n"
"s_add_u32 s82, s82, s53\n"
"ds_read_b64_tr_b16 a[82:83], v3 offset:52864\n"
"v_mfma_f32_16x16x32_bf16 v[36:39], a[0:3], a[164:167], v[36:39]\n"
"s_add_u32 s83, s83, s54\n"
"ds_read_b64_tr_b16 a[108:109], v3 offset:57472\n"
"v_mfma_f32_16x16x32_bf16 a[212:215], a[60:63], a[164:167], a[212:215]\n"
"ds_read_b64_tr_b16 a[110:111], v3 offset:61056\n"
"v_mfma_f32_16x16x32_bf16 a[196:199], a[56:59], a[164:167], a[196:199]\n"
"buffer_load_dwordx4 a[160:163], v6, s[28:31], s91 offen offset:0 sc1\n"
"v_mfma_f32_16x16x32_bf16 a[200:203], a[56:59], a[168:171], a[200:203]\n"
"ds_read_b64_tr_b16 a[84:85], v3 offset:49312\n"
"v_mfma_f32_16x16x32_bf16 a[216:219], a[60:63], a[168:171], a[216:219]\n"
"ds_read_b64_tr_b16 a[86:87], v3 offset:52896\n"
"v_mfma_f32_16x16x32_bf16 v[40:43], a[0:3], a[168:171], v[40:43]\n"
"ds_read_b64_tr_b16 a[112:113], v3 offset:57504\n"
"v_mfma_f32_16x16x32_bf16 v[152:155], a[28:31], a[168:171], v[152:155]\n"
"v_mfma_f32_16x16x32_bf16 v[56:59], a[4:7], a[168:171], v[56:59]\n"
"ds_read_b64_tr_b16 a[114:115], v3 offset:61088\n"
"v_mfma_f32_16x16x32_bf16 v[168:171], a[32:35], a[168:171], v[168:171]\n"
"ds_read_b64_tr_b16 a[88:89], v3 offset:49344\n"
"v_mfma_f32_16x16x32_bf16 v[72:75], a[8:11], a[168:171], v[72:75]\n"
"ds_read_b64_tr_b16 a[90:91], v3 offset:52928\n"
"v_mfma_f32_16x16x32_bf16 v[184:187], a[36:39], a[168:171], v[184:187]\n"
"buffer_load_dwordx4 a[164:167], v6, s[28:31], s91 offen offset:1024 sc1\n"
"v_mfma_f32_16x16x32_bf16 v[88:91], a[12:15], a[168:171], v[88:91]\n"
"ds_read_b64_tr_b16 a[116:117], v3 offset:57536\n"
"v_mfma_f32_16x16x32_bf16 v[200:203], a[40:43], a[168:171], v[200:203]\n"
"ds_read_b64_tr_b16 a[118:119], v3 offset:61120\n"
"v_mfma_f32_16x16x32_bf16 v[104:107], a[16:19], a[168:171], v[104:107]\n"
"v_mfma_f32_16x16x32_bf16 v[216:219], a[44:47], a[168:171], v[216:219]\n"
"v_mfma_f32_16x16x32_bf16 v[120:123], a[20:23], a[168:171], v[120:123]\n"
"s_lshr_b32 s88, s61, 3\n"
"v_mfma_f32_16x16x32_bf16 v[232:235], a[48:51], a[168:171], v[232:235]\n"
"s_lshl_b32 s88, s88, 13\n"
"v_mfma_f32_16x16x32_bf16 v[136:139], a[24:27], a[168:171], v[136:139]\n"
"s_add_u32 s88, s88, 266240\n"
"v_mfma_f32_16x16x32_bf16 v[248:251], a[52:55], a[168:171], v[248:251]\n"
"buffer_load_dwordx4 a[168:171], v6, s[28:31], s91 offen offset:2048 sc1\n"
"v_mfma_f32_16x16x32_bf16 v[252:255], a[52:55], a[172:175], v[252:255]\n"
"v_xor_b32 v3, 0x10000, v3\n"
"s_mul_i32 s14, s54, 12\n"
"v_mfma_f32_16x16x32_bf16 v[140:143], a[24:27], a[172:175], v[140:143]\n"
"v_xor_b32 v4, 0x10000, v4\n"
"s_lshl_b32 s15, s54, 2\n"
"v_mfma_f32_16x16x32_bf16 v[236:239], a[48:51], a[172:175], v[236:239]\n"
"v_xor_b32 v5, 0x10000, v5\n"
"v_lshl_add_u32 v9, v18, 3, s88\n"
"v_mfma_f32_16x16x32_bf16 v[124:127], a[20:23], a[172:175], v[124:127]\n"
"v_mfma_f32_16x16x32_bf16 v[220:223], a[44:47], a[172:175], v[220:223]\n"
"s_cmp_eq_u32 s48, 3\n"
"s_cselect_b64 vcc, -1, 0\n"
"s_cselect_b32 s14, s77, s14\n"
"s_cselect_b32 s15, 262144, s15\n"
"s_cselect_b32 s8, s32, s24\n"
"s_cselect_b32 s9, s33, s25\n"
"s_cselect_b32 s10, s34, s26\n"
"v_mfma_f32_16x16x32_bf16 v[108:111], a[16:19], a[172:175], v[108:111]\n"
"v_mfma_f32_16x16x32_bf16 v[204:207], a[40:43], a[172:175], v[204:207]\n"
"v_cndmask_b32 v8, v8, v9, vcc\n"
"v_mfma_f32_16x16x32_bf16 v[92:95], a[12:15], a[172:175], v[92:95]\n"
"v_mfma_f32_16x16x32_bf16 v[188:191], a[36:39], a[172:175], v[188:191]\n"
"v_mfma_f32_16x16x32_bf16 v[76:79], a[8:11], a[172:175], v[76:79]\n"
"v_mfma_f32_16x16x32_bf16 v[172:175], a[32:35], a[172:175], v[172:175]\n"
"v_mfma_f32_16x16x32_bf16 v[60:63], a[4:7], a[172:175], v[60:63]\n"
"v_mfma_f32_16x16x32_bf16 v[156:159], a[28:31], a[172:175], v[156:159]\n"
"s_waitcnt vmcnt(22) lgkmcnt(0)\n"
"s_barrier\n"
"v_mfma_f32_16x16x32_bf16 v[44:47], a[0:3], a[172:175], v[44:47]\n"
"v_mfma_f32_16x16x32_bf16 a[220:223], a[60:63], a[172:175], a[220:223]\n"
"v_mfma_f32_16x16x32_bf16 a[204:207], a[56:59], a[172:175], a[204:207]\n"
"buffer_load_dwordx4 a[172:175], v6, s[28:31], s91 offen offset:3072 sc1\n"
"v_mfma_f32_16x16x32_bf16 a[192:195], a[120:123], a[176:179], a[192:195]\n"
"s_add_u32 s91, s91, 16384\n"
"ds_read_b64_tr_b16 a[56:57], v4 offset:7168\n"
"v_mfma_f32_16x16x32_bf16 a[208:211], a[124:127], a[176:179], a[208:211]\n"
"s_add_u32 m0, s55, 49152\n"
"ds_read_b64_tr_b16 a[58:59], v4 offset:15360\n"
"v_mfma_f32_16x16x32_bf16 v[32:35], a[64:67], a[176:179], v[32:35]\n"
"ds_read_b64_tr_b16 a[60:61], v5 offset:7168\n"
"v_mfma_f32_16x16x32_bf16 v[144:147], a[92:95], a[176:179], v[144:147]\n"
"buffer_load_dwordx4 v10, s[20:23], s82 offen offset:0 lds\n"
"v_mfma_f32_16x16x32_bf16 v[48:51], a[68:71], a[176:179], v[48:51]\n"
"ds_read_b64_tr_b16 a[62:63], v5 offset:15360\n"
"v_mfma_f32_16x16x32_bf16 v[160:163], a[96:99], a[176:179], v[160:163]\n"
"ds_read_b64_tr_b16 a[0:1], v3 offset:0\n"
"v_mfma_f32_16x16x32_bf16 v[64:67], a[72:75], a[176:179], v[64:67]\n"
"ds_read_b64_tr_b16 a[2:3], v3 offset:3584\n"
"v_mfma_f32_16x16x32_bf16 v[176:179], a[100:103], a[176:179], v[176:179]\n"
"buffer_load_dwordx4 v11, s[20:23], s82 offen offset:1024 lds\n"
"v_mfma_f32_16x16x32_bf16 v[80:83], a[76:79], a[176:179], v[80:83]\n"
"ds_read_b64_tr_b16 a[28:29], v3 offset:8192\n"
"v_mfma_f32_16x16x32_bf16 v[192:195], a[104:107], a[176:179], v[192:195]\n"
"ds_read_b64_tr_b16 a[30:31], v3 offset:11776\n"
"v_mfma_f32_16x16x32_bf16 v[96:99], a[80:83], a[176:179], v[96:99]\n"
"ds_read_b64_tr_b16 a[4:5], v3 offset:32\n"
"v_mfma_f32_16x16x32_bf16 v[208:211], a[108:111], a[176:179], v[208:211]\n"
"buffer_load_dwordx4 v12, s[20:23], s82 offen offset:2048 lds\n"
"v_mfma_f32_16x16x32_bf16 v[112:115], a[84:87], a[176:179], v[112:115]\n"
"ds_read_b64_tr_b16 a[6:7], v3 offset:3616\n"
"v_mfma_f32_16x16x32_bf16 v[224:227], a[112:115], a[176:179], v[224:227]\n"
"ds_read_b64_tr_b16 a[32:33], v3 offset:8224\n"
"v_mfma_f32_16x16x32_bf16 v[128:131], a[88:91], a[176:179], v[128:131]\n"
"ds_read_b64_tr_b16 a[34:35], v3 offset:11808\n"
"v_mfma_f32_16x16x32_bf16 v[240:243], a[116:119], a[176:179], v[240:243]\n"
"buffer_load_dwordx4 v13, s[24:27], s83 offen offset:3072 lds\n"
"v_mfma_f32_16x16x32_bf16 v[244:247], a[116:119], a[180:183], v[244:247]\n"
"s_add_u32 s82, s82, s53\n"
"ds_read_b64_tr_b16 a[8:9], v3 offset:64\n"
"v_mfma_f32_16x16x32_bf16 v[132:135], a[88:91], a[180:183], v[132:135]\n"
"s_add_u32 s83, s83, s54\n"
"ds_read_b64_tr_b16 a[10:11], v3 offset:3648\n"
"v_mfma_f32_16x16x32_bf16 v[228:231], a[112:115], a[180:183], v[228:231]\n"
"ds_read_b64_tr_b16 a[36:37], v3 offset:8256\n"
"v_mfma_f32_16x16x32_bf16 v[116:119], a[84:87], a[180:183], v[116:119]\n"
"buffer_load_dwordx4 a[176:179], v6, s[28:31], s91 offen offset:0 sc1\n"
"v_mfma_f32_16x16x32_bf16 v[212:215], a[108:111], a[180:183], v[212:215]\n"
"ds_read_b64_tr_b16 a[38:39], v3 offset:11840\n"
"v_mfma_f32_16x16x32_bf16 v[100:103], a[80:83], a[180:183], v[100:103]\n"
"ds_read_b64_tr_b16 a[12:13], v3 offset:96\n"
"v_mfma_f32_16x16x32_bf16 v[196:199], a[104:107], a[180:183], v[196:199]\n"
"ds_read_b64_tr_b16 a[14:15], v3 offset:3680\n"
"v_mfma_f32_16x16x32_bf16 v[84:87], a[76:79], a[180:183], v[84:87]\n"
"v_mfma_f32_16x16x32_bf16 v[180:183], a[100:103], a[180:183], v[180:183]\n"
"ds_read_b64_tr_b16 a[40:41], v3 offset:8288\n"
"v_mfma_f32_16x16x32_bf16 v[68:71], a[72:75], a[180:183], v[68:71]\n"
"ds_read_b64_tr_b16 a[42:43], v3 offset:11872\n"
"v_mfma_f32_16x16x32_bf16 v[164:167], a[96:99], a[180:183], v[164:167]\n"
"ds_read_b64_tr_b16 a[16:17], v3 offset:128\n"
"v_mfma_f32_16x16x32_bf16 v[52:55], a[68:71], a[180:183], v[52:55]\n"
"v_mfma_f32_16x16x32_bf16 v[148:151], a[92:95], a[180:183], v[148:151]\n"
"ds_read_b64_tr_b16 a[18:19], v3 offset:3712\n"
"v_mfma_f32_16x16x32_bf16 v[36:39], a[64:67], a[180:183], v[36:39]\n"
"ds_read_b64_tr_b16 a[44:45], v3 offset:8320\n"
"v_mfma_f32_16x16x32_bf16 a[212:215], a[124:127], a[180:183], a[212:215]\n"
"ds_read_b64_tr_b16 a[46:47], v3 offset:11904\n"
"v_mfma_f32_16x16x32_bf16 a[196:199], a[120:123], a[180:183], a[196:199]\n"
"buffer_load_dwordx4 a[180:183], v6, s[28:31], s91 offen offset:1024 sc1\n"
"v_mfma_f32_16x16x32_bf16 a[200:203], a[120:123], a[184:187], a[200:203]\n"
"ds_read_b64_tr_b16 a[20:21], v3 offset:160\n"
"v_mfma_f32_16x16x32_bf16 a[216:219], a[124:127], a[184:187], a[216:219]\n"
"ds_read_b64_tr_b16 a[22:23], v3 offset:3744\n"
"v_mfma_f32_16x16x32_bf16 v[40:43], a[64:67], a[184:187], v[40:43]\n"
"ds_read_b64_tr_b16 a[48:49], v3 offset:8352\n"
"v_mfma_f32_16x16x32_bf16 v[152:155], a[92:95], a[184:187], v[152:155]\n"
"v_mfma_f32_16x16x32_bf16 v[56:59], a[68:71], a[184:187], v[56:59]\n"
"ds_read_b64_tr_b16 a[50:51], v3 offset:11936\n"
"v_mfma_f32_16x16x32_bf16 v[168:171], a[96:99], a[184:187], v[168:171]\n"
"ds_read_b64_tr_b16 a[24:25], v3 offset:192\n"
"v_mfma_f32_16x16x32_bf16 v[72:75], a[72:75], a[184:187], v[72:75]\n"
"ds_read_b64_tr_b16 a[26:27], v3 offset:3776\n"
"v_mfma_f32_16x16x32_bf16 v[184:187], a[100:103], a[184:187], v[184:187]\n"
"v_mfma_f32_16x16x32_bf16 v[88:91], a[76:79], a[184:187], v[88:91]\n"
"ds_read_b64_tr_b16 a[52:53], v3 offset:8384\n"
"v_mfma_f32_16x16x32_bf16 v[200:203], a[104:107], a[184:187], v[200:203]\n"
"ds_read_b64_tr_b16 a[54:55], v3 offset:11968\n"
"v_mfma_f32_16x16x32_bf16 v[104:107], a[80:83], a[184:187], v[104:107]\n"
"v_mfma_f32_16x16x32_bf16 v[216:219], a[108:111], a[184:187], v[216:219]\n"
"v_mfma_f32_16x16x32_bf16 v[120:123], a[84:87], a[184:187], v[120:123]\n"
"v_mfma_f32_16x16x32_bf16 v[232:235], a[112:115], a[184:187], v[232:235]\n"
"v_mfma_f32_16x16x32_bf16 v[136:139], a[88:91], a[184:187], v[136:139]\n"
"v_mfma_f32_16x16x32_bf16 v[248:251], a[116:119], a[184:187], v[248:251]\n"
"buffer_load_dwordx4 a[184:187], v6, s[28:31], s91 offen offset:2048 sc1\n"
"v_mfma_f32_16x16x32_bf16 v[252:255], a[116:119], a[188:191], v[252:255]\n"
"v_mfma_f32_16x16x32_bf16 v[140:143], a[88:91], a[188:191], v[140:143]\n"
"v_mfma_f32_16x16x32_bf16 v[236:239], a[112:115], a[188:191], v[236:239]\n"
"v_mfma_f32_16x16x32_bf16 v[124:127], a[84:87], a[188:191], v[124:127]\n"
"v_mfma_f32_16x16x32_bf16 v[220:223], a[108:111], a[188:191], v[220:223]\n"
"v_mfma_f32_16x16x32_bf16 v[108:111], a[80:83], a[188:191], v[108:111]\n"
"v_mfma_f32_16x16x32_bf16 v[204:207], a[104:107], a[188:191], v[204:207]\n"
"v_mfma_f32_16x16x32_bf16 v[92:95], a[76:79], a[188:191], v[92:95]\n"
"v_mfma_f32_16x16x32_bf16 v[188:191], a[100:103], a[188:191], v[188:191]\n"
"v_mfma_f32_16x16x32_bf16 v[76:79], a[72:75], a[188:191], v[76:79]\n"
"v_mfma_f32_16x16x32_bf16 v[172:175], a[96:99], a[188:191], v[172:175]\n"
"v_mov_b32 v20, s80\n"
"v_mov_b32 v21, s81\n"
"v_mfma_f32_16x16x32_bf16 v[60:63], a[68:71], a[188:191], v[60:63]\n"
"v_mfma_f32_16x16x32_bf16 v[156:159], a[92:95], a[188:191], v[156:159]\n"
"s_waitcnt vmcnt(22) lgkmcnt(0)\n"
"s_barrier\n"
"v_mfma_f32_16x16x32_bf16 v[44:47], a[64:67], a[188:191], v[44:47]\n"
"s_waitcnt vmcnt(16)\n"
"s_mov_b64 exec, 1\n"
"buffer_store_dwordx2 v[20:21], off, s[16:19], s57 sc1\n"
"s_mov_b64 exec, -1\n"
"v_mfma_f32_16x16x32_bf16 a[220:223], a[124:127], a[188:191], a[220:223]\n"
"v_mfma_f32_16x16x32_bf16 a[204:207], a[120:123], a[188:191], a[204:207]\n"
"buffer_load_dwordx4 a[188:191], v6, s[28:31], s91 offen offset:3072 sc1\n"
".p2align 6\n"
"Lg1_%=:\n"
"v_mfma_f32_16x16x32_bf16 a[192:195], a[56:59], a[128:131], a[192:195]\n"
"ds_read_b64_tr_b16 a[120:121], v4 offset:23552\n"
"v_mfma_f32_16x16x32_bf16 a[208:211], a[60:63], a[128:131], a[208:211]\n"
"s_add_u32 m0, s86, s55\n"
"ds_read_b64_tr_b16 a[122:123], v4 offset:31744\n"
"v_mfma_f32_16x16x32_bf16 v[32:35], a[0:3], a[128:131], v[32:35]\n"
"s_add_u32 s91, s91, 16384\n"
"ds_read_b64_tr_b16 a[124:125], v5 offset:23552\n"
"v_mfma_f32_16x16x32_bf16 v[144:147], a[28:31], a[128:131], v[144:147]\n"
"buffer_load_dwordx4 v10, s[20:23], s82 offen offset:0 lds\n"
"v_mfma_f32_16x16x32_bf16 v[48:51], a[4:7], a[128:131], v[48:51]\n"
"ds_read_b64_tr_b16 a[126:127], v5 offset:31744\n"
"v_mfma_f32_16x16x32_bf16 v[160:163], a[32:35], a[128:131], v[160:163]\n"
"ds_read_b64_tr_b16 a[64:65], v3 offset:16384\n"
"v_mfma_f32_16x16x32_bf16 v[64:67], a[8:11], a[128:131], v[64:67]\n"
"ds_read_b64_tr_b16 a[66:67], v3 offset:19968\n"
"v_mfma_f32_16x16x32_bf16 v[176:179], a[36:39], a[128:131], v[176:179]\n"
"v_mfma_f32_16x16x32_bf16 v[80:83], a[12:15], a[128:131], v[80:83]\n"
"ds_read_b64_tr_b16 a[92:93], v3 offset:24576\n"
"v_mfma_f32_16x16x32_bf16 v[192:195], a[40:43], a[128:131], v[192:195]\n"
"ds_read_b64_tr_b16 a[94:95], v3 offset:28160\n"
"v_mfma_f32_16x16x32_bf16 v[96:99], a[16:19], a[128:131], v[96:99]\n"
"ds_read_b64_tr_b16 a[68:69], v3 offset:16416\n"
"v_mfma_f32_16x16x32_bf16 v[208:211], a[44:47], a[128:131], v[208:211]\n"
"buffer_load_dwordx4 v11, s[20:23], s82 offen offset:1024 lds\n"
"v_mfma_f32_16x16x32_bf16 v[112:115], a[20:23], a[128:131], v[112:115]\n"
"ds_read_b64_tr_b16 a[70:71], v3 offset:20000\n"
"v_mfma_f32_16x16x32_bf16 v[224:227], a[48:51], a[128:131], v[224:227]\n"
"ds_read_b64_tr_b16 a[96:97], v3 offset:24608\n"
"v_mfma_f32_16x16x32_bf16 v[128:131], a[24:27], a[128:131], v[128:131]\n"
"ds_read_b64_tr_b16 a[98:99], v3 offset:28192\n"
"v_mfma_f32_16x16x32_bf16 v[240:243], a[52:55], a[128:131], v[240:243]\n"
"buffer_load_dwordx4 a[128:131], v6, s[28:31], s91 offen offset:0 sc1\n"
"v_mfma_f32_16x16x32_bf16 v[244:247], a[52:55], a[132:135], v[244:247]\n"
"ds_read_b64_tr_b16 a[72:73], v3 offset:16448\n"
"v_mfma_f32_16x16x32_bf16 v[132:135], a[24:27], a[132:135], v[132:135]\n"
"ds_read_b64_tr_b16 a[74:75], v3 offset:20032\n"
"v_mfma_f32_16x16x32_bf16 v[228:231], a[48:51], a[132:135], v[228:231]\n"
"ds_read_b64_tr_b16 a[100:101], v3 offset:24640\n"
"v_mfma_f32_16x16x32_bf16 v[116:119], a[20:23], a[132:135], v[116:119]\n"
"buffer_load_dwordx4 v12, s[20:23], s82 offen offset:2048 lds\n"
"v_mfma_f32_16x16x32_bf16 v[212:215], a[44:47], a[132:135], v[212:215]\n"
"ds_read_b64_tr_b16 a[102:103], v3 offset:28224\n"
"v_mfma_f32_16x16x32_bf16 v[100:103], a[16:19], a[132:135], v[100:103]\n"
"ds_read_b64_tr_b16 a[76:77], v3 offset:16480\n"
"v_mfma_f32_16x16x32_bf16 v[196:199], a[40:43], a[132:135], v[196:199]\n"
"ds_read_b64_tr_b16 a[78:79], v3 offset:20064\n"
"v_mfma_f32_16x16x32_bf16 v[84:87], a[12:15], a[132:135], v[84:87]\n"
"v_mfma_f32_16x16x32_bf16 v[180:183], a[36:39], a[132:135], v[180:183]\n"
"ds_read_b64_tr_b16 a[104:105], v3 offset:24672\n"
"v_mfma_f32_16x16x32_bf16 v[68:71], a[8:11], a[132:135], v[68:71]\n"
"ds_read_b64_tr_b16 a[106:107], v3 offset:28256\n"
"v_mfma_f32_16x16x32_bf16 v[164:167], a[32:35], a[132:135], v[164:167]\n"
"ds_read_b64_tr_b16 a[80:81], v3 offset:16512\n"
"v_mfma_f32_16x16x32_bf16 v[52:55], a[4:7], a[132:135], v[52:55]\n"
"buffer_load_dwordx4 v13, s[24:27], s83 offen offset:3072 lds\n"
"v_mfma_f32_16x16x32_bf16 v[148:151], a[28:31], a[132:135], v[148:151]\n"
"ds_read_b64_tr_b16 a[82:83], v3 offset:20096\n"
"v_mfma_f32_16x16x32_bf16 v[36:39], a[0:3], a[132:135], v[36:39]\n"
"ds_read_b64_tr_b16 a[108:109], v3 offset:24704\n"
"v_mfma_f32_16x16x32_bf16 a[212:215], a[60:63], a[132:135], a[212:215]\n"
"ds_read_b64_tr_b16 a[110:111], v3 offset:28288\n"
"v_mfma_f32_16x16x32_bf16 a[196:199], a[56:59], a[132:135], a[196:199]\n"
"buffer_load_dwordx4 a[132:135], v6, s[28:31], s91 offen offset:1024 sc1\n"
"v_mfma_f32_16x16x32_bf16 a[200:203], a[56:59], a[136:139], a[200:203]\n"
"ds_read_b64_tr_b16 a[84:85], v3 offset:16544\n"
"v_mfma_f32_16x16x32_bf16 a[216:219], a[60:63], a[136:139], a[216:219]\n"
"ds_read_b64_tr_b16 a[86:87], v3 offset:20128\n"
"v_mfma_f32_16x16x32_bf16 v[40:43], a[0:3], a[136:139], v[40:43]\n"
"ds_read_b64_tr_b16 a[112:113], v3 offset:24736\n"
"v_mfma_f32_16x16x32_bf16 v[152:155], a[28:31], a[136:139], v[152:155]\n"
"buffer_load_dword v2, v8, s[8:11], s14 offen\n"
"v_mfma_f32_16x16x32_bf16 v[56:59], a[4:7], a[136:139], v[56:59]\n"
"s_add_u32 s14, s14, s15\n"
"ds_read_b64_tr_b16 a[114:115], v3 offset:28320\n"
"v_mfma_f32_16x16x32_bf16 v[168:171], a[32:35], a[136:139], v[168:171]\n"
"ds_read_b64_tr_b16 a[88:89], v3 offset:16576\n"
"v_mfma_f32_16x16x32_bf16 v[72:75], a[8:11], a[136:139], v[72:75]\n"
"ds_read_b64_tr_b16 a[90:91], v3 offset:20160\n"
"v_mfma_f32_16x16x32_bf16 v[184:187], a[36:39], a[136:139], v[184:187]\n"
"v_mfma_f32_16x16x32_bf16 v[88:91], a[12:15], a[136:139], v[88:91]\n"
"ds_read_b64_tr_b16 a[116:117], v3 offset:24768\n"
"v_mfma_f32_16x16x32_bf16 v[200:203], a[40:43], a[136:139], v[200:203]\n"
"ds_read_b64_tr_b16 a[118:119], v3 offset:28352\n"
"v_mfma_f32_16x16x32_bf16 v[104:107], a[16:19], a[136:139], v[104:107]\n"
"v_mfma_f32_16x16x32_bf16 v[216:219], a[44:47], a[136:139], v[216:219]\n"
"v_mfma_f32_16x16x32_bf16 v[120:123], a[20:23], a[136:139], v[120:123]\n"
"s_add_u32 s86, s86, 16384\n"
"v_mfma_f32_16x16x32_bf16 v[232:235], a[48:51], a[136:139], v[232:235]\n"
"s_and_b32 s86, s86, 131071\n"
"v_mfma_f32_16x16x32_bf16 v[136:139], a[24:27], a[136:139], v[136:139]\n"
"v_mfma_f32_16x16x32_bf16 v[248:251], a[52:55], a[136:139], v[248:251]\n"
"buffer_load_dwordx4 a[136:139], v6, s[28:31], s91 offen offset:2048 sc1\n"
"v_mfma_f32_16x16x32_bf16 v[252:255], a[52:55], a[140:143], v[252:255]\n"
"v_mfma_f32_16x16x32_bf16 v[140:143], a[24:27], a[140:143], v[140:143]\n"
"v_mfma_f32_16x16x32_bf16 v[236:239], a[48:51], a[140:143], v[236:239]\n"
"v_mfma_f32_16x16x32_bf16 v[124:127], a[20:23], a[140:143], v[124:127]\n"
"v_mfma_f32_16x16x32_bf16 v[220:223], a[44:47], a[140:143], v[220:223]\n"
"v_mfma_f32_16x16x32_bf16 v[108:111], a[16:19], a[140:143], v[108:111]\n"
"v_mfma_f32_16x16x32_bf16 v[204:207], a[40:43], a[140:143], v[204:207]\n"
"v_mfma_f32_16x16x32_bf16 v[92:95], a[12:15], a[140:143], v[92:95]\n"
"v_mfma_f32_16x16x32_bf16 v[188:191], a[36:39], a[140:143], v[188:191]\n"
"s_add_u32 s82, s82, s53\n"
"v_mfma_f32_16x16x32_bf16 v[76:79], a[8:11], a[140:143], v[76:79]\n"
"s_add_u32 s83, s83, s54\n"
"v_mfma_f32_16x16x32_bf16 v[172:175], a[32:35], a[140:143], v[172:175]\n"
"v_mfma_f32_16x16x32_bf16 v[60:63], a[4:7], a[140:143], v[60:63]\n"
"v_mfma_f32_16x16x32_bf16 v[156:159], a[28:31], a[140:143], v[156:159]\n"
"s_waitcnt vmcnt(10) lgkmcnt(0)\n"
"s_barrier\n"
"v_mfma_f32_16x16x32_bf16 v[44:47], a[0:3], a[140:143], v[44:47]\n"
"v_mfma_f32_16x16x32_bf16 a[220:223], a[60:63], a[140:143], a[220:223]\n"
"v_mfma_f32_16x16x32_bf16 a[204:207], a[56:59], a[140:143], a[204:207]\n"
"buffer_load_dwordx4 a[140:143], v6, s[28:31], s91 offen offset:3072 sc1\n"
"v_mfma_f32_16x16x32_bf16 a[192:195], a[120:123], a[144:147], a[192:195]\n"
"ds_read_b64_tr_b16 a[56:57], v4 offset:39936\n"
"v_mfma_f32_16x16x32_bf16 a[208:211], a[124:127], a[144:147], a[208:211]\n"
"s_add_u32 m0, s86, s55\n"
"ds_read_b64_tr_b16 a[58:59], v4 offset:48128\n"
"v_mfma_f32_16x16x32_bf16 v[32:35], a[64:67], a[144:147], v[32:35]\n"
"s_add_u32 s91, s91, 16384\n"
"ds_read_b64_tr_b16 a[60:61], v5 offset:39936\n"
"v_mfma_f32_16x16x32_bf16 v[144:147], a[92:95], a[144:147], v[144:147]\n"
"buffer_load_dwordx4 v10, s[20:23], s82 offen offset:0 lds\n"
"v_mfma_f32_16x16x32_bf16 v[48:51], a[68:71], a[144:147], v[48:51]\n"
"ds_read_b64_tr_b16 a[62:63], v5 offset:48128\n"
"v_mfma_f32_16x16x32_bf16 v[160:163], a[96:99], a[144:147], v[160:163]\n"
"ds_read_b64_tr_b16 a[0:1], v3 offset:32768\n"
"v_mfma_f32_16x16x32_bf16 v[64:67], a[72:75], a[144:147], v[64:67]\n"
"ds_read_b64_tr_b16 a[2:3], v3 offset:36352\n"
"v_mfma_f32_16x16x32_bf16 v[176:179], a[100:103], a[144:147], v[176:179]\n"
"v_mfma_f32_16x16x32_bf16 v[80:83], a[76:79], a[144:147], v[80:83]\n"
"ds_read_b64_tr_b16 a[28:29], v3 offset:40960\n"
"v_mfma_f32_16x16x32_bf16 v[192:195], a[104:107], a[144:147], v[192:195]\n"
"ds_read_b64_tr_b16 a[30:31], v3 offset:44544\n"
"v_mfma_f32_16x16x32_bf16 v[96:99], a[80:83], a[144:147], v[96:99]\n"
"ds_read_b64_tr_b16 a[4:5], v3 offset:32800\n"
"v_mfma_f32_16x16x32_bf16 v[208:211], a[108:111], a[144:147], v[208:211]\n"
"buffer_load_dwordx4 v11, s[20:23], s82 offen offset:1024 lds\n"
"v_mfma_f32_16x16x32_bf16 v[112:115], a[84:87], a[144:147], v[112:115]\n"
"ds_read_b64_tr_b16 a[6:7], v3 offset:36384\n"
"v_mfma_f32_16x16x32_bf16 v[224:227], a[112:115], a[144:147], v[224:227]\n"
"ds_read_b64_tr_b16 a[32:33], v3 offset:40992\n"
"v_mfma_f32_16x16x32_bf16 v[128:131], a[88:91], a[144:147], v[128:131]\n"
"ds_read_b64_tr_b16 a[34:35], v3 offset:44576\n"
"v_mfma_f32_16x16x32_bf16 v[240:243], a[116:119], a[144:147], v[240:243]\n"
"buffer_load_dwordx4 a[144:147], v6, s[28:31], s91 offen offset:0 sc1\n"
"v_mfma_f32_16x16x32_bf16 v[244:247], a[116:119], a[148:151], v[244:247]\n"
"ds_read_b64_tr_b16 a[8:9], v3 offset:32832\n"
"v_mfma_f32_16x16x32_bf16 v[132:135], a[88:91], a[148:151], v[132:135]\n"
"ds_read_b64_tr_b16 a[10:11], v3 offset:36416\n"
"v_mfma_f32_16x16x32_bf16 v[228:231], a[112:115], a[148:151], v[228:231]\n"
"ds_read_b64_tr_b16 a[36:37], v3 offset:41024\n"
"v_mfma_f32_16x16x32_bf16 v[116:119], a[84:87], a[148:151], v[116:119]\n"
"buffer_load_dwordx4 v12, s[20:23], s82 offen offset:2048 lds\n"
"v_mfma_f32_16x16x32_bf16 v[212:215], a[108:111], a[148:151], v[212:215]\n"
"ds_read_b64_tr_b16 a[38:39], v3 offset:44608\n"
"v_mfma_f32_16x16x32_bf16 v[100:103], a[80:83], a[148:151], v[100:103]\n"
"ds_read_b64_tr_b16 a[12:13], v3 offset:32864\n"
"v_mfma_f32_16x16x32_bf16 v[196:199], a[104:107], a[148:151], v[196:199]\n"
"ds_read_b64_tr_b16 a[14:15], v3 offset:36448\n"
"v_mfma_f32_16x16x32_bf16 v[84:87], a[76:79], a[148:151], v[84:87]\n"
"v_mfma_f32_16x16x32_bf16 v[180:183], a[100:103], a[148:151], v[180:183]\n"
"ds_read_b64_tr_b16 a[40:41], v3 offset:41056\n"
"v_mfma_f32_16x16x32_bf16 v[68:71], a[72:75], a[148:151], v[68:71]\n"
"ds_read_b64_tr_b16 a[42:43], v3 offset:44640\n"
"v_mfma_f32_16x16x32_bf16 v[164:167], a[96:99], a[148:151], v[164:167]\n"
"ds_read_b64_tr_b16 a[16:17], v3 offset:32896\n"
"v_mfma_f32_16x16x32_bf16 v[52:55], a[68:71], a[148:151], v[52:55]\n"
"buffer_load_dwordx4 v13, s[24:27], s83 offen offset:3072 lds\n"
"v_mfma_f32_16x16x32_bf16 v[148:151], a[92:95], a[148:151], v[148:151]\n"
"ds_read_b64_tr_b16 a[18:19], v3 offset:36480\n"
"v_mfma_f32_16x16x32_bf16 v[36:39], a[64:67], a[148:151], v[36:39]\n"
"ds_read_b64_tr_b16 a[44:45], v3 offset:41088\n"
"v_mfma_f32_16x16x32_bf16 a[212:215], a[124:127], a[148:151], a[212:215]\n"
"ds_read_b64_tr_b16 a[46:47], v3 offset:44672\n"
"v_mfma_f32_16x16x32_bf16 a[196:199], a[120:123], a[148:151], a[196:199]\n"
"buffer_load_dwordx4 a[148:151], v6, s[28:31], s91 offen offset:1024 sc1\n"
"v_mfma_f32_16x16x32_bf16 a[200:203], a[120:123], a[152:155], a[200:203]\n"
"ds_read_b64_tr_b16 a[20:21], v3 offset:32928\n"
"v_mfma_f32_16x16x32_bf16 a[216:219], a[124:127], a[152:155], a[216:219]\n"
"ds_read_b64_tr_b16 a[22:23], v3 offset:36512\n"
"v_mfma_f32_16x16x32_bf16 v[40:43], a[64:67], a[152:155], v[40:43]\n"
"ds_read_b64_tr_b16 a[48:49], v3 offset:41120\n"
"v_mfma_f32_16x16x32_bf16 v[152:155], a[92:95], a[152:155], v[152:155]\n"
"v_mfma_f32_16x16x32_bf16 v[56:59], a[68:71], a[152:155], v[56:59]\n"
"ds_read_b64_tr_b16 a[50:51], v3 offset:44704\n"
"v_mfma_f32_16x16x32_bf16 v[168:171], a[96:99], a[152:155], v[168:171]\n"
"ds_read_b64_tr_b16 a[24:25], v3 offset:32960\n"
"v_mfma_f32_16x16x32_bf16 v[72:75], a[72:75], a[152:155], v[72:75]\n"
"ds_read_b64_tr_b16 a[26:27], v3 offset:36544\n"
"v_mfma_f32_16x16x32_bf16 v[184:187], a[100:103], a[152:155], v[184:187]\n"
"v_mfma_f32_16x16x32_bf16 v[88:91], a[76:79], a[152:155], v[88:91]\n"
"ds_read_b64_tr_b16 a[52:53], v3 offset:41152\n"
"v_mfma_f32_16x16x32_bf16 v[200:203], a[104:107], a[152:155], v[200:203]\n"
"ds_read_b64_tr_b16 a[54:55], v3 offset:44736\n"
"v_mfma_f32_16x16x32_bf16 v[104:107], a[80:83], a[152:155], v[104:107]\n"
"v_mfma_f32_16x16x32_bf16 v[216:219], a[108:111], a[152:155], v[216:219]\n"
"v_mfma_f32_16x16x32_bf16 v[120:123], a[84:87], a[152:155], v[120:123]\n"
"s_add_u32 s86, s86, 16384\n"
"v_mfma_f32_16x16x32_bf16 v[232:235], a[112:115], a[152:155], v[232:235]\n"
"s_and_b32 s86, s86, 131071\n"
"v_mfma_f32_16x16x32_bf16 v[136:139], a[88:91], a[152:155], v[136:139]\n"
"v_mfma_f32_16x16x32_bf16 v[248:251], a[116:119], a[152:155], v[248:251]\n"
"buffer_load_dwordx4 a[152:155], v6, s[28:31], s91 offen offset:2048 sc1\n"
"v_mfma_f32_16x16x32_bf16 v[252:255], a[116:119], a[156:159], v[252:255]\n"
"v_mfma_f32_16x16x32_bf16 v[140:143], a[88:91], a[156:159], v[140:143]\n"
"v_mfma_f32_16x16x32_bf16 v[236:239], a[112:115], a[156:159], v[236:239]\n"
"v_mfma_f32_16x16x32_bf16 v[124:127], a[84:87], a[156:159], v[124:127]\n"
"v_mfma_f32_16x16x32_bf16 v[220:223], a[108:111], a[156:159], v[220:223]\n"
"v_mfma_f32_16x16x32_bf16 v[108:111], a[80:83], a[156:159], v[108:111]\n"
"v_mfma_f32_16x16x32_bf16 v[204:207], a[104:107], a[156:159], v[204:207]\n"
"v_mfma_f32_16x16x32_bf16 v[92:95], a[76:79], a[156:159], v[92:95]\n"
"v_mfma_f32_16x16x32_bf16 v[188:191], a[100:103], a[156:159], v[188:191]\n"
"s_add_u32 s82, s82, s53\n"
"v_mfma_f32_16x16x32_bf16 v[76:79], a[72:75], a[156:159], v[76:79]\n"
"s_add_u32 s83, s83, s54\n"
"v_mfma_f32_16x16x32_bf16 v[172:175], a[96:99], a[156:159], v[172:175]\n"
"v_mfma_f32_16x16x32_bf16 v[60:63], a[68:71], a[156:159], v[60:63]\n"
"v_mfma_f32_16x16x32_bf16 v[156:159], a[92:95], a[156:159], v[156:159]\n"
"s_waitcnt lgkmcnt(0)\n"
"v_mfma_f32_16x16x32_bf16 v[44:47], a[64:67], a[156:159], v[44:47]\n"
"v_mfma_f32_16x16x32_bf16 a[220:223], a[124:127], a[156:159], a[220:223]\n"
"v_mfma_f32_16x16x32_bf16 a[204:207], a[120:123], a[156:159], a[204:207]\n"
"buffer_load_dwordx4 a[156:159], v6, s[28:31], s91 offen offset:3072 sc1\n"
"v_mfma_f32_16x16x32_bf16 a[192:195], a[56:59], a[160:163], a[192:195]\n"
"ds_read_b64_tr_b16 a[120:121], v4 offset:56320\n"
"v_mfma_f32_16x16x32_bf16 a[208:211], a[60:63], a[160:163], a[208:211]\n"
"s_add_u32 m0, s86, s55\n"
"ds_read_b64_tr_b16 a[122:123], v4 offset:64512\n"
"v_mfma_f32_16x16x32_bf16 v[32:35], a[0:3], a[160:163], v[32:35]\n"
"s_add_u32 s91, s91, 16384\n"
"ds_read_b64_tr_b16 a[124:125], v5 offset:56320\n"
"v_mfma_f32_16x16x32_bf16 v[144:147], a[28:31], a[160:163], v[144:147]\n"
"buffer_load_dwordx4 v10, s[20:23], s82 offen offset:0 lds\n"
"v_mfma_f32_16x16x32_bf16 v[48:51], a[4:7], a[160:163], v[48:51]\n"
"ds_read_b64_tr_b16 a[126:127], v5 offset:64512\n"
"v_mfma_f32_16x16x32_bf16 v[160:163], a[32:35], a[160:163], v[160:163]\n"
"ds_read_b64_tr_b16 a[64:65], v3 offset:49152\n"
"v_mfma_f32_16x16x32_bf16 v[64:67], a[8:11], a[160:163], v[64:67]\n"
"ds_read_b64_tr_b16 a[66:67], v3 offset:52736\n"
"v_mfma_f32_16x16x32_bf16 v[176:179], a[36:39], a[160:163], v[176:179]\n"
"v_mfma_f32_16x16x32_bf16 v[80:83], a[12:15], a[160:163], v[80:83]\n"
"ds_read_b64_tr_b16 a[92:93], v3 offset:57344\n"
"v_mfma_f32_16x16x32_bf16 v[192:195], a[40:43], a[160:163], v[192:195]\n"
"ds_read_b64_tr_b16 a[94:95], v3 offset:60928\n"
"v_mfma_f32_16x16x32_bf16 v[96:99], a[16:19], a[160:163], v[96:99]\n"
"ds_read_b64_tr_b16 a[68:69], v3 offset:49184\n"
"v_mfma_f32_16x16x32_bf16 v[208:211], a[44:47], a[160:163], v[208:211]\n"
"buffer_load_dwordx4 v11, s[20:23], s82 offen offset:1024 lds\n"
"v_mfma_f32_16x16x32_bf16 v[112:115], a[20:23], a[160:163], v[112:115]\n"
"ds_read_b64_tr_b16 a[70:71], v3 offset:52768\n"
"v_mfma_f32_16x16x32_bf16 v[224:227], a[48:51], a[160:163], v[224:227]\n"
"ds_read_b64_tr_b16 a[96:97], v3 offset:57376\n"
"v_mfma_f32_16x16x32_bf16 v[128:131], a[24:27], a[160:163], v[128:131]\n"
"ds_read_b64_tr_b16 a[98:99], v3 offset:60960\n"
"v_mfma_f32_16x16x32_bf16 v[240:243], a[52:55], a[160:163], v[240:243]\n"
"buffer_load_dwordx4 a[160:163], v6, s[28:31], s91 offen offset:0 sc1\n"
"v_mfma_f32_16x16x32_bf16 v[244:247], a[52:55], a[164:167], v[244:247]\n"
"ds_read_b64_tr_b16 a[72:73], v3 offset:49216\n"
"v_mfma_f32_16x16x32_bf16 v[132:135], a[24:27], a[164:167], v[132:135]\n"
"ds_read_b64_tr_b16 a[74:75], v3 offset:52800\n"
"v_mfma_f32_16x16x32_bf16 v[228:231], a[48:51], a[164:167], v[228:231]\n"
"ds_read_b64_tr_b16 a[100:101], v3 offset:57408\n"
"v_mfma_f32_16x16x32_bf16 v[116:119], a[20:23], a[164:167], v[116:119]\n"
"buffer_load_dwordx4 v12, s[20:23], s82 offen offset:2048 lds\n"
"v_mfma_f32_16x16x32_bf16 v[212:215], a[44:47], a[164:167], v[212:215]\n"
"ds_read_b64_tr_b16 a[102:103], v3 offset:60992\n"
"v_mfma_f32_16x16x32_bf16 v[100:103], a[16:19], a[164:167], v[100:103]\n"
"ds_read_b64_tr_b16 a[76:77], v3 offset:49248\n"
"v_mfma_f32_16x16x32_bf16 v[196:199], a[40:43], a[164:167], v[196:199]\n"
"ds_read_b64_tr_b16 a[78:79], v3 offset:52832\n"
"v_mfma_f32_16x16x32_bf16 v[84:87], a[12:15], a[164:167], v[84:87]\n"
"v_mfma_f32_16x16x32_bf16 v[180:183], a[36:39], a[164:167], v[180:183]\n"
"ds_read_b64_tr_b16 a[104:105], v3 offset:57440\n"
"v_mfma_f32_16x16x32_bf16 v[68:71], a[8:11], a[164:167], v[68:71]\n"
"ds_read_b64_tr_b16 a[106:107], v3 offset:61024\n"
"v_mfma_f32_16x16x32_bf16 v[164:167], a[32:35], a[164:167], v[164:167]\n"
"ds_read_b64_tr_b16 a[80:81], v3 offset:49280\n"
"v_mfma_f32_16x16x32_bf16 v[52:55], a[4:7], a[164:167], v[52:55]\n"
"buffer_load_dwordx4 v13, s[24:27], s83 offen offset:3072 lds\n"
"v_mfma_f32_16x16x32_bf16 v[148:151], a[28:31], a[164:167], v[148:151]\n"
"ds_read_b64_tr_b16 a[82:83], v3 offset:52864\n"
"v_mfma_f32_16x16x32_bf16 v[36:39], a[0:3], a[164:167], v[36:39]\n"
"ds_read_b64_tr_b16 a[108:109], v3 offset:57472\n"
"v_mfma_f32_16x16x32_bf16 a[212:215], a[60:63], a[164:167], a[212:215]\n"
"ds_read_b64_tr_b16 a[110:111], v3 offset:61056\n"
"v_mfma_f32_16x16x32_bf16 a[196:199], a[56:59], a[164:167], a[196:199]\n"
"buffer_load_dwordx4 a[164:167], v6, s[28:31], s91 offen offset:1024 sc1\n"
"v_mfma_f32_16x16x32_bf16 a[200:203], a[56:59], a[168:171], a[200:203]\n"
"ds_read_b64_tr_b16 a[84:85], v3 offset:49312\n"
"v_mfma_f32_16x16x32_bf16 a[216:219], a[60:63], a[168:171], a[216:219]\n"
"ds_read_b64_tr_b16 a[86:87], v3 offset:52896\n"
"v_mfma_f32_16x16x32_bf16 v[40:43], a[0:3], a[168:171], v[40:43]\n"
"ds_read_b64_tr_b16 a[112:113], v3 offset:57504\n"
"v_mfma_f32_16x16x32_bf16 v[152:155], a[28:31], a[168:171], v[152:155]\n"
"v_mfma_f32_16x16x32_bf16 v[56:59], a[4:7], a[168:171], v[56:59]\n"
"ds_read_b64_tr_b16 a[114:115], v3 offset:61088\n"
"v_mfma_f32_16x16x32_bf16 v[168:171], a[32:35], a[168:171], v[168:171]\n"
"ds_read_b64_tr_b16 a[88:89], v3 offset:49344\n"
"v_mfma_f32_16x16x32_bf16 v[72:75], a[8:11], a[168:171], v[72:75]\n"
"ds_read_b64_tr_b16 a[90:91], v3 offset:52928\n"
"v_mfma_f32_16x16x32_bf16 v[184:187], a[36:39], a[168:171], v[184:187]\n"
"v_mfma_f32_16x16x32_bf16 v[88:91], a[12:15], a[168:171], v[88:91]\n"
"ds_read_b64_tr_b16 a[116:117], v3 offset:57536\n"
"v_mfma_f32_16x16x32_bf16 v[200:203], a[40:43], a[168:171], v[200:203]\n"
"ds_read_b64_tr_b16 a[118:119], v3 offset:61120\n"
"v_mfma_f32_16x16x32_bf16 v[104:107], a[16:19], a[168:171], v[104:107]\n"
"v_mfma_f32_16x16x32_bf16 v[216:219], a[44:47], a[168:171], v[216:219]\n"
"v_mfma_f32_16x16x32_bf16 v[120:123], a[20:23], a[168:171], v[120:123]\n"
"s_add_u32 s86, s86, 16384\n"
"v_mfma_f32_16x16x32_bf16 v[232:235], a[48:51], a[168:171], v[232:235]\n"
"s_and_b32 s86, s86, 131071\n"
"v_mfma_f32_16x16x32_bf16 v[136:139], a[24:27], a[168:171], v[136:139]\n"
"v_mfma_f32_16x16x32_bf16 v[248:251], a[52:55], a[168:171], v[248:251]\n"
"buffer_load_dwordx4 a[168:171], v6, s[28:31], s91 offen offset:2048 sc1\n"
"v_mfma_f32_16x16x32_bf16 v[252:255], a[52:55], a[172:175], v[252:255]\n"
"v_xor_b32 v3, 0x10000, v3\n"
"v_mfma_f32_16x16x32_bf16 v[140:143], a[24:27], a[172:175], v[140:143]\n"
"v_xor_b32 v4, 0x10000, v4\n"
"v_mfma_f32_16x16x32_bf16 v[236:239], a[48:51], a[172:175], v[236:239]\n"
"v_xor_b32 v5, 0x10000, v5\n"
"v_mfma_f32_16x16x32_bf16 v[124:127], a[20:23], a[172:175], v[124:127]\n"
"v_mfma_f32_16x16x32_bf16 v[220:223], a[44:47], a[172:175], v[220:223]\n"
"v_mfma_f32_16x16x32_bf16 v[108:111], a[16:19], a[172:175], v[108:111]\n"
"v_mfma_f32_16x16x32_bf16 v[204:207], a[40:43], a[172:175], v[204:207]\n"
"v_mfma_f32_16x16x32_bf16 v[92:95], a[12:15], a[172:175], v[92:95]\n"
"v_mfma_f32_16x16x32_bf16 v[188:191], a[36:39], a[172:175], v[188:191]\n"
"s_add_u32 s82, s82, s53\n"
"v_mfma_f32_16x16x32_bf16 v[76:79], a[8:11], a[172:175], v[76:79]\n"
"s_add_u32 s83, s83, s54\n"
"v_mfma_f32_16x16x32_bf16 v[172:175], a[32:35], a[172:175], v[172:175]\n"
"v_mfma_f32_16x16x32_bf16 v[60:63], a[4:7], a[172:175], v[60:63]\n"
"v_mfma_f32_16x16x32_bf16 v[156:159], a[28:31], a[172:175], v[156:159]\n"
"s_waitcnt vmcnt(10) lgkmcnt(0)\n"
"s_barrier\n"
"v_mfma_f32_16x16x32_bf16 v[44:47], a[0:3], a[172:175], v[44:47]\n"
"v_mfma_f32_16x16x32_bf16 a[220:223], a[60:63], a[172:175], a[220:223]\n"
"v_mfma_f32_16x16x32_bf16 a[204:207], a[56:59], a[172:175], a[204:207]\n"
"buffer_load_dwordx4 a[172:175], v6, s[28:31], s91 offen offset:3072 sc1\n"
"v_mfma_f32_16x16x32_bf16 a[192:195], a[120:123], a[176:179], a[192:195]\n"
"ds_read_b64_tr_b16 a[56:57], v4 offset:7168\n"
"v_mfma_f32_16x16x32_bf16 a[208:211], a[124:127], a[176:179], a[208:211]\n"
"s_add_u32 m0, s86, s55\n"
"ds_read_b64_tr_b16 a[58:59], v4 offset:15360\n"
"v_mfma_f32_16x16x32_bf16 v[32:35], a[64:67], a[176:179], v[32:35]\n"
"s_add_u32 s91, s91, 16384\n"
"ds_read_b64_tr_b16 a[60:61], v5 offset:7168\n"
"v_mfma_f32_16x16x32_bf16 v[144:147], a[92:95], a[176:179], v[144:147]\n"
"buffer_load_dwordx4 v10, s[20:23], s82 offen offset:0 lds\n"
"v_mfma_f32_16x16x32_bf16 v[48:51], a[68:71], a[176:179], v[48:51]\n"
"ds_read_b64_tr_b16 a[62:63], v5 offset:15360\n"
"v_mfma_f32_16x16x32_bf16 v[160:163], a[96:99], a[176:179], v[160:163]\n"
"ds_read_b64_tr_b16 a[0:1], v3 offset:0\n"
"v_mfma_f32_16x16x32_bf16 v[64:67], a[72:75], a[176:179], v[64:67]\n"
"ds_read_b64_tr_b16 a[2:3], v3 offset:3584\n"
"v_mfma_f32_16x16x32_bf16 v[176:179], a[100:103], a[176:179], v[176:179]\n"
"v_mfma_f32_16x16x32_bf16 v[80:83], a[76:79], a[176:179], v[80:83]\n"
"ds_read_b64_tr_b16 a[28:29], v3 offset:8192\n"
"v_mfma_f32_16x16x32_bf16 v[192:195], a[104:107], a[176:179], v[192:195]\n"
"ds_read_b64_tr_b16 a[30:31], v3 offset:11776\n"
"v_mfma_f32_16x16x32_bf16 v[96:99], a[80:83], a[176:179], v[96:99]\n"
"ds_read_b64_tr_b16 a[4:5], v3 offset:32\n"
"v_mfma_f32_16x16x32_bf16 v[208:211], a[108:111], a[176:179], v[208:211]\n"
"buffer_load_dwordx4 v11, s[20:23], s82 offen offset:1024 lds\n"
"v_mfma_f32_16x16x32_bf16 v[112:115], a[84:87], a[176:179], v[112:115]\n"
"ds_read_b64_tr_b16 a[6:7], v3 offset:3616\n"
"v_mfma_f32_16x16x32_bf16 v[224:227], a[112:115], a[176:179], v[224:227]\n"
"ds_read_b64_tr_b16 a[32:33], v3 offset:8224\n"
"v_mfma_f32_16x16x32_bf16 v[128:131], a[88:91], a[176:179], v[128:131]\n"
"ds_read_b64_tr_b16 a[34:35], v3 offset:11808\n"
"v_mfma_f32_16x16x32_bf16 v[240:243], a[116:119], a[176:179], v[240:243]\n"
"buffer_load_dwordx4 a[176:179], v6, s[28:31], s91 offen offset:0 sc1\n"
"v_mfma_f32_16x16x32_bf16 v[244:247], a[116:119], a[180:183], v[244:247]\n"
"ds_read_b64_tr_b16 a[8:9], v3 offset:64\n"
"v_mfma_f32_16x16x32_bf16 v[132:135], a[88:91], a[180:183], v[132:135]\n"
"ds_read_b64_tr_b16 a[10:11], v3 offset:3648\n"
"v_mfma_f32_16x16x32_bf16 v[228:231], a[112:115], a[180:183], v[228:231]\n"
"ds_read_b64_tr_b16 a[36:37], v3 offset:8256\n"
"v_mfma_f32_16x16x32_bf16 v[116:119], a[84:87], a[180:183], v[116:119]\n"
"buffer_load_dwordx4 v12, s[20:23], s82 offen offset:2048 lds\n"
"v_mfma_f32_16x16x32_bf16 v[212:215], a[108:111], a[180:183], v[212:215]\n"
"ds_read_b64_tr_b16 a[38:39], v3 offset:11840\n"
"v_mfma_f32_16x16x32_bf16 v[100:103], a[80:83], a[180:183], v[100:103]\n"
"ds_read_b64_tr_b16 a[12:13], v3 offset:96\n"
"v_mfma_f32_16x16x32_bf16 v[196:199], a[104:107], a[180:183], v[196:199]\n"
"ds_read_b64_tr_b16 a[14:15], v3 offset:3680\n"
"v_mfma_f32_16x16x32_bf16 v[84:87], a[76:79], a[180:183], v[84:87]\n"
"v_mfma_f32_16x16x32_bf16 v[180:183], a[100:103], a[180:183], v[180:183]\n"
"ds_read_b64_tr_b16 a[40:41], v3 offset:8288\n"
"v_mfma_f32_16x16x32_bf16 v[68:71], a[72:75], a[180:183], v[68:71]\n"
"ds_read_b64_tr_b16 a[42:43], v3 offset:11872\n"
"v_mfma_f32_16x16x32_bf16 v[164:167], a[96:99], a[180:183], v[164:167]\n"
"ds_read_b64_tr_b16 a[16:17], v3 offset:128\n"
"v_mfma_f32_16x16x32_bf16 v[52:55], a[68:71], a[180:183], v[52:55]\n"
"buffer_load_dwordx4 v13, s[24:27], s83 offen offset:3072 lds\n"
"v_mfma_f32_16x16x32_bf16 v[148:151], a[92:95], a[180:183], v[148:151]\n"
"ds_read_b64_tr_b16 a[18:19], v3 offset:3712\n"
"v_mfma_f32_16x16x32_bf16 v[36:39], a[64:67], a[180:183], v[36:39]\n"
"ds_read_b64_tr_b16 a[44:45], v3 offset:8320\n"
"v_mfma_f32_16x16x32_bf16 a[212:215], a[124:127], a[180:183], a[212:215]\n"
"ds_read_b64_tr_b16 a[46:47], v3 offset:11904\n"
"v_mfma_f32_16x16x32_bf16 a[196:199], a[120:123], a[180:183], a[196:199]\n"
"buffer_load_dwordx4 a[180:183], v6, s[28:31], s91 offen offset:1024 sc1\n"
"v_mfma_f32_16x16x32_bf16 a[200:203], a[120:123], a[184:187], a[200:203]\n"
"ds_read_b64_tr_b16 a[20:21], v3 offset:160\n"
"v_mfma_f32_16x16x32_bf16 a[216:219], a[124:127], a[184:187], a[216:219]\n"
"ds_read_b64_tr_b16 a[22:23], v3 offset:3744\n"
"v_mfma_f32_16x16x32_bf16 v[40:43], a[64:67], a[184:187], v[40:43]\n"
"ds_read_b64_tr_b16 a[48:49], v3 offset:8352\n"
"v_mfma_f32_16x16x32_bf16 v[152:155], a[92:95], a[184:187], v[152:155]\n"
"v_mfma_f32_16x16x32_bf16 v[56:59], a[68:71], a[184:187], v[56:59]\n"
"ds_read_b64_tr_b16 a[50:51], v3 offset:11936\n"
"v_mfma_f32_16x16x32_bf16 v[168:171], a[96:99], a[184:187], v[168:171]\n"
"ds_read_b64_tr_b16 a[24:25], v3 offset:192\n"
"v_mfma_f32_16x16x32_bf16 v[72:75], a[72:75], a[184:187], v[72:75]\n"
"ds_read_b64_tr_b16 a[26:27], v3 offset:3776\n"
"v_mfma_f32_16x16x32_bf16 v[184:187], a[100:103], a[184:187], v[184:187]\n"
"v_mfma_f32_16x16x32_bf16 v[88:91], a[76:79], a[184:187], v[88:91]\n"
"ds_read_b64_tr_b16 a[52:53], v3 offset:8384\n"
"v_mfma_f32_16x16x32_bf16 v[200:203], a[104:107], a[184:187], v[200:203]\n"
"ds_read_b64_tr_b16 a[54:55], v3 offset:11968\n"
"v_mfma_f32_16x16x32_bf16 v[104:107], a[80:83], a[184:187], v[104:107]\n"
"v_mfma_f32_16x16x32_bf16 v[216:219], a[108:111], a[184:187], v[216:219]\n"
"v_mfma_f32_16x16x32_bf16 v[120:123], a[84:87], a[184:187], v[120:123]\n"
"s_add_u32 s86, s86, 16384\n"
"v_mfma_f32_16x16x32_bf16 v[232:235], a[112:115], a[184:187], v[232:235]\n"
"s_and_b32 s86, s86, 131071\n"
"v_mfma_f32_16x16x32_bf16 v[136:139], a[88:91], a[184:187], v[136:139]\n"
"v_mfma_f32_16x16x32_bf16 v[248:251], a[116:119], a[184:187], v[248:251]\n"
"buffer_load_dwordx4 a[184:187], v6, s[28:31], s91 offen offset:2048 sc1\n"
"v_mfma_f32_16x16x32_bf16 v[252:255], a[116:119], a[188:191], v[252:255]\n"
"v_mfma_f32_16x16x32_bf16 v[140:143], a[88:91], a[188:191], v[140:143]\n"
"v_mfma_f32_16x16x32_bf16 v[236:239], a[112:115], a[188:191], v[236:239]\n"
"v_mfma_f32_16x16x32_bf16 v[124:127], a[84:87], a[188:191], v[124:127]\n"
"v_mfma_f32_16x16x32_bf16 v[220:223], a[108:111], a[188:191], v[220:223]\n"
"v_mfma_f32_16x16x32_bf16 v[108:111], a[80:83], a[188:191], v[108:111]\n"
"v_mfma_f32_16x16x32_bf16 v[204:207], a[104:107], a[188:191], v[204:207]\n"
"v_mfma_f32_16x16x32_bf16 v[92:95], a[76:79], a[188:191], v[92:95]\n"
"v_mfma_f32_16x16x32_bf16 v[188:191], a[100:103], a[188:191], v[188:191]\n"
"s_add_u32 s82, s82, s53\n"
"v_mfma_f32_16x16x32_bf16 v[76:79], a[72:75], a[188:191], v[76:79]\n"
"s_add_u32 s83, s83, s54\n"
"v_mfma_f32_16x16x32_bf16 v[172:175], a[96:99], a[188:191], v[172:175]\n"
"v_mfma_f32_16x16x32_bf16 v[60:63], a[68:71], a[188:191], v[60:63]\n"
"v_mfma_f32_16x16x32_bf16 v[156:159], a[92:95], a[188:191], v[156:159]\n"
"s_waitcnt lgkmcnt(0)\n"
"v_mfma_f32_16x16x32_bf16 v[44:47], a[64:67], a[188:191], v[44:47]\n"
"s_sub_u32 s84, s84, 1\n"
"v_mfma_f32_16x16x32_bf16 a[220:223], a[124:127], a[188:191], a[220:223]\n"
"s_cmp_lg_u32 s84, 0\n"
"v_mfma_f32_16x16x32_bf16 a[204:207], a[120:123], a[188:191], a[204:207]\n"
"buffer_load_dwordx4 a[188:191], v6, s[28:31], s91 offen offset:3072 sc1\n"
"s_cbranch_scc1 Lg1_%=\n"
"v_mfma_f32_16x16x32_bf16 a[192:195], a[56:59], a[128:131], a[192:195]\n"
"ds_read_b64_tr_b16 a[120:121], v4 offset:23552\n"
"v_mfma_f32_16x16x32_bf16 a[208:211], a[60:63], a[128:131], a[208:211]\n"
"s_mov_b32 s88, 4587520\n"
"ds_read_b64_tr_b16 a[122:123], v4 offset:31744\n"
"v_mfma_f32_16x16x32_bf16 v[32:35], a[0:3], a[128:131], v[32:35]\n"
"ds_read_b64_tr_b16 a[124:125], v5 offset:23552\n"
"v_mfma_f32_16x16x32_bf16 v[144:147], a[28:31], a[128:131], v[144:147]\n"
"buffer_load_dwordx4 v[20:23], v18, s[16:19], s88 offen sc1\n"
"v_mfma_f32_16x16x32_bf16 v[48:51], a[4:7], a[128:131], v[48:51]\n"
"ds_read_b64_tr_b16 a[126:127], v5 offset:31744\n"
"v_mfma_f32_16x16x32_bf16 v[160:163], a[32:35], a[128:131], v[160:163]\n"
"ds_read_b64_tr_b16 a[64:65], v3 offset:16384\n"
"v_mfma_f32_16x16x32_bf16 v[64:67], a[8:11], a[128:131], v[64:67]\n"
"ds_read_b64_tr_b16 a[66:67], v3 offset:19968\n"
"v_mfma_f32_16x16x32_bf16 v[176:179], a[36:39], a[128:131], v[176:179]\n"
"buffer_load_dwordx4 v[24:27], v18, s[16:19], s88 offen offset:1024 sc1\n"
"v_mfma_f32_16x16x32_bf16 v[80:83], a[12:15], a[128:131], v[80:83]\n"
"ds_read_b64_tr_b16 a[92:93], v3 offset:24576\n"
"v_mfma_f32_16x16x32_bf16 v[192:195], a[40:43], a[128:131], v[192:195]\n"
"ds_read_b64_tr_b16 a[94:95], v3 offset:28160\n"
"v_mfma_f32_16x16x32_bf16 v[96:99], a[16:19], a[128:131], v[96:99]\n"
"ds_read_b64_tr_b16 a[68:69], v3 offset:16416\n"
"v_mfma_f32_16x16x32_bf16 v[208:211], a[44:47], a[128:131], v[208:211]\n"
"s_getpc_b64 s[92:93]\n"
"Lct1_%=:\n"
"s_and_b32 s93, s93, 0xffff\n"
"v_mfma_f32_16x16x32_bf16 v[112:115], a[20:23], a[128:131], v[112:115]\n"
"ds_read_b64_tr_b16 a[70:71], v3 offset:20000\n"
"v_mfma_f32_16x16x32_bf16 v[224:227], a[48:51], a[128:131], v[224:227]\n"
"ds_read_b64_tr_b16 a[96:97], v3 offset:24608\n"
"v_mfma_f32_16x16x32_bf16 v[128:131], a[24:27], a[128:131], v[128:131]\n"
"ds_read_b64_tr_b16 a[98:99], v3 offset:28192\n"
"v_mfma_f32_16x16x32_bf16 v[240:243], a[52:55], a[128:131], v[240:243]\n"
"s_mov_b32 s94, Lkend0-Lct1_%=\n"
"s_mov_b32 s95, 0x20000\n"
"v_mfma_f32_16x16x32_bf16 v[244:247], a[52:55], a[132:135], v[244:247]\n"
"ds_read_b64_tr_b16 a[72:73], v3 offset:16448\n"
"v_mfma_f32_16x16x32_bf16 v[132:135], a[24:27], a[132:135], v[132:135]\n"
"ds_read_b64_tr_b16 a[74:75], v3 offset:20032\n"
"v_mfma_f32_16x16x32_bf16 v[228:231], a[48:51], a[132:135], v[228:231]\n"
"ds_read_b64_tr_b16 a[100:101], v3 offset:24640\n"
"v_mfma_f32_16x16x32_bf16 v[116:119], a[20:23], a[132:135], v[116:119]\n"
"s_lshr_b32 s90, s78, 14\n"
"s_lshl_b32 s90, s90, 8\n"
"v_mfma_f32_16x16x32_bf16 v[212:215], a[44:47], a[132:135], v[212:215]\n"
"ds_read_b64_tr_b16 a[102:103], v3 offset:28224\n"
"v_mfma_f32_16x16x32_bf16 v[100:103], a[16:19], a[132:135], v[100:103]\n"
"ds_read_b64_tr_b16 a[76:77], v3 offset:16480\n"
"v_mfma_f32_16x16x32_bf16 v[196:199], a[40:43], a[132:135], v[196:199]\n"
"ds_read_b64_tr_b16 a[78:79], v3 offset:20064\n"
"v_mfma_f32_16x16x32_bf16 v[84:87], a[12:15], a[132:135], v[84:87]\n"
"v_lshlrev_b32 v9, 3, v18\n"
"v_mfma_f32_16x16x32_bf16 v[180:183], a[36:39], a[132:135], v[180:183]\n"
"ds_read_b64_tr_b16 a[104:105], v3 offset:24672\n"
"v_mfma_f32_16x16x32_bf16 v[68:71], a[8:11], a[132:135], v[68:71]\n"
"ds_read_b64_tr_b16 a[106:107], v3 offset:28256\n"
"v_mfma_f32_16x16x32_bf16 v[164:167], a[32:35], a[132:135], v[164:167]\n"
"ds_read_b64_tr_b16 a[80:81], v3 offset:16512\n"
"v_mfma_f32_16x16x32_bf16 v[52:55], a[4:7], a[132:135], v[52:55]\n"
"s_mov_b64 exec, 3\n"
"buffer_load_dword v2, v9, s[92:95], s90 offen\n"
"s_mov_b64 exec, -1\n"
"v_mfma_f32_16x16x32_bf16 v[148:151], a[28:31], a[132:135], v[148:151]\n"
"ds_read_b64_tr_b16 a[82:83], v3 offset:20096\n"
"v_mfma_f32_16x16x32_bf16 v[36:39], a[0:3], a[132:135], v[36:39]\n"
"ds_read_b64_tr_b16 a[108:109], v3 offset:24704\n"
"v_mfma_f32_16x16x32_bf16 a[212:215], a[60:63], a[132:135], a[212:215]\n"
"ds_read_b64_tr_b16 a[110:111], v3 offset:28288\n"
"v_mfma_f32_16x16x32_bf16 a[196:199], a[56:59], a[132:135], a[196:199]\n"
"v_mfma_f32_16x16x32_bf16 a[200:203], a[56:59], a[136:139], a[200:203]\n"
"ds_read_b64_tr_b16 a[84:85], v3 offset:16544\n"
"v_mfma_f32_16x16x32_bf16 a[216:219], a[60:63], a[136:139], a[216:219]\n"
"ds_read_b64_tr_b16 a[86:87], v3 offset:20128\n"
"v_mfma_f32_16x16x32_bf16 v[40:43], a[0:3], a[136:139], v[40:43]\n"
"ds_read_b64_tr_b16 a[112:113], v3 offset:24736\n"
"v_mfma_f32_16x16x32_bf16 v[152:155], a[28:31], a[136:139], v[152:155]\n"
"v_mfma_f32_16x16x32_bf16 v[56:59], a[4:7], a[136:139], v[56:59]\n"
"ds_read_b64_tr_b16 a[114:115], v3 offset:28320\n"
"v_mfma_f32_16x16x32_bf16 v[168:171], a[32:35], a[136:139], v[168:171]\n"
"ds_read_b64_tr_b16 a[88:89], v3 offset:16576\n"
"v_mfma_f32_16x16x32_bf16 v[72:75], a[8:11], a[136:139], v[72:75]\n"
"ds_read_b64_tr_b16 a[90:91], v3 offset:20160\n"
"v_mfma_f32_16x16x32_bf16 v[184:187], a[36:39], a[136:139], v[184:187]\n"
"v_mfma_f32_16x16x32_bf16 v[88:91], a[12:15], a[136:139], v[88:91]\n"
"ds_read_b64_tr_b16 a[116:117], v3 offset:24768\n"
"v_mfma_f32_16x16x32_bf16 v[200:203], a[40:43], a[136:139], v[200:203]\n"
"ds_read_b64_tr_b16 a[118:119], v3 offset:28352\n"
"v_mfma_f32_16x16x32_bf16 v[104:107], a[16:19], a[136:139], v[104:107]\n"
"v_mfma_f32_16x16x32_bf16 v[216:219], a[44:47], a[136:139], v[216:219]\n"
"v_mfma_f32_16x16x32_bf16 v[120:123], a[20:23], a[136:139], v[120:123]\n"
"v_mfma_f32_16x16x32_bf16 v[232:235], a[48:51], a[136:139], v[232:235]\n"
"v_mfma_f32_16x16x32_bf16 v[136:139], a[24:27], a[136:139], v[136:139]\n"
"v_mfma_f32_16x16x32_bf16 v[248:251], a[52:55], a[136:139], v[248:251]\n"
"v_mfma_f32_16x16x32_bf16 v[252:255], a[52:55], a[140:143], v[252:255]\n"
"v_mfma_f32_16x16x32_bf16 v[140:143], a[24:27], a[140:143], v[140:143]\n"
"v_mfma_f32_16x16x32_bf16 v[236:239], a[48:51], a[140:143], v[236:239]\n"
"v_mfma_f32_16x16x32_bf16 v[124:127], a[20:23], a[140:143], v[124:127]\n"
"v_mfma_f32_16x16x32_bf16 v[220:223], a[44:47], a[140:143], v[220:223]\n"
"v_mfma_f32_16x16x32_bf16 v[108:111], a[16:19], a[140:143], v[108:111]\n"
"v_mfma_f32_16x16x32_bf16 v[204:207], a[40:43], a[140:143], v[204:207]\n"
"v_mfma_f32_16x16x32_bf16 v[92:95], a[12:15], a[140:143], v[92:95]\n"
"v_mfma_f32_16x16x32_bf16 v[188:191], a[36:39], a[140:143], v[188:191]\n"
"v_mfma_f32_16x16x32_bf16 v[76:79], a[8:11], a[140:143], v[76:79]\n"
"v_mfma_f32_16x16x32_bf16 v[172:175], a[32:35], a[140:143], v[172:175]\n"
"v_mfma_f32_16x16x32_bf16 v[60:63], a[4:7], a[140:143], v[60:63]\n"
"v_mfma_f32_16x16x32_bf16 v[156:159], a[28:31], a[140:143], v[156:159]\n"
"s_waitcnt vmcnt(14) lgkmcnt(0)\n"
"s_barrier\n"
"v_mfma_f32_16x16x32_bf16 v[44:47], a[0:3], a[140:143], v[44:47]\n"
"v_mfma_f32_16x16x32_bf16 a[220:223], a[60:63], a[140:143], a[220:223]\n"
"v_mfma_f32_16x16x32_bf16 a[204:207], a[56:59], a[140:143], a[204:207]\n"
"v_mfma_f32_16x16x32_bf16 a[192:195], a[120:123], a[144:147], a[192:195]\n"
"s_waitcnt vmcnt(1)\n"
"ds_read_b64_tr_b16 a[56:57], v4 offset:39936\n"
"v_cmp_ne_u64 vcc, s[80:81], v[20:21]\n"
"v_mfma_f32_16x16x32_bf16 a[208:211], a[124:127], a[144:147], a[208:211]\n"
"ds_read_b64_tr_b16 a[58:59], v4 offset:48128\n"
"v_cmp_ne_u64 s[92:93], s[80:81], v[22:23]\n"
"v_mfma_f32_16x16x32_bf16 v[32:35], a[64:67], a[144:147], v[32:35]\n"
"s_or_b64 vcc, vcc, s[92:93]\n"
"ds_read_b64_tr_b16 a[60:61], v5 offset:39936\n"
"v_cmp_ne_u64 s[92:93], s[80:81], v[24:25]\n"
"v_mfma_f32_16x16x32_bf16 v[144:147], a[92:95], a[144:147], v[144:147]\n"
"s_or_b64 vcc, vcc, s[92:93]\n"
"v_cmp_ne_u64 s[92:93], s[80:81], v[26:27]\n"
"v_mfma_f32_16x16x32_bf16 v[48:51], a[68:71], a[144:147], v[48:51]\n"
"s_or_b64 vcc, vcc, s[92:93]\n"
"s_cbranch_vccnz Lf2s_%=\n"
"Lf2ok_%=:\n"
"ds_read_b64_tr_b16 a[62:63], v5 offset:48128\n"
"v_mfma_f32_16x16x32_bf16 v[160:163], a[96:99], a[144:147], v[160:163]\n"
"s_mov_b32 s88, 4456448\n"
"ds_read_b64_tr_b16 a[0:1], v3 offset:32768\n"
"v_mfma_f32_16x16x32_bf16 v[64:67], a[72:75], a[144:147], v[64:67]\n"
"s_mov_b32 s12, 4194304\n"
"ds_read_b64_tr_b16 a[2:3], v3 offset:36352\n"
"v_mfma_f32_16x16x32_bf16 v[176:179], a[100:103], a[144:147], v[176:179]\n"
"buffer_load_dwordx4 v[20:23], v18, s[16:19], s88 offen sc1\n"
"v_mfma_f32_16x16x32_bf16 v[80:83], a[76:79], a[144:147], v[80:83]\n"
"ds_read_b64_tr_b16 a[28:29], v3 offset:40960\n"
"v_mfma_f32_16x16x32_bf16 v[192:195], a[104:107], a[144:147], v[192:195]\n"
"ds_read_b64_tr_b16 a[30:31], v3 offset:44544\n"
"v_mfma_f32_16x16x32_bf16 v[96:99], a[80:83], a[144:147], v[96:99]\n"
"s_add_u32 m0, s55, 0\n"
"ds_read_b64_tr_b16 a[4:5], v3 offset:32800\n"
"v_mfma_f32_16x16x32_bf16 v[208:211], a[108:111], a[144:147], v[208:211]\n"
"buffer_load_dwordx4 v6, s[16:19], s12 offen offset:0 sc1 lds\n"
"v_mfma_f32_16x16x32_bf16 v[112:115], a[84:87], a[144:147], v[112:115]\n"
"ds_read_b64_tr_b16 a[6:7], v3 offset:36384\n"
"v_mfma_f32_16x16x32_bf16 v[224:227], a[112:115], a[144:147], v[224:227]\n"
"ds_read_b64_tr_b16 a[32:33], v3 offset:40992\n"
"v_mfma_f32_16x16x32_bf16 v[128:131], a[88:91], a[144:147], v[128:131]\n"
"ds_read_b64_tr_b16 a[34:35], v3 offset:44576\n"
"v_mfma_f32_16x16x32_bf16 v[240:243], a[116:119], a[144:147], v[240:243]\n"
"buffer_load_dwordx4 v6, s[16:19], s12 offen offset:1024 sc1 lds\n"
"v_mfma_f32_16x16x32_bf16 v[244:247], a[116:119], a[148:151], v[244:247]\n"
"ds_read_b64_tr_b16 a[8:9], v3 offset:32832\n"
"v_mfma_f32_16x16x32_bf16 v[132:135], a[88:91], a[148:151], v[132:135]\n"
"ds_read_b64_tr_b16 a[10:11], v3 offset:36416\n"
"v_mfma_f32_16x16x32_bf16 v[228:231], a[112:115], a[148:151], v[228:231]\n"
"ds_read_b64_tr_b16 a[36:37], v3 offset:41024\n"
"v_mfma_f32_16x16x32_bf16 v[116:119], a[84:87], a[148:151], v[116:119]\n"
"buffer_load_dwordx4 v6, s[16:19], s12 offen offset:2048 sc1 lds\n"
"v_mfma_f32_16x16x32_bf16 v[212:215], a[108:111], a[148:151], v[212:215]\n"
"ds_read_b64_tr_b16 a[38:39], v3 offset:44608\n"
"v_mfma_f32_16x16x32_bf16 v[100:103], a[80:83], a[148:151], v[100:103]\n"
"ds_read_b64_tr_b16 a[12:13], v3 offset:32864\n"
"v_mfma_f32_16x16x32_bf16 v[196:199], a[104:107], a[148:151], v[196:199]\n"
"ds_read_b64_tr_b16 a[14:15], v3 offset:36448\n"
"v_mfma_f32_16x16x32_bf16 v[84:87], a[76:79], a[148:151], v[84:87]\n"
"buffer_load_dwordx4 v6, s[16:19], s12 offen offset:3072 sc1 lds\n"
"v_mfma_f32_16x16x32_bf16 v[180:183], a[100:103], a[148:151], v[180:183]\n"
"s_add_u32 s12, s12, 16384\n"
"ds_read_b64_tr_b16 a[40:41], v3 offset:41056\n"
"v_mfma_f32_16x16x32_bf16 v[68:71], a[72:75], a[148:151], v[68:71]\n"
"ds_read_b64_tr_b16 a[42:43], v3 offset:44640\n"
"v_mfma_f32_16x16x32_bf16 v[164:167], a[96:99], a[148:151], v[164:167]\n"
"s_add_u32 m0, s55, 16384\n"
"ds_read_b64_tr_b16 a[16:17], v3 offset:32896\n"
"v_mfma_f32_16x16x32_bf16 v[52:55], a[68:71], a[148:151], v[52:55]\n"
"buffer_load_dwordx4 v6, s[16:19], s12 offen offset:0 sc1 lds\n"
"v_mfma_f32_16x16x32_bf16 v[148:151], a[92:95], a[148:151], v[148:151]\n"
"ds_read_b64_tr_b16 a[18:19], v3 offset:36480\n"
"v_mfma_f32_16x16x32_bf16 v[36:39], a[64:67], a[148:151], v[36:39]\n"
"ds_read_b64_tr_b16 a[44:45], v3 offset:41088\n"
"v_mfma_f32_16x16x32_bf16 a[212:215], a[124:127], a[148:151], a[212:215]\n"
"ds_read_b64_tr_b16 a[46:47], v3 offset:44672\n"
"v_mfma_f32_16x16x32_bf16 a[196:199], a[120:123], a[148:151], a[196:199]\n"
"buffer_load_dwordx4 v6, s[16:19], s12 offen offset:1024 sc1 lds\n"
"v_mfma_f32_16x16x32_bf16 a[200:203], a[120:123], a[152:155], a[200:203]\n"
"ds_read_b64_tr_b16 a[20:21], v3 offset:32928\n"
"v_mfma_f32_16x16x32_bf16 a[216:219], a[124:127], a[152:155], a[216:219]\n"
"ds_read_b64_tr_b16 a[22:23], v3 offset:36512\n"
"v_mfma_f32_16x16x32_bf16 v[40:43], a[64:67], a[152:155], v[40:43]\n"
"ds_read_b64_tr_b16 a[48:49], v3 offset:41120\n"
"v_mfma_f32_16x16x32_bf16 v[152:155], a[92:95], a[152:155], v[152:155]\n"
"buffer_load_dwordx4 v6, s[16:19], s12 offen offset:2048 sc1 lds\n"
"v_mfma_f32_16x16x32_bf16 v[56:59], a[68:71], a[152:155], v[56:59]\n"
"ds_read_b64_tr_b16 a[50:51], v3 offset:44704\n"
"v_mfma_f32_16x16x32_bf16 v[168:171], a[96:99], a[152:155], v[168:171]\n"
"ds_read_b64_tr_b16 a[24:25], v3 offset:32960\n"
"v_mfma_f32_16x16x32_bf16 v[72:75], a[72:75], a[152:155], v[72:75]\n"
"ds_read_b64_tr_b16 a[26:27], v3 offset:36544\n"
"v_mfma_f32_16x16x32_bf16 v[184:187], a[100:103], a[152:155], v[184:187]\n"
"buffer_load_dwordx4 v6, s[16:19], s12 offen offset:3072 sc1 lds\n"
"v_mfma_f32_16x16x32_bf16 v[88:91], a[76:79], a[152:155], v[88:91]\n"
"s_add_u32 s12, s12, 16384\n"
"ds_read_b64_tr_b16 a[52:53], v3 offset:41152\n"
"v_mfma_f32_16x16x32_bf16 v[200:203], a[104:107], a[152:155], v[200:203]\n"
"ds_read_b64_tr_b16 a[54:55], v3 offset:44736\n"
"v_mfma_f32_16x16x32_bf16 v[104:107], a[80:83], a[152:155], v[104:107]\n"
"s_add_u32 m0, s55, 32768\n"
"v_mfma_f32_16x16x32_bf16 v[216:219], a[108:111], a[152:155], v[216:219]\n"
"buffer_load_dwordx4 v6, s[16:19], s12 offen offset:0 sc1 lds\n"
"v_mfma_f32_16x16x32_bf16 v[120:123], a[84:87], a[152:155], v[120:123]\n"
"v_mfma_f32_16x16x32_bf16 v[232:235], a[112:115], a[152:155], v[232:235]\n"
"buffer_load_dwordx4 v6, s[16:19], s12 offen offset:1024 sc1 lds\n"
"v_mfma_f32_16x16x32_bf16 v[136:139], a[88:91], a[152:155], v[136:139]\n"
"v_mfma_f32_16x16x32_bf16 v[248:251], a[116:119], a[152:155], v[248:251]\n"
"buffer_load_dwordx4 v6, s[16:19], s12 offen offset:2048 sc1 lds\n"
"v_mfma_f32_16x16x32_bf16 v[252:255], a[116:119], a[156:159], v[252:255]\n"
"v_mfma_f32_16x16x32_bf16 v[140:143], a[88:91], a[156:159], v[140:143]\n"
"buffer_load_dwordx4 v6, s[16:19], s12 offen offset:3072 sc1 lds\n"
"v_mfma_f32_16x16x32_bf16 v[236:239], a[112:115], a[156:159], v[236:239]\n"
"s_add_u32 s12, s12, 16384\n"
"v_mfma_f32_16x16x32_bf16 v[124:127], a[84:87], a[156:159], v[124:127]\n"
"s_add_u32 m0, s55, 49152\n"
"v_mfma_f32_16x16x32_bf16 v[220:223], a[108:111], a[156:159], v[220:223]\n"
"buffer_load_dwordx4 v6, s[16:19], s12 offen offset:0 sc1 lds\n"
"v_mfma_f32_16x16x32_bf16 v[108:111], a[80:83], a[156:159], v[108:111]\n"
"v_mfma_f32_16x16x32_bf16 v[204:207], a[104:107], a[156:159], v[204:207]\n"
"buffer_load_dwordx4 v6, s[16:19], s12 offen offset:1024 sc1 lds\n"
"v_mfma_f32_16x16x32_bf16 v[92:95], a[76:79], a[156:159], v[92:95]\n"
"v_mfma_f32_16x16x32_bf16 v[188:191], a[100:103], a[156:159], v[188:191]\n"
"buffer_load_dwordx4 v6, s[16:19], s12 offen offset:2048 sc1 lds\n"
"v_mfma_f32_16x16x32_bf16 v[76:79], a[72:75], a[156:159], v[76:79]\n"
"v_mfma_f32_16x16x32_bf16 v[172:175], a[96:99], a[156:159], v[172:175]\n"
"buffer_load_dwordx4 v6, s[16:19], s12 offen offset:3072 sc1 lds\n"
"v_mfma_f32_16x16x32_bf16 v[60:63], a[68:71], a[156:159], v[60:63]\n"
"s_add_u32 s12, s12, 16384\n"
"v_mfma_f32_16x16x32_bf16 v[156:159], a[92:95], a[156:159], v[156:159]\n"
"s_waitcnt vmcnt(20) lgkmcnt(0)\n"
"s_barrier\n"
"v_mfma_f32_16x16x32_bf16 v[44:47], a[64:67], a[156:159], v[44:47]\n"
"s_mov_b32 s88, 4452352\n"
"v_mfma_f32_16x16x32_bf16 a[220:223], a[124:127], a[156:159], a[220:223]\n"
"s_add_u32 s86, s58, 8192\n"
"v_mfma_f32_16x16x32_bf16 a[204:207], a[120:123], a[156:159], a[204:207]\n"
"s_add_u32 s87, s58, 16384\n"
"v_mfma_f32_16x16x32_bf16 a[192:195], a[56:59], a[160:163], a[192:195]\n"
"s_waitcnt vmcnt(16)\n"
"ds_read_b64_tr_b16 a[120:121], v4 offset:56320\n"
"v_max_u32 v20, v20, v22\n"
"v_mfma_f32_16x16x32_bf16 a[208:211], a[60:63], a[160:163], a[208:211]\n"
"s_mov_b32 s90, 0x53800000\n"
"ds_read_b64_tr_b16 a[122:123], v4 offset:64512\n"
"v_max_u32 v21, v21, v23\n"
"v_mfma_f32_16x16x32_bf16 v[32:35], a[0:3], a[160:163], v[32:35]\n"
"s_mov_b32 s20, s32\n"
"ds_read_b64_tr_b16 a[124:125], v5 offset:56320\n"
"v_max_u32_dpp v20, v20, v20 quad_perm:[1,0,3,2] row_mask:0xf bank_mask:0xf\n"
"v_mfma_f32_16x16x32_bf16 v[144:147], a[28:31], a[160:163], v[144:147]\n"
"s_mov_b32 s21, s33\n"
"ds_read_b64_tr_b16 a[126:127], v5 offset:64512\n"
"v_max_u32_dpp v20, v20, v20 quad_perm:[2,3,0,1] row_mask:0xf bank_mask:0xf\n"
"v_mfma_f32_16x16x32_bf16 v[48:51], a[4:7], a[160:163], v[48:51]\n"
"s_mov_b32 s22, s34\n"
"ds_read_b64_tr_b16 a[64:65], v3 offset:49152\n"
"v_max_u32_dpp v20, v20, v20 row_half_mirror row_mask:0xf bank_mask:0xf\n"
"v_mfma_f32_16x16x32_bf16 v[160:163], a[32:35], a[160:163], v[160:163]\n"
"s_mov_b32 s82, s77\n"
"ds_read_b64_tr_b16 a[66:67], v3 offset:52736\n"
"v_max_u32_dpp v20, v20, v20 row_mirror row_mask:0xf bank_mask:0xf\n"
"v_mfma_f32_16x16x32_bf16 v[64:67], a[8:11], a[160:163], v[64:67]\n"
"s_mov_b32 s53, 131072\n"
"ds_read_b64_tr_b16 a[92:93], v3 offset:57344\n"
"v_mov_b32 v22, v20\n"
"v_mfma_f32_16x16x32_bf16 v[176:179], a[36:39], a[160:163], v[176:179]\n"
"ds_read_b64_tr_b16 a[94:95], v3 offset:60928\n"
"v_permlane16_swap_b32 v20, v22\n"
"v_mfma_f32_16x16x32_bf16 v[80:83], a[12:15], a[160:163], v[80:83]\n"
"ds_read_b64_tr_b16 a[68:69], v3 offset:49184\n"
"v_max_u32 v20, v20, v22\n"
"v_mfma_f32_16x16x32_bf16 v[192:195], a[40:43], a[160:163], v[192:195]\n"
"ds_read_b64_tr_b16 a[70:71], v3 offset:52768\n"
"v_mov_b32 v22, v20\n"
"v_mfma_f32_16x16x32_bf16 v[96:99], a[16:19], a[160:163], v[96:99]\n"
"ds_read_b64_tr_b16 a[96:97], v3 offset:57376\n"
"v_permlane32_swap_b32 v20, v22\n"
"v_mfma_f32_16x16x32_bf16 v[208:211], a[44:47], a[160:163], v[208:211]\n"
"ds_read_b64_tr_b16 a[98:99], v3 offset:60960\n"
"v_max_u32 v20, v20, v22\n"
"v_mfma_f32_16x16x32_bf16 v[112:115], a[20:23], a[160:163], v[112:115]\n"
"ds_read_b64_tr_b16 a[72:73], v3 offset:49216\n"
"v_cmp_nge_f32 s[92:93], s90, v21\n"
"v_mfma_f32_16x16x32_bf16 v[224:227], a[48:51], a[160:163], v[224:227]\n"
"ds_read_b64_tr_b16 a[74:75], v3 offset:52800\n"
"v_mov_b32 v9, v20\n"
"v_mfma_f32_16x16x32_bf16 v[128:131], a[24:27], a[160:163], v[128:131]\n"
"ds_read_b64_tr_b16 a[100:101], v3 offset:57408\n"
"v_accvgpr_read_b32 v10, a244\n"
"v_mfma_f32_16x16x32_bf16 v[240:243], a[52:55], a[160:163], v[240:243]\n"
"ds_read_b64_tr_b16 a[102:103], v3 offset:60992\n"
"v_mov_b32 v17, 0xdddd\n"
"v_mfma_f32_16x16x32_bf16 v[244:247], a[52:55], a[164:167], v[244:247]\n"
"ds_read_b64_tr_b16 a[76:77], v3 offset:49248\n"
"v_accvgpr_read_b32 v30, a250\n"
"v_mfma_f32_16x16x32_bf16 v[132:135], a[24:27], a[164:167], v[132:135]\n"
"ds_read_b64_tr_b16 a[78:79], v3 offset:52832\n"
"v_add_u32 v11, s70, v10\n"
"v_mfma_f32_16x16x32_bf16 v[228:231], a[48:51], a[164:167], v[228:231]\n"
"ds_read_b64_tr_b16 a[104:105], v3 offset:57440\n"
"v_add_u32 v10, s69, v10\n"
"v_mfma_f32_16x16x32_bf16 v[116:119], a[20:23], a[164:167], v[116:119]\n"
"ds_read_b64_tr_b16 a[106:107], v3 offset:61024\n"
"v_add_u32 v11, 0xfffffc00, v11\n"
"v_mfma_f32_16x16x32_bf16 v[212:215], a[44:47], a[164:167], v[212:215]\n"
"ds_read_b64_tr_b16 a[80:81], v3 offset:49280\n"
"v_add_u32 v12, 63488, v10\n"
"v_mfma_f32_16x16x32_bf16 v[100:103], a[16:19], a[164:167], v[100:103]\n"
"ds_read_b64_tr_b16 a[82:83], v3 offset:52864\n"
"v_add_u32 v13, 63488, v11\n"
"v_mfma_f32_16x16x32_bf16 v[196:199], a[40:43], a[164:167], v[196:199]\n"
"ds_read_b64_tr_b16 a[108:109], v3 offset:57472\n"
"v_mov_b32 v14, 0\n"
"v_mfma_f32_16x16x32_bf16 v[84:87], a[12:15], a[164:167], v[84:87]\n"
"ds_read_b64_tr_b16 a[110:111], v3 offset:61056\n"
"v_mov_b32 v15, 0\n"
"v_mfma_f32_16x16x32_bf16 v[180:183], a[36:39], a[164:167], v[180:183]\n"
"ds_read_b64_tr_b16 a[84:85], v3 offset:49312\n"
"v_mfma_f32_16x16x32_bf16 v[68:71], a[8:11], a[164:167], v[68:71]\n"
"ds_read_b64_tr_b16 a[86:87], v3 offset:52896\n"
"v_mfma_f32_16x16x32_bf16 v[164:167], a[32:35], a[164:167], v[164:167]\n"
"ds_read_b64_tr_b16 a[112:113], v3 offset:57504\n"
"v_mfma_f32_16x16x32_bf16 v[52:55], a[4:7], a[164:167], v[52:55]\n"
"ds_read_b64_tr_b16 a[114:115], v3 offset:61088\n"
"v_mfma_f32_16x16x32_bf16 v[148:151], a[28:31], a[164:167], v[148:151]\n"
"ds_read_b64_tr_b16 a[88:89], v3 offset:49344\n"
"v_mfma_f32_16x16x32_bf16 v[36:39], a[0:3], a[164:167], v[36:39]\n"
"ds_read_b64_tr_b16 a[90:91], v3 offset:52928\n"
"v_mfma_f32_16x16x32_bf16 a[212:215], a[60:63], a[164:167], a[212:215]\n"
"ds_read_b64_tr_b16 a[116:117], v3 offset:57536\n"
"v_mfma_f32_16x16x32_bf16 a[196:199], a[56:59], a[164:167], a[196:199]\n"
"s_waitcnt lgkmcnt(9)\n"
"ds_read_b64_tr_b16 a[118:119], v3 offset:61120\n"
"v_mfma_f32_16x16x32_bf16 a[192:195], a[120:123], a[176:179], a[192:195]\n"
"buffer_load_dwordx4 a[144:147], v18, s[16:19], s88 offen sc1\n"
"v_mfma_f32_16x16x32_bf16 a[208:211], a[124:127], a[176:179], a[208:211]\n"
"v_mfma_f32_16x16x32_bf16 v[32:35], a[64:67], a[176:179], v[32:35]\n"
"buffer_load_dwordx4 a[148:151], v18, s[16:19], s88 offen offset:1024 sc1\n"
"v_mfma_f32_16x16x32_bf16 v[144:147], a[92:95], a[176:179], v[144:147]\n"
"v_mfma_f32_16x16x32_bf16 v[48:51], a[68:71], a[176:179], v[48:51]\n"
"s_waitcnt vmcnt(2)\n"
"s_barrier\n"
"v_mfma_f32_16x16x32_bf16 v[160:163], a[96:99], a[176:179], v[160:163]\n"
"v_mfma_f32_16x16x32_bf16 v[64:67], a[72:75], a[176:179], v[64:67]\n"
"s_waitcnt lgkmcnt(0)\n"
"v_mfma_f32_16x16x32_bf16 v[176:179], a[100:103], a[176:179], v[176:179]\n"
"v_accvgpr_read_b32 v20, a192\n"
"v_accvgpr_read_b32 v21, a193\n"
"v_mfma_f32_16x16x32_bf16 v[80:83], a[76:79], a[176:179], v[80:83]\n"
"v_accvgpr_read_b32 v22, a194\n"
"v_accvgpr_read_b32 v23, a195\n"
"v_mfma_f32_16x16x32_bf16 v[192:195], a[104:107], a[176:179], v[192:195]\n"
"v_accvgpr_read_b32 v24, a208\n"
"v_accvgpr_read_b32 v25, a209\n"
"v_mfma_f32_16x16x32_bf16 v[96:99], a[80:83], a[176:179], v[96:99]\n"
"v_accvgpr_read_b32 v26, a210\n"
"v_accvgpr_read_b32 v27, a211\n"
"v_mfma_f32_16x16x32_bf16 v[208:211], a[108:111], a[176:179], v[208:211]\n"
"v_max_f32 v32, 0, v32\n"
"v_max_f32 v33, 0, v33\n"
"v_mfma_f32_16x16x32_bf16 v[112:115], a[84:87], a[176:179], v[112:115]\n"
"v_max_f32 v34, 0, v34\n"
"v_max_f32 v35, 0, v35\n"
"v_mfma_f32_16x16x32_bf16 v[224:227], a[112:115], a[176:179], v[224:227]\n"
"v_mul_f32 v32, v32, v144\n"
"v_mul_f32 v33, v33, v145\n"
"v_mfma_f32_16x16x32_bf16 v[128:131], a[88:91], a[176:179], v[128:131]\n"
"v_mul_f32 v34, v34, v146\n"
"v_mul_f32 v35, v35, v147\n"
"v_mfma_f32_16x16x32_bf16 v[240:243], a[116:119], a[176:179], v[240:243]\n"
"v_cvt_pk_bf16_f32 v144, v32, v33\n"
"v_cvt_pk_bf16_f32 v145, v34, v35\n"
"v_mfma_f32_16x16x32_bf16 a[196:199], a[120:123], a[180:183], a[196:199]\n"
"v_cvt_pk_bf16_f32 v32, v20, v21\n"
"v_cvt_pk_bf16_f32 v33, v22, v23\n"
"v_mfma_f32_16x16x32_bf16 a[212:215], a[124:127], a[180:183], a[212:215]\n"
"v_cvt_pk_bf16_f32 v34, v24, v25\n"
"v_cvt_pk_bf16_f32 v35, v26, v27\n"
"v_mfma_f32_16x16x32_bf16 v[36:39], a[64:67], a[180:183], v[36:39]\n"
"v_max_f32 v48, 0, v48\n"
"v_max_f32 v49, 0, v49\n"
"v_mfma_f32_16x16x32_bf16 v[148:151], a[92:95], a[180:183], v[148:151]\n"
"v_max_f32 v50, 0, v50\n"
"v_max_f32 v51, 0, v51\n"
"v_mfma_f32_16x16x32_bf16 v[52:55], a[68:71], a[180:183], v[52:55]\n"
"v_mul_f32 v48, v48, v160\n"
"v_mul_f32 v49, v49, v161\n"
"v_mfma_f32_16x16x32_bf16 v[164:167], a[96:99], a[180:183], v[164:167]\n"
"v_mul_f32 v50, v50, v162\n"
"v_mul_f32 v51, v51, v163\n"
"v_mfma_f32_16x16x32_bf16 v[68:71], a[72:75], a[180:183], v[68:71]\n"
"v_cvt_pk_bf16_f32 v146, v48, v49\n"
"v_cvt_pk_bf16_f32 v147, v50, v51\n"
"v_mfma_f32_16x16x32_bf16 v[180:183], a[100:103], a[180:183], v[180:183]\n"
"buffer_store_dwordx4 v[144:147], v18, s[16:19], s58 offen offset:0 sc1\n"
"v_mfma_f32_16x16x32_bf16 v[84:87], a[76:79], a[180:183], v[84:87]\n"
"v_max_f32 v64, 0, v64\n"
"v_max_f32 v65, 0, v65\n"
"v_mfma_f32_16x16x32_bf16 v[196:199], a[104:107], a[180:183], v[196:199]\n"
"v_max_f32 v66, 0, v66\n"
"v_max_f32 v67, 0, v67\n"
"v_mfma_f32_16x16x32_bf16 v[100:103], a[80:83], a[180:183], v[100:103]\n"
"v_mul_f32 v64, v64, v176\n"
"v_mul_f32 v65, v65, v177\n"
"v_mfma_f32_16x16x32_bf16 v[212:215], a[108:111], a[180:183], v[212:215]\n"
"v_mul_f32 v66, v66, v178\n"
"v_mul_f32 v67, v67, v179\n"
"v_mfma_f32_16x16x32_bf16 v[116:119], a[84:87], a[180:183], v[116:119]\n"
"v_cvt_pk_bf16_f32 v176, v64, v65\n"
"v_cvt_pk_bf16_f32 v177, v66, v67\n"
"v_mfma_f32_16x16x32_bf16 v[228:231], a[112:115], a[180:183], v[228:231]\n"
"v_max_f32 v80, 0, v80\n"
"v_max_f32 v81, 0, v81\n"
"v_mfma_f32_16x16x32_bf16 v[132:135], a[88:91], a[180:183], v[132:135]\n"
"v_max_f32 v82, 0, v82\n"
"v_max_f32 v83, 0, v83\n"
"v_mfma_f32_16x16x32_bf16 v[244:247], a[116:119], a[180:183], v[244:247]\n"
"v_mul_f32 v80, v80, v192\n"
"v_mul_f32 v81, v81, v193\n"
"v_mfma_f32_16x16x32_bf16 v[248:251], a[52:55], a[168:171], v[248:251]\n"
"v_mul_f32 v82, v82, v194\n"
"v_mul_f32 v83, v83, v195\n"
"v_mfma_f32_16x16x32_bf16 v[136:139], a[24:27], a[168:171], v[136:139]\n"
"v_cvt_pk_bf16_f32 v178, v80, v81\n"
"v_cvt_pk_bf16_f32 v179, v82, v83\n"
"v_mfma_f32_16x16x32_bf16 v[232:235], a[48:51], a[168:171], v[232:235]\n"
"buffer_store_dwordx4 v[176:179], v18, s[16:19], s86 offen offset:0 sc1\n"
"v_mfma_f32_16x16x32_bf16 v[120:123], a[20:23], a[168:171], v[120:123]\n"
"v_max_f32 v96, 0, v96\n"
"v_max_f32 v97, 0, v97\n"
"v_mfma_f32_16x16x32_bf16 v[216:219], a[44:47], a[168:171], v[216:219]\n"
"v_max_f32 v98, 0, v98\n"
"v_max_f32 v99, 0, v99\n"
"v_mfma_f32_16x16x32_bf16 v[104:107], a[16:19], a[168:171], v[104:107]\n"
"v_mul_f32 v96, v96, v208\n"
"v_mul_f32 v97, v97, v209\n"
"v_mfma_f32_16x16x32_bf16 v[200:203], a[40:43], a[168:171], v[200:203]\n"
"v_mul_f32 v98, v98, v210\n"
"v_mul_f32 v99, v99, v211\n"
"v_mfma_f32_16x16x32_bf16 v[88:91], a[12:15], a[168:171], v[88:91]\n"
"v_cvt_pk_bf16_f32 v208, v96, v97\n"
"v_cvt_pk_bf16_f32 v209, v98, v99\n"
"v_mfma_f32_16x16x32_bf16 v[184:187], a[36:39], a[168:171], v[184:187]\n"
"v_max_f32 v112, 0, v112\n"
"v_max_f32 v113, 0, v113\n"
"v_mfma_f32_16x16x32_bf16 v[72:75], a[8:11], a[168:171], v[72:75]\n"
"v_max_f32 v114, 0, v114\n"
"v_max_f32 v115, 0, v115\n"
"v_mfma_f32_16x16x32_bf16 v[168:171], a[32:35], a[168:171], v[168:171]\n"
"v_mul_f32 v112, v112, v224\n"
"v_mul_f32 v113, v113, v225\n"
"v_mfma_f32_16x16x32_bf16 v[56:59], a[4:7], a[168:171], v[56:59]\n"
"v_mul_f32 v114, v114, v226\n"
"v_mul_f32 v115, v115, v227\n"
"v_mfma_f32_16x16x32_bf16 v[152:155], a[28:31], a[168:171], v[152:155]\n"
"v_cvt_pk_bf16_f32 v210, v112, v113\n"
"v_cvt_pk_bf16_f32 v211, v114, v115\n"
"v_mfma_f32_16x16x32_bf16 v[40:43], a[0:3], a[168:171], v[40:43]\n"
"buffer_store_dwordx4 v[208:211], v18, s[16:19], s87 offen offset:0 sc1\n"
"v_mfma_f32_16x16x32_bf16 a[216:219], a[60:63], a[168:171], a[216:219]\n"
"v_max_f32 v128, 0, v128\n"
"v_max_f32 v129, 0, v129\n"
"v_mfma_f32_16x16x32_bf16 a[200:203], a[56:59], a[168:171], a[200:203]\n"
"v_max_f32 v130, 0, v130\n"
"v_max_f32 v131, 0, v131\n"
"v_mfma_f32_16x16x32_bf16 a[200:203], a[120:123], a[184:187], a[200:203]\n"
"v_mul_f32 v128, v128, v240\n"
"v_mul_f32 v129, v129, v241\n"
"v_mfma_f32_16x16x32_bf16 a[216:219], a[124:127], a[184:187], a[216:219]\n"
"v_mul_f32 v130, v130, v242\n"
"v_mul_f32 v131, v131, v243\n"
"v_mfma_f32_16x16x32_bf16 v[40:43], a[64:67], a[184:187], v[40:43]\n"
"v_cvt_pk_bf16_f32 v240, v128, v129\n"
"v_cvt_pk_bf16_f32 v241, v130, v131\n"
"v_mfma_f32_16x16x32_bf16 v[152:155], a[92:95], a[184:187], v[152:155]\n"
"buffer_store_dwordx2 v[240:241], v18, s[16:19], s59 offen offset:0 sc1\n"
"v_mfma_f32_16x16x32_bf16 v[56:59], a[68:71], a[184:187], v[56:59]\n"
"v_accvgpr_read_b32 v20, a196\n"
"v_accvgpr_read_b32 v21, a197\n"
"v_mfma_f32_16x16x32_bf16 v[168:171], a[96:99], a[184:187], v[168:171]\n"
"v_accvgpr_read_b32 v22, a198\n"
"v_accvgpr_read_b32 v23, a199\n"
"v_mfma_f32_16x16x32_bf16 v[72:75], a[72:75], a[184:187], v[72:75]\n"
"v_accvgpr_read_b32 v24, a212\n"
"v_accvgpr_read_b32 v25, a213\n"
"v_mfma_f32_16x16x32_bf16 v[184:187], a[100:103], a[184:187], v[184:187]\n"
"v_accvgpr_read_b32 v26, a214\n"
"v_accvgpr_read_b32 v27, a215\n"
"v_mfma_f32_16x16x32_bf16 v[88:91], a[76:79], a[184:187], v[88:91]\n"
"v_max_f32 v36, 0, v36\n"
"v_max_f32 v37, 0, v37\n"
"v_mfma_f32_16x16x32_bf16 v[200:203], a[104:107], a[184:187], v[200:203]\n"
"v_max_f32 v38, 0, v38\n"
"v_max_f32 v39, 0, v39\n"
"v_mfma_f32_16x16x32_bf16 v[104:107], a[80:83], a[184:187], v[104:107]\n"
"v_mul_f32 v36, v36, v148\n"
"v_mul_f32 v37, v37, v149\n"
"v_mfma_f32_16x16x32_bf16 v[216:219], a[108:111], a[184:187], v[216:219]\n"
"v_mul_f32 v38, v38, v150\n"
"v_mul_f32 v39, v39, v151\n"
"v_mfma_f32_16x16x32_bf16 v[120:123], a[84:87], a[184:187], v[120:123]\n"
"v_cvt_pk_bf16_f32 v148, v36, v37\n"
"v_cvt_pk_bf16_f32 v149, v38, v39\n"
"v_mfma_f32_16x16x32_bf16 v[232:235], a[112:115], a[184:187], v[232:235]\n"
"v_cvt_pk_bf16_f32 v36, v20, v21\n"
"v_cvt_pk_bf16_f32 v37, v22, v23\n"
"v_mfma_f32_16x16x32_bf16 v[136:139], a[88:91], a[184:187], v[136:139]\n"
"v_cvt_pk_bf16_f32 v38, v24, v25\n"
"v_cvt_pk_bf16_f32 v39, v26, v27\n"
"v_mfma_f32_16x16x32_bf16 v[248:251], a[116:119], a[184:187], v[248:251]\n"
"v_max_f32 v52, 0, v52\n"
"v_max_f32 v53, 0, v53\n"
"v_mfma_f32_16x16x32_bf16 v[252:255], a[52:55], a[172:175], v[252:255]\n"
"v_max_f32 v54, 0, v54\n"
"v_max_f32 v55, 0, v55\n"
"v_mfma_f32_16x16x32_bf16 v[140:143], a[24:27], a[172:175], v[140:143]\n"
"v_mul_f32 v52, v52, v164\n"
"v_mul_f32 v53, v53, v165\n"
"v_mfma_f32_16x16x32_bf16 v[236:239], a[48:51], a[172:175], v[236:239]\n"
"v_mul_f32 v54, v54, v166\n"
"v_mul_f32 v55, v55, v167\n"
"v_mfma_f32_16x16x32_bf16 v[124:127], a[20:23], a[172:175], v[124:127]\n"
"v_cvt_pk_bf16_f32 v150, v52, v53\n"
"v_cvt_pk_bf16_f32 v151, v54, v55\n"
"v_mfma_f32_16x16x32_bf16 v[220:223], a[44:47], a[172:175], v[220:223]\n"
"buffer_store_dwordx4 v[148:151], v18, s[16:19], s58 offen offset:1024 sc1\n"
"v_mfma_f32_16x16x32_bf16 v[108:111], a[16:19], a[172:175], v[108:111]\n"
"v_max_f32 v68, 0, v68\n"
"v_max_f32 v69, 0, v69\n"
"v_mfma_f32_16x16x32_bf16 v[204:207], a[40:43], a[172:175], v[204:207]\n"
"v_max_f32 v70, 0, v70\n"
"v_max_f32 v71, 0, v71\n"
"v_mfma_f32_16x16x32_bf16 v[92:95], a[12:15], a[172:175], v[92:95]\n"
"v_mul_f32 v68, v68, v180\n"
"v_mul_f32 v69, v69, v181\n"
"v_mfma_f32_16x16x32_bf16 v[188:191], a[36:39], a[172:175], v[188:191]\n"
"v_mul_f32 v70, v70, v182\n"
"v_mul_f32 v71, v71, v183\n"
"v_mfma_f32_16x16x32_bf16 v[76:79], a[8:11], a[172:175], v[76:79]\n"
"v_cvt_pk_bf16_f32 v180, v68, v69\n"
"v_cvt_pk_bf16_f32 v181, v70, v71\n"
"v_mfma_f32_16x16x32_bf16 v[172:175], a[32:35], a[172:175], v[172:175]\n"
"v_max_f32 v84, 0, v84\n"
"v_max_f32 v85, 0, v85\n"
"v_mfma_f32_16x16x32_bf16 v[60:63], a[4:7], a[172:175], v[60:63]\n"
"v_max_f32 v86, 0, v86\n"
"v_max_f32 v87, 0, v87\n"
"v_mfma_f32_16x16x32_bf16 v[156:159], a[28:31], a[172:175], v[156:159]\n"
"v_mul_f32 v84, v84, v196\n"
"v_mul_f32 v85, v85, v197\n"
"v_mfma_f32_16x16x32_bf16 v[44:47], a[0:3], a[172:175], v[44:47]\n"
"v_mul_f32 v86, v86, v198\n"
"v_mul_f32 v87, v87, v199\n"
"v_mfma_f32_16x16x32_bf16 a[220:223], a[60:63], a[172:175], a[220:223]\n"
"v_cvt_pk_bf16_f32 v182, v84, v85\n"
"v_cvt_pk_bf16_f32 v183, v86, v87\n"
"v_mfma_f32_16x16x32_bf16 a[204:207], a[56:59], a[172:175], a[204:207]\n"
"buffer_store_dwordx4 v[180:183], v18, s[16:19], s86 offen offset:1024 sc1\n"
"v_mfma_f32_16x16x32_bf16 a[204:207], a[120:123], a[188:191], a[204:207]\n"
"v_max_f32 v100, 0, v100\n"
"v_max_f32 v101, 0, v101\n"
"v_mfma_f32_16x16x32_bf16 a[220:223], a[124:127], a[188:191], a[220:223]\n"
"v_max_f32 v102, 0, v102\n"
"v_max_f32 v103, 0, v103\n"
"v_mfma_f32_16x16x32_bf16 v[44:47], a[64:67], a[188:191], v[44:47]\n"
"v_mul_f32 v100, v100, v212\n"
"v_mul_f32 v101, v101, v213\n"
"v_mfma_f32_16x16x32_bf16 v[156:159], a[92:95], a[188:191], v[156:159]\n"
"v_mul_f32 v102, v102, v214\n"
"v_mul_f32 v103, v103, v215\n"
"v_mfma_f32_16x16x32_bf16 v[60:63], a[68:71], a[188:191], v[60:63]\n"
"v_cvt_pk_bf16_f32 v212, v100, v101\n"
"v_cvt_pk_bf16_f32 v213, v102, v103\n"
"v_mfma_f32_16x16x32_bf16 v[172:175], a[96:99], a[188:191], v[172:175]\n"
"v_max_f32 v116, 0, v116\n"
"v_max_f32 v117, 0, v117\n"
"v_mfma_f32_16x16x32_bf16 v[76:79], a[72:75], a[188:191], v[76:79]\n"
"v_max_f32 v118, 0, v118\n"
"v_max_f32 v119, 0, v119\n"
"v_mfma_f32_16x16x32_bf16 v[188:191], a[100:103], a[188:191], v[188:191]\n"
"v_mul_f32 v116, v116, v228\n"
"v_mul_f32 v117, v117, v229\n"
"v_mfma_f32_16x16x32_bf16 v[92:95], a[76:79], a[188:191], v[92:95]\n"
"v_mul_f32 v118, v118, v230\n"
"v_mul_f32 v119, v119, v231\n"
"v_mfma_f32_16x16x32_bf16 v[204:207], a[104:107], a[188:191], v[204:207]\n"
"v_cvt_pk_bf16_f32 v214, v116, v117\n"
"v_cvt_pk_bf16_f32 v215, v118, v119\n"
"v_mfma_f32_16x16x32_bf16 v[108:111], a[80:83], a[188:191], v[108:111]\n"
"buffer_store_dwordx4 v[212:215], v18, s[16:19], s87 offen offset:1024 sc1\n"
"v_mfma_f32_16x16x32_bf16 v[220:223], a[108:111], a[188:191], v[220:223]\n"
"v_max_f32 v132, 0, v132\n"
"v_max_f32 v133, 0, v133\n"
"v_mfma_f32_16x16x32_bf16 v[124:127], a[84:87], a[188:191], v[124:127]\n"
"v_max_f32 v134, 0, v134\n"
"v_max_f32 v135, 0, v135\n"
"v_mfma_f32_16x16x32_bf16 v[236:239], a[112:115], a[188:191], v[236:239]\n"
"v_mul_f32 v132, v132, v244\n"
"v_mul_f32 v133, v133, v245\n"
"v_mfma_f32_16x16x32_bf16 v[140:143], a[88:91], a[188:191], v[140:143]\n"
"v_mul_f32 v134, v134, v246\n"
"v_mul_f32 v135, v135, v247\n"
"v_mfma_f32_16x16x32_bf16 v[252:255], a[116:119], a[188:191], v[252:255]\n"
"v_cvt_pk_bf16_f32 v244, v132, v133\n"
"v_cvt_pk_bf16_f32 v245, v134, v135\n"
"buffer_store_dwordx2 v[244:245], v18, s[16:19], s59 offen offset:1024 sc1\n"
"v_permlane16_swap_b32 v32, v36\n"
"v_permlane16_swap_b32 v33, v37\n"
"v_permlane16_swap_b32 v34, v38\n"
"v_permlane16_swap_b32 v35, v39\n"
"v_accvgpr_read_b32 v20, a200\n"
"v_accvgpr_read_b32 v21, a201\n"
"v_accvgpr_read_b32 v22, a202\n"
"v_accvgpr_read_b32 v23, a203\n"
"v_accvgpr_read_b32 v24, a216\n"
"v_accvgpr_read_b32 v25, a217\n"
"v_accvgpr_read_b32 v26, a218\n"
"v_accvgpr_read_b32 v27, a219\n"
"v_max_f32 v40, 0, v40\n"
"v_max_f32 v41, 0, v41\n"
"v_max_f32 v42, 0, v42\n"
"v_max_f32 v43, 0, v43\n"
"v_pk_mul_f32 v[40:41], v[40:41], v[152:153]\n"
"v_pk_mul_f32 v[42:43], v[42:43], v[154:155]\n"
"v_cvt_pk_bf16_f32 v152, v40, v41\n"
"v_cvt_pk_bf16_f32 v153, v42, v43\n"
"v_cvt_pk_bf16_f32 v40, v20, v21\n"
"v_cvt_pk_bf16_f32 v41, v22, v23\n"
"v_cvt_pk_bf16_f32 v42, v24, v25\n"
"v_cvt_pk_bf16_f32 v43, v26, v27\n"
"v_max_f32 v56, 0, v56\n"
"v_max_f32 v57, 0, v57\n"
"v_max_f32 v58, 0, v58\n"
"v_max_f32 v59, 0, v59\n"
"v_pk_mul_f32 v[56:57], v[56:57], v[168:169]\n"
"v_pk_mul_f32 v[58:59], v[58:59], v[170:171]\n"
"v_cvt_pk_bf16_f32 v154, v56, v57\n"
"v_cvt_pk_bf16_f32 v155, v58, v59\n"
"v_max_f32 v72, 0, v72\n"
"v_max_f32 v73, 0, v73\n"
"v_max_f32 v74, 0, v74\n"
"v_max_f32 v75, 0, v75\n"
"v_pk_mul_f32 v[72:73], v[72:73], v[184:185]\n"
"v_pk_mul_f32 v[74:75], v[74:75], v[186:187]\n"
"v_cvt_pk_bf16_f32 v184, v72, v73\n"
"v_cvt_pk_bf16_f32 v185, v74, v75\n"
"v_max_f32 v88, 0, v88\n"
"v_max_f32 v89, 0, v89\n"
"v_max_f32 v90, 0, v90\n"
"v_max_f32 v91, 0, v91\n"
"v_pk_mul_f32 v[88:89], v[88:89], v[200:201]\n"
"v_pk_mul_f32 v[90:91], v[90:91], v[202:203]\n"
"v_cvt_pk_bf16_f32 v186, v88, v89\n"
"v_cvt_pk_bf16_f32 v187, v90, v91\n"
"v_max_f32 v104, 0, v104\n"
"v_max_f32 v105, 0, v105\n"
"v_max_f32 v106, 0, v106\n"
"v_max_f32 v107, 0, v107\n"
"v_pk_mul_f32 v[104:105], v[104:105], v[216:217]\n"
"v_pk_mul_f32 v[106:107], v[106:107], v[218:219]\n"
"v_cvt_pk_bf16_f32 v216, v104, v105\n"
"v_cvt_pk_bf16_f32 v217, v106, v107\n"
"v_max_f32 v120, 0, v120\n"
"v_max_f32 v121, 0, v121\n"
"v_max_f32 v122, 0, v122\n"
"v_max_f32 v123, 0, v123\n"
"v_pk_mul_f32 v[120:121], v[120:121], v[232:233]\n"
"v_pk_mul_f32 v[122:123], v[122:123], v[234:235]\n"
"v_cvt_pk_bf16_f32 v218, v120, v121\n"
"v_cvt_pk_bf16_f32 v219, v122, v123\n"
"v_max_f32 v136, 0, v136\n"
"v_max_f32 v137, 0, v137\n"
"v_max_f32 v138, 0, v138\n"
"v_max_f32 v139, 0, v139\n"
"v_pk_mul_f32 v[136:137], v[136:137], v[248:249]\n"
"v_pk_mul_f32 v[138:139], v[138:139], v[250:251]\n"
"v_cvt_pk_bf16_f32 v248, v136, v137\n"
"v_cvt_pk_bf16_f32 v249, v138, v139\n"
"v_accvgpr_read_b32 v20, a204\n"
"v_accvgpr_read_b32 v21, a205\n"
"v_accvgpr_read_b32 v22, a206\n"
"v_accvgpr_read_b32 v23, a207\n"
"v_accvgpr_read_b32 v24, a220\n"
"v_accvgpr_read_b32 v25, a221\n"
"v_accvgpr_read_b32 v26, a222\n"
"v_accvgpr_read_b32 v27, a223\n"
"v_max_f32 v44, 0, v44\n"
"v_max_f32 v45, 0, v45\n"
"v_max_f32 v46, 0, v46\n"
"v_max_f32 v47, 0, v47\n"
"v_pk_mul_f32 v[44:45], v[44:45], v[156:157]\n"
"v_pk_mul_f32 v[46:47], v[46:47], v[158:159]\n"
"v_cvt_pk_bf16_f32 v156, v44, v45\n"
"v_cvt_pk_bf16_f32 v157, v46, v47\n"
"v_cvt_pk_bf16_f32 v44, v20, v21\n"
"v_cvt_pk_bf16_f32 v45, v22, v23\n"
"v_cvt_pk_bf16_f32 v46, v24, v25\n"
"v_cvt_pk_bf16_f32 v47, v26, v27\n"
"v_max_f32 v60, 0, v60\n"
"v_max_f32 v61, 0, v61\n"
"v_max_f32 v62, 0, v62\n"
"v_max_f32 v63, 0, v63\n"
"v_pk_mul_f32 v[60:61], v[60:61], v[172:173]\n"
"v_pk_mul_f32 v[62:63], v[62:63], v[174:175]\n"
"v_cvt_pk_bf16_f32 v158, v60, v61\n"
"v_cvt_pk_bf16_f32 v159, v62, v63\n"
"v_accvgpr_write_b32 a176, v32\n"
"v_accvgpr_write_b32 a177, v33\n"
"v_accvgpr_write_b32 a178, v34\n"
"v_accvgpr_write_b32 a179, v35\n"
"v_accvgpr_write_b32 a180, v36\n"
"v_accvgpr_write_b32 a181, v37\n"
"v_accvgpr_write_b32 a182, v38\n"
"v_accvgpr_write_b32 a183, v39\n"
"v_max_f32 v76, 0, v76\n"
"v_max_f32 v77, 0, v77\n"
"v_max_f32 v78, 0, v78\n"
"v_max_f32 v79, 0, v79\n"
"v_pk_mul_f32 v[76:77], v[76:77], v[188:189]\n"
"v_pk_mul_f32 v[78:79], v[78:79], v[190:191]\n"
"v_cvt_pk_bf16_f32 v188, v76, v77\n"
"v_cvt_pk_bf16_f32 v189, v78, v79\n"
"s_waitcnt vmcnt(8)\n"
"v_mfma_f32_32x32x16_bf16 v[48:63], a[144:147], a[176:179], 0\n"
"ds_read_b128 a[128:131], v18 offset:0\n"
"ds_read_b128 a[132:135], v18 offset:1024\n"
"v_max_f32 v92, 0, v92\n"
"v_max_f32 v93, 0, v93\n"
"v_max_f32 v94, 0, v94\n"
"v_max_f32 v95, 0, v95\n"
"v_pk_mul_f32 v[92:93], v[92:93], v[204:205]\n"
"v_pk_mul_f32 v[94:95], v[94:95], v[206:207]\n"
"v_cvt_pk_bf16_f32 v190, v92, v93\n"
"v_cvt_pk_bf16_f32 v191, v94, v95\n"
"v_max_f32 v108, 0, v108\n"
"v_max_f32 v109, 0, v109\n"
"v_max_f32 v110, 0, v110\n"
"v_max_f32 v111, 0, v111\n"
"v_pk_mul_f32 v[108:109], v[108:109], v[220:221]\n"
"v_pk_mul_f32 v[110:111], v[110:111], v[222:223]\n"
"v_cvt_pk_bf16_f32 v220, v108, v109\n"
"v_cvt_pk_bf16_f32 v221, v110, v111\n"
"v_mfma_f32_32x32x16_bf16 v[48:63], a[148:151], a[180:183], v[48:63]\n"
"ds_read_b128 a[136:139], v18 offset:2048\n"
"ds_read_b128 a[140:143], v18 offset:3072\n"
"v_max_f32 v124, 0, v124\n"
"v_max_f32 v125, 0, v125\n"
"v_max_f32 v126, 0, v126\n"
"v_max_f32 v127, 0, v127\n"
"v_pk_mul_f32 v[124:125], v[124:125], v[236:237]\n"
"v_pk_mul_f32 v[126:127], v[126:127], v[238:239]\n"
"v_cvt_pk_bf16_f32 v222, v124, v125\n"
"v_cvt_pk_bf16_f32 v223, v126, v127\n"
"v_max_f32 v140, 0, v140\n"
"v_max_f32 v141, 0, v141\n"
"v_max_f32 v142, 0, v142\n"
"v_max_f32 v143, 0, v143\n"
"v_pk_mul_f32 v[140:141], v[140:141], v[252:253]\n"
"v_pk_mul_f32 v[142:143], v[142:143], v[254:255]\n"
"v_cvt_pk_bf16_f32 v252, v140, v141\n"
"v_cvt_pk_bf16_f32 v253, v142, v143\n"
"v_accvgpr_write_b32 a208, 0\n"
"v_accvgpr_write_b32 a209, 0\n"
"v_accvgpr_write_b32 a212, 0\n"
"v_accvgpr_write_b32 a213, 0\n"
"v_permlane16_swap_b32 v40, v44\n"
"v_permlane16_swap_b32 v41, v45\n"
"v_permlane16_swap_b32 v42, v46\n"
"v_permlane16_swap_b32 v43, v47\n"
"v_dot2c_f32_bf16 v14, v32, v32\n"
"v_dot2c_f32_bf16 v15, v40, v40\n"
"v_dot2c_f32_bf16 v14, v33, v33\n"
"v_dot2c_f32_bf16 v15, v41, v41\n"
"v_dot2c_f32_bf16 v14, v34, v34\n"
"v_dot2c_f32_bf16 v15, v42, v42\n"
"v_dot2c_f32_bf16 v14, v35, v35\n"
"v_dot2c_f32_bf16 v15, v43, v43\n"
"v_dot2c_f32_bf16 v14, v36, v36\n"
"v_dot2c_f32_bf16 v15, v44, v44\n"
"v_dot2c_f32_bf16 v14, v37, v37\n"
"v_dot2c_f32_bf16 v15, v45, v45\n"
"v_dot2c_f32_bf16 v14, v38, v38\n"
"v_dot2c_f32_bf16 v15, v46, v46\n"
"v_dot2c_f32_bf16 v14, v39, v39\n"
"v_dot2c_f32_bf16 v15, v47, v47\n"
"v_mov_b32 v20, 0\n"
"v_mov_b32 v21, 0\n"
"v_mov_b32 v22, 0\n"
"v_mov_b32 v23, 0\n"
"v_accvgpr_write_b32 a184, v40\n"
"v_accvgpr_write_b32 a185, v41\n"
"v_accvgpr_write_b32 a186, v42\n"
"v_accvgpr_write_b32 a187, v43\n"
"v_mfma_f32_32x32x16_bf16 v[96:111], v[20:23], v[20:23], 0\n"
"v_accvgpr_write_b32 a188, v44\n"
"v_accvgpr_write_b32 a189, v45\n"
"v_accvgpr_write_b32 a190, v46\n"
"v_accvgpr_write_b32 a191, v47\n"
"v_permlane32_swap_b32 v14, v15\n"
"v_add_f32 v14, v14, v15\n"
"v_mfma_f32_32x32x16_bf16 v[112:127], v[20:23], v[20:23], 0\n"
"v_mul_f32 v14, v14, v9\n"
"v_cmp_nge_f32 vcc, 0x45fd2000, v14\n"
"v_mov_b32 v24, v6\n"
"s_waitcnt lgkmcnt(0)\n"
"s_or_b64 vcc, vcc, s[92:93]\n"
"s_cselect_b32 s15, 1, 0\n"
"v_mfma_f32_32x32x16_bf16 v[64:79], a[144:147], a[184:187], 0\n"
"v_exp_f32 v48, v48\n"
"v_exp_f32 v49, v49\n"
"v_exp_f32 v50, v50\n"
"v_exp_f32 v51, v51\n"
"v_mfma_f32_32x32x16_bf16 v[64:79], a[148:151], a[188:191], v[64:79]\n"
"buffer_store_dwordx4 v[152:155], v18, s[16:19], s58 offen offset:2048 sc1\n"
"v_exp_f32 v52, v52\n"
"v_exp_f32 v53, v53\n"
"v_exp_f32 v54, v54\n"
"v_exp_f32 v55, v55\n"
"v_exp_f32 v56, v56\n"
"v_exp_f32 v57, v57\n"
"v_exp_f32 v58, v58\n"
"v_exp_f32 v59, v59\n"
"v_exp_f32 v60, v60\n"
"v_exp_f32 v61, v61\n"
"v_exp_f32 v62, v62\n"
"v_exp_f32 v63, v63\n"
"v_mfma_f32_32x32x16_bf16 v[80:95], a[128:131], a[176:179], 0\n"
"v_exp_f32 v64, v64\n"
"v_exp_f32 v65, v65\n"
"v_exp_f32 v66, v66\n"
"v_mfma_f32_32x32x16_bf16 v[80:95], a[132:135], a[180:183], v[80:95]\n"
"v_exp_f32 v67, v67\n"
"v_exp_f32 v68, v68\n"
"v_exp_f32 v69, v69\n"
"v_smfmac_f32_32x32x32_bf16 v[96:111], a[136:139], v[48:55], v17\n"
"v_exp_f32 v70, v70\n"
"v_exp_f32 v71, v71\n"
"v_exp_f32 v72, v72\n"
"v_smfmac_f32_16x16x64_bf16 a[208:211], a[240:243], v[48:55], v17\n"
"ds_read_b128 a[144:147], v18 offset:4096\n"
"v_exp_f32 v73, v73\n"
"v_smfmac_f32_32x32x32_bf16 v[96:111], a[140:143], v[56:63], v17\n"
"s_add_u32 m0, s55, 65536\n"
"v_exp_f32 v74, v74\n"
"v_exp_f32 v75, v75\n"
"v_exp_f32 v76, v76\n"
"v_smfmac_f32_16x16x64_bf16 a[208:211], a[240:243], v[56:63], v17\n"
"ds_read_b128 a[148:151], v18 offset:5120\n"
"v_exp_f32 v77, v77\n"
"v_exp_f32 v78, v78\n"
"v_exp_f32 v79, v79\n"
"v_mfma_f32_32x32x16_bf16 v[32:47], a[128:131], a[184:187], 0\n"
"v_exp_f32 v80, v80\n"
"v_exp_f32 v81, v81\n"
"v_exp_f32 v82, v82\n"
"v_mfma_f32_32x32x16_bf16 v[32:47], a[132:135], a[188:191], v[32:47]\n"
"v_exp_f32 v83, v83\n"
"v_exp_f32 v84, v84\n"
"v_exp_f32 v85, v85\n"
"v_smfmac_f32_32x32x32_bf16 v[112:127], a[136:139], v[64:71], v17\n"
"buffer_load_dwordx4 v10, s[20:23], s82 offen offset:0 sc1 lds\n"
"v_exp_f32 v86, v86\n"
"v_exp_f32 v87, v87\n"
"v_exp_f32 v88, v88\n"
"v_smfmac_f32_16x16x64_bf16 a[212:215], a[240:243], v[64:71], v17\n"
"ds_read_b128 a[152:155], v18 offset:6144\n"
"v_exp_f32 v89, v89\n"
"v_smfmac_f32_32x32x32_bf16 v[112:127], a[140:143], v[72:79], v17\n"
"buffer_store_dwordx4 v[184:187], v18, s[16:19], s86 offen offset:2048 sc1\n"
"v_exp_f32 v90, v90\n"
"v_exp_f32 v91, v91\n"
"v_exp_f32 v92, v92\n"
"v_smfmac_f32_16x16x64_bf16 a[212:215], a[240:243], v[72:79], v17\n"
"s_waitcnt lgkmcnt(2)\n"
"ds_read_b128 a[156:159], v18 offset:7168\n"
"v_exp_f32 v93, v93\n"
"v_exp_f32 v94, v94\n"
"v_exp_f32 v95, v95\n"
"v_mfma_f32_32x32x16_bf16 v[48:63], a[144:147], a[176:179], 0\n"
"s_waitcnt lgkmcnt(2)\n"
"v_exp_f32 v32, v32\n"
"v_exp_f32 v33, v33\n"
"v_exp_f32 v34, v34\n"
"v_mfma_f32_32x32x16_bf16 v[48:63], a[148:151], a[180:183], v[48:63]\n"
"s_waitcnt lgkmcnt(1)\n"
"v_exp_f32 v35, v35\n"
"v_exp_f32 v36, v36\n"
"v_exp_f32 v37, v37\n"
"v_smfmac_f32_32x32x32_bf16 v[96:111], a[152:155], v[80:87], v17\n"
"buffer_load_dwordx4 v11, s[20:23], s82 offen offset:1024 sc1 lds\n"
"v_exp_f32 v38, v38\n"
"v_exp_f32 v39, v39\n"
"v_exp_f32 v40, v40\n"
"v_smfmac_f32_16x16x64_bf16 a[208:211], a[240:243], v[80:87], v17\n"
"s_waitcnt lgkmcnt(0)\n"
"ds_read_b128 a[128:131], v18 offset:8192\n"
"v_exp_f32 v41, v41\n"
"v_smfmac_f32_32x32x32_bf16 v[96:111], a[156:159], v[88:95], v17\n"
"v_exp_f32 v42, v42\n"
"v_exp_f32 v43, v43\n"
"v_exp_f32 v44, v44\n"
"v_smfmac_f32_16x16x64_bf16 a[208:211], a[240:243], v[88:95], v17\n"
"ds_read_b128 a[132:135], v18 offset:9216\n"
"v_exp_f32 v45, v45\n"
"v_exp_f32 v46, v46\n"
"v_exp_f32 v47, v47\n"
"v_mfma_f32_32x32x16_bf16 v[64:79], a[144:147], a[184:187], 0\n"
"v_exp_f32 v48, v48\n"
"v_exp_f32 v49, v49\n"
"v_exp_f32 v50, v50\n"
"v_mfma_f32_32x32x16_bf16 v[64:79], a[148:151], a[188:191], v[64:79]\n"
"v_exp_f32 v51, v51\n"
"v_exp_f32 v52, v52\n"
"v_exp_f32 v53, v53\n"
"v_smfmac_f32_32x32x32_bf16 v[112:127], a[152:155], v[32:39], v17\n"
"buffer_load_dwordx4 v12, s[20:23], s82 offen offset:2048 sc1 lds\n"
"v_exp_f32 v54, v54\n"
"v_exp_f32 v55, v55\n"
"v_exp_f32 v56, v56\n"
"v_smfmac_f32_16x16x64_bf16 a[212:215], a[240:243], v[32:39], v17\n"
"ds_read_b128 a[136:139], v18 offset:10240\n"
"v_exp_f32 v57, v57\n"
"v_smfmac_f32_32x32x32_bf16 v[112:127], a[156:159], v[40:47], v17\n"
"buffer_store_dwordx4 v[216:219], v18, s[16:19], s87 offen offset:2048 sc1\n"
"v_exp_f32 v58, v58\n"
"v_exp_f32 v59, v59\n"
"v_exp_f32 v60, v60\n"
"v_smfmac_f32_16x16x64_bf16 a[212:215], a[240:243], v[40:47], v17\n"
"s_waitcnt lgkmcnt(2)\n"
"ds_read_b128 a[140:143], v18 offset:11264\n"
"v_exp_f32 v61, v61\n"
"v_exp_f32 v62, v62\n"
"v_exp_f32 v63, v63\n"
"v_mfma_f32_32x32x16_bf16 v[80:95], a[128:131], a[176:179], 0\n"
"s_waitcnt lgkmcnt(2)\n"
"v_exp_f32 v64, v64\n"
"v_exp_f32 v65, v65\n"
"v_exp_f32 v66, v66\n"
"v_mfma_f32_32x32x16_bf16 v[80:95], a[132:135], a[180:183], v[80:95]\n"
"s_waitcnt lgkmcnt(1)\n"
"v_exp_f32 v67, v67\n"
"v_exp_f32 v68, v68\n"
"v_exp_f32 v69, v69\n"
"v_smfmac_f32_32x32x32_bf16 v[96:111], a[136:139], v[48:55], v17\n"
"buffer_load_dwordx4 v13, s[20:23], s82 offen offset:3072 sc1 lds\n"
"v_exp_f32 v70, v70\n"
"v_exp_f32 v71, v71\n"
"v_exp_f32 v72, v72\n"
"v_smfmac_f32_16x16x64_bf16 a[208:211], a[240:243], v[48:55], v17\n"
"s_waitcnt lgkmcnt(0)\n"
"ds_read_b128 a[144:147], v18 offset:12288\n"
"v_exp_f32 v73, v73\n"
"v_smfmac_f32_32x32x32_bf16 v[96:111], a[140:143], v[56:63], v17\n"
"v_exp_f32 v74, v74\n"
"v_exp_f32 v75, v75\n"
"v_exp_f32 v76, v76\n"
"v_smfmac_f32_16x16x64_bf16 a[208:211], a[240:243], v[56:63], v17\n"
"ds_read_b128 a[148:151], v18 offset:13312\n"
"v_exp_f32 v77, v77\n"
"v_exp_f32 v78, v78\n"
"v_exp_f32 v79, v79\n"
"v_mfma_f32_32x32x16_bf16 v[32:47], a[128:131], a[184:187], 0\n"
"s_add_u32 s82, s82, s53\n"
"v_exp_f32 v80, v80\n"
"v_exp_f32 v81, v81\n"
"v_exp_f32 v82, v82\n"
"v_mfma_f32_32x32x16_bf16 v[32:47], a[132:135], a[188:191], v[32:47]\n"
"ds_read_b128 a[152:155], v18 offset:14336\n"
"v_exp_f32 v83, v83\n"
"v_exp_f32 v84, v84\n"
"v_exp_f32 v85, v85\n"
"v_smfmac_f32_32x32x32_bf16 v[112:127], a[136:139], v[64:71], v17\n"
"v_exp_f32 v86, v86\n"
"v_exp_f32 v87, v87\n"
"v_exp_f32 v88, v88\n"
"v_smfmac_f32_16x16x64_bf16 a[212:215], a[240:243], v[64:71], v17\n"
"ds_read_b128 a[156:159], v18 offset:15360\n"
"v_exp_f32 v89, v89\n"
"v_smfmac_f32_32x32x32_bf16 v[112:127], a[140:143], v[72:79], v17\n"
"buffer_store_dwordx2 v[248:249], v18, s[16:19], s59 offen offset:2048 sc1\n"
"v_exp_f32 v90, v90\n"
"v_exp_f32 v91, v91\n"
"v_exp_f32 v92, v92\n"
"v_smfmac_f32_16x16x64_bf16 a[212:215], a[240:243], v[72:79], v17\n"
"s_waitcnt lgkmcnt(3)\n"
"v_exp_f32 v93, v93\n"
"v_exp_f32 v94, v94\n"
"v_exp_f32 v95, v95\n"
"v_mfma_f32_32x32x16_bf16 v[48:63], a[144:147], a[176:179], 0\n"
"s_waitcnt lgkmcnt(0)\n"
"s_barrier\n"
"v_exp_f32 v32, v32\n"
"v_exp_f32 v33, v33\n"
"v_exp_f32 v34, v34\n"
"v_mfma_f32_32x32x16_bf16 v[48:63], a[148:151], a[180:183], v[48:63]\n"
"v_exp_f32 v35, v35\n"
"v_exp_f32 v36, v36\n"
"v_exp_f32 v37, v37\n"
"v_smfmac_f32_32x32x32_bf16 v[96:111], a[152:155], v[80:87], v17\n"
"v_exp_f32 v38, v38\n"
"v_exp_f32 v39, v39\n"
"v_exp_f32 v40, v40\n"
"v_smfmac_f32_16x16x64_bf16 a[208:211], a[240:243], v[80:87], v17\n"
"ds_read_b128 a[128:131], v18 offset:16384\n"
"v_exp_f32 v41, v41\n"
"v_smfmac_f32_32x32x32_bf16 v[96:111], a[156:159], v[88:95], v17\n"
"s_add_u32 m0, s55, 81920\n"
"v_exp_f32 v42, v42\n"
"v_exp_f32 v43, v43\n"
"v_exp_f32 v44, v44\n"
"v_smfmac_f32_16x16x64_bf16 a[208:211], a[240:243], v[88:95], v17\n"
"ds_read_b128 a[132:135], v18 offset:17408\n"
"v_exp_f32 v45, v45\n"
"v_exp_f32 v46, v46\n"
"v_exp_f32 v47, v47\n"
"v_mfma_f32_32x32x16_bf16 v[64:79], a[144:147], a[184:187], 0\n"
"v_exp_f32 v48, v48\n"
"v_exp_f32 v49, v49\n"
"v_exp_f32 v50, v50\n"
"v_mfma_f32_32x32x16_bf16 v[64:79], a[148:151], a[188:191], v[64:79]\n"
"v_exp_f32 v51, v51\n"
"v_exp_f32 v52, v52\n"
"v_exp_f32 v53, v53\n"
"v_smfmac_f32_32x32x32_bf16 v[112:127], a[152:155], v[32:39], v17\n"
"buffer_load_dwordx4 v10, s[20:23], s82 offen offset:0 sc1 lds\n"
"v_exp_f32 v54, v54\n"
"v_exp_f32 v55, v55\n"
"v_exp_f32 v56, v56\n"
"v_smfmac_f32_16x16x64_bf16 a[212:215], a[240:243], v[32:39], v17\n"
"ds_read_b128 a[136:139], v18 offset:18432\n"
"v_exp_f32 v57, v57\n"
"v_smfmac_f32_32x32x32_bf16 v[112:127], a[156:159], v[40:47], v17\n"
"buffer_store_dwordx4 v[156:159], v18, s[16:19], s58 offen offset:3072 sc1\n"
"v_exp_f32 v58, v58\n"
"v_exp_f32 v59, v59\n"
"v_exp_f32 v60, v60\n"
"v_smfmac_f32_16x16x64_bf16 a[212:215], a[240:243], v[40:47], v17\n"
"s_waitcnt lgkmcnt(2)\n"
"ds_read_b128 a[140:143], v18 offset:19456\n"
"v_exp_f32 v61, v61\n"
"v_exp_f32 v62, v62\n"
"v_exp_f32 v63, v63\n"
"v_mfma_f32_32x32x16_bf16 v[80:95], a[128:131], a[176:179], 0\n"
"s_waitcnt lgkmcnt(2)\n"
"v_exp_f32 v64, v64\n"
"v_exp_f32 v65, v65\n"
"v_exp_f32 v66, v66\n"
"v_mfma_f32_32x32x16_bf16 v[80:95], a[132:135], a[180:183], v[80:95]\n"
"s_waitcnt lgkmcnt(1)\n"
"v_exp_f32 v67, v67\n"
"v_exp_f32 v68, v68\n"
"v_exp_f32 v69, v69\n"
"v_smfmac_f32_32x32x32_bf16 v[96:111], a[136:139], v[48:55], v17\n"
"buffer_load_dwordx4 v11, s[20:23], s82 offen offset:1024 sc1 lds\n"
"v_exp_f32 v70, v70\n"
"v_exp_f32 v71, v71\n"
"v_exp_f32 v72, v72\n"
"v_smfmac_f32_16x16x64_bf16 a[208:211], a[240:243], v[48:55], v17\n"
"s_waitcnt lgkmcnt(0)\n"
"ds_read_b128 a[144:147], v18 offset:20480\n"
"v_exp_f32 v73, v73\n"
"v_smfmac_f32_32x32x32_bf16 v[96:111], a[140:143], v[56:63], v17\n"
"v_exp_f32 v74, v74\n"
"v_exp_f32 v75, v75\n"
"v_exp_f32 v76, v76\n"
"v_smfmac_f32_16x16x64_bf16 a[208:211], a[240:243], v[56:63], v17\n"
"ds_read_b128 a[148:151], v18 offset:21504\n"
"v_exp_f32 v77, v77\n"
"v_exp_f32 v78, v78\n"
"v_exp_f32 v79, v79\n"
"v_mfma_f32_32x32x16_bf16 v[32:47], a[128:131], a[184:187], 0\n"
"v_exp_f32 v80, v80\n"
"v_exp_f32 v81, v81\n"
"v_exp_f32 v82, v82\n"
"v_mfma_f32_32x32x16_bf16 v[32:47], a[132:135], a[188:191], v[32:47]\n"
"v_exp_f32 v83, v83\n"
"v_exp_f32 v84, v84\n"
"v_exp_f32 v85, v85\n"
"v_smfmac_f32_32x32x32_bf16 v[112:127], a[136:139], v[64:71], v17\n"
"buffer_load_dwordx4 v12, s[20:23], s82 offen offset:2048 sc1 lds\n"
"v_exp_f32 v86, v86\n"
"v_exp_f32 v87, v87\n"
"v_exp_f32 v88, v88\n"
"v_smfmac_f32_16x16x64_bf16 a[212:215], a[240:243], v[64:71], v17\n"
"ds_read_b128 a[152:155], v18 offset:22528\n"
"v_exp_f32 v89, v89\n"
"v_smfmac_f32_32x32x32_bf16 v[112:127], a[140:143], v[72:79], v17\n"
"buffer_store_dwordx4 v[188:191], v18, s[16:19], s86 offen offset:3072 sc1\n"
"v_exp_f32 v90, v90\n"
"v_exp_f32 v91, v91\n"
"v_exp_f32 v92, v92\n"
"v_smfmac_f32_16x16x64_bf16 a[212:215], a[240:243], v[72:79], v17\n"
"s_waitcnt lgkmcnt(2)\n"
"ds_read_b128 a[156:159], v18 offset:23552\n"
"v_exp_f32 v93, v93\n"
"v_exp_f32 v94, v94\n"
"v_exp_f32 v95, v95\n"
"v_mfma_f32_32x32x16_bf16 v[48:63], a[144:147], a[176:179], 0\n"
"s_waitcnt lgkmcnt(2)\n"
"v_exp_f32 v32, v32\n"
"v_exp_f32 v33, v33\n"
"v_exp_f32 v34, v34\n"
"v_mfma_f32_32x32x16_bf16 v[48:63], a[148:151], a[180:183], v[48:63]\n"
"s_waitcnt lgkmcnt(1)\n"
"v_exp_f32 v35, v35\n"
"v_exp_f32 v36, v36\n"
"v_exp_f32 v37, v37\n"
"v_smfmac_f32_32x32x32_bf16 v[96:111], a[152:155], v[80:87], v17\n"
"buffer_load_dwordx4 v13, s[20:23], s82 offen offset:3072 sc1 lds\n"
"v_exp_f32 v38, v38\n"
"v_exp_f32 v39, v39\n"
"v_exp_f32 v40, v40\n"
"v_smfmac_f32_16x16x64_bf16 a[208:211], a[240:243], v[80:87], v17\n"
"s_waitcnt lgkmcnt(0)\n"
"ds_read_b128 a[128:131], v18 offset:24576\n"
"v_exp_f32 v41, v41\n"
"v_smfmac_f32_32x32x32_bf16 v[96:111], a[156:159], v[88:95], v17\n"
"v_exp_f32 v42, v42\n"
"v_exp_f32 v43, v43\n"
"v_exp_f32 v44, v44\n"
"v_smfmac_f32_16x16x64_bf16 a[208:211], a[240:243], v[88:95], v17\n"
"ds_read_b128 a[132:135], v18 offset:25600\n"
"v_exp_f32 v45, v45\n"
"v_exp_f32 v46, v46\n"
"v_exp_f32 v47, v47\n"
"v_mfma_f32_32x32x16_bf16 v[64:79], a[144:147], a[184:187], 0\n"
"s_add_u32 s82, s82, s53\n"
"v_exp_f32 v48, v48\n"
"v_exp_f32 v49, v49\n"
"v_exp_f32 v50, v50\n"
"v_mfma_f32_32x32x16_bf16 v[64:79], a[148:151], a[188:191], v[64:79]\n"
"v_exp_f32 v51, v51\n"
"v_exp_f32 v52, v52\n"
"v_exp_f32 v53, v53\n"
"v_smfmac_f32_32x32x32_bf16 v[112:127], a[152:155], v[32:39], v17\n"
"v_exp_f32 v54, v54\n"
"v_exp_f32 v55, v55\n"
"v_exp_f32 v56, v56\n"
"v_smfmac_f32_16x16x64_bf16 a[212:215], a[240:243], v[32:39], v17\n"
"ds_read_b128 a[136:139], v18 offset:26624\n"
"v_exp_f32 v57, v57\n"
"v_smfmac_f32_32x32x32_bf16 v[112:127], a[156:159], v[40:47], v17\n"
"buffer_store_dwordx4 v[220:223], v18, s[16:19], s87 offen offset:3072 sc1\n"
"v_exp_f32 v58, v58\n"
"v_exp_f32 v59, v59\n"
"v_exp_f32 v60, v60\n"
"v_smfmac_f32_16x16x64_bf16 a[212:215], a[240:243], v[40:47], v17\n"
"s_waitcnt lgkmcnt(2)\n"
"ds_read_b128 a[140:143], v18 offset:27648\n"
"v_exp_f32 v61, v61\n"
"v_exp_f32 v62, v62\n"
"v_exp_f32 v63, v63\n"
"v_mfma_f32_32x32x16_bf16 v[80:95], a[128:131], a[176:179], 0\n"
"s_waitcnt lgkmcnt(2)\n"
"v_exp_f32 v64, v64\n"
"v_exp_f32 v65, v65\n"
"v_exp_f32 v66, v66\n"
"v_mfma_f32_32x32x16_bf16 v[80:95], a[132:135], a[180:183], v[80:95]\n"
"s_waitcnt lgkmcnt(1)\n"
"v_exp_f32 v67, v67\n"
"v_exp_f32 v68, v68\n"
"v_exp_f32 v69, v69\n"
"v_smfmac_f32_32x32x32_bf16 v[96:111], a[136:139], v[48:55], v17\n"
"v_exp_f32 v70, v70\n"
"v_exp_f32 v71, v71\n"
"v_exp_f32 v72, v72\n"
"v_smfmac_f32_16x16x64_bf16 a[208:211], a[240:243], v[48:55], v17\n"
"s_waitcnt lgkmcnt(0)\n"
"ds_read_b128 a[144:147], v18 offset:28672\n"
"v_exp_f32 v73, v73\n"
"v_smfmac_f32_32x32x32_bf16 v[96:111], a[140:143], v[56:63], v17\n"
"v_exp_f32 v74, v74\n"
"v_exp_f32 v75, v75\n"
"v_exp_f32 v76, v76\n"
"v_smfmac_f32_16x16x64_bf16 a[208:211], a[240:243], v[56:63], v17\n"
"ds_read_b128 a[148:151], v18 offset:29696\n"
"v_exp_f32 v77, v77\n"
"v_exp_f32 v78, v78\n"
"v_exp_f32 v79, v79\n"
"v_mfma_f32_32x32x16_bf16 v[32:47], a[128:131], a[184:187], 0\n"
"v_exp_f32 v80, v80\n"
"v_exp_f32 v81, v81\n"
"v_exp_f32 v82, v82\n"
"v_mfma_f32_32x32x16_bf16 v[32:47], a[132:135], a[188:191], v[32:47]\n"
"v_exp_f32 v83, v83\n"
"v_exp_f32 v84, v84\n"
"v_exp_f32 v85, v85\n"
"v_smfmac_f32_32x32x32_bf16 v[112:127], a[136:139], v[64:71], v17\n"
"v_exp_f32 v86, v86\n"
"v_exp_f32 v87, v87\n"
"v_exp_f32 v88, v88\n"
"v_smfmac_f32_16x16x64_bf16 a[212:215], a[240:243], v[64:71], v17\n"
"ds_read_b128 a[152:155], v18 offset:30720\n"
"v_exp_f32 v89, v89\n"
"v_smfmac_f32_32x32x32_bf16 v[112:127], a[140:143], v[72:79], v17\n"
"buffer_store_dwordx2 v[252:253], v18, s[16:19], s59 offen offset:3072 sc1\n"
"v_exp_f32 v90, v90\n"
"v_exp_f32 v91, v91\n"
"v_exp_f32 v92, v92\n"
"v_smfmac_f32_16x16x64_bf16 a[212:215], a[240:243], v[72:79], v17\n"
"s_waitcnt lgkmcnt(2)\n"
"ds_read_b128 a[156:159], v18 offset:31744\n"
"v_exp_f32 v93, v93\n"
"v_exp_f32 v94, v94\n"
"v_exp_f32 v95, v95\n"
"v_mfma_f32_32x32x16_bf16 v[48:63], a[144:147], a[176:179], 0\n"
"s_waitcnt lgkmcnt(2)\n"
"v_exp_f32 v32, v32\n"
"v_exp_f32 v33, v33\n"
"v_exp_f32 v34, v34\n"
"v_mfma_f32_32x32x16_bf16 v[48:63], a[148:151], a[180:183], v[48:63]\n"
"s_waitcnt lgkmcnt(1)\n"
"v_exp_f32 v35, v35\n"
"v_exp_f32 v36, v36\n"
"v_exp_f32 v37, v37\n"
"v_smfmac_f32_32x32x32_bf16 v[96:111], a[152:155], v[80:87], v17\n"
"v_exp_f32 v38, v38\n"
"v_exp_f32 v39, v39\n"
"v_exp_f32 v40, v40\n"
"v_smfmac_f32_16x16x64_bf16 a[208:211], a[240:243], v[80:87], v17\n"
"s_waitcnt lgkmcnt(0)\n"
"ds_read_b128 a[128:131], v18 offset:32768\n"
"v_exp_f32 v41, v41\n"
"v_smfmac_f32_32x32x32_bf16 v[96:111], a[156:159], v[88:95], v17\n"
"s_add_u32 m0, s55, 0\n"
"v_exp_f32 v42, v42\n"
"v_exp_f32 v43, v43\n"
"v_exp_f32 v44, v44\n"
"v_smfmac_f32_16x16x64_bf16 a[208:211], a[240:243], v[88:95], v17\n"
"ds_read_b128 a[132:135], v18 offset:33792\n"
"v_exp_f32 v45, v45\n"
"v_exp_f32 v46, v46\n"
"v_exp_f32 v47, v47\n"
"v_mfma_f32_32x32x16_bf16 v[64:79], a[144:147], a[184:187], 0\n"
"v_exp_f32 v48, v48\n"
"v_exp_f32 v49, v49\n"
"v_exp_f32 v50, v50\n"
"v_mfma_f32_32x32x16_bf16 v[64:79], a[148:151], a[188:191], v[64:79]\n"
"v_exp_f32 v51, v51\n"
"v_exp_f32 v52, v52\n"
"v_exp_f32 v53, v53\n"
"v_smfmac_f32_32x32x32_bf16 v[112:127], a[152:155], v[32:39], v17\n"
"buffer_load_dwordx4 v24, s[16:19], s12 offen offset:0 sc1 lds\n"
"v_exp_f32 v54, v54\n"
"v_exp_f32 v55, v55\n"
"v_exp_f32 v56, v56\n"
"v_smfmac_f32_16x16x64_bf16 a[212:215], a[240:243], v[32:39], v17\n"
"ds_read_b128 a[136:139], v18 offset:34816\n"
"v_exp_f32 v57, v57\n"
"v_smfmac_f32_32x32x32_bf16 v[112:127], a[156:159], v[40:47], v17\n"
"v_exp_f32 v58, v58\n"
"v_exp_f32 v59, v59\n"
"v_exp_f32 v60, v60\n"
"v_smfmac_f32_16x16x64_bf16 a[212:215], a[240:243], v[40:47], v17\n"
"s_waitcnt lgkmcnt(2)\n"
"ds_read_b128 a[140:143], v18 offset:35840\n"
"v_exp_f32 v61, v61\n"
"v_exp_f32 v62, v62\n"
"v_exp_f32 v63, v63\n"
"v_mfma_f32_32x32x16_bf16 v[80:95], a[128:131], a[176:179], 0\n"
"s_waitcnt lgkmcnt(2)\n"
"v_exp_f32 v64, v64\n"
"v_exp_f32 v65, v65\n"
"v_exp_f32 v66, v66\n"
"v_mfma_f32_32x32x16_bf16 v[80:95], a[132:135], a[180:183], v[80:95]\n"
"s_waitcnt lgkmcnt(1)\n"
"v_exp_f32 v67, v67\n"
"v_exp_f32 v68, v68\n"
"v_exp_f32 v69, v69\n"
"v_smfmac_f32_32x32x32_bf16 v[96:111], a[136:139], v[48:55], v17\n"
"buffer_load_dwordx4 v24, s[16:19], s12 offen offset:1024 sc1 lds\n"
"v_exp_f32 v70, v70\n"
"v_exp_f32 v71, v71\n"
"v_exp_f32 v72, v72\n"
"v_smfmac_f32_16x16x64_bf16 a[208:211], a[240:243], v[48:55], v17\n"
"s_waitcnt lgkmcnt(0)\n"
"ds_read_b128 a[144:147], v18 offset:36864\n"
"v_exp_f32 v73, v73\n"
"v_smfmac_f32_32x32x32_bf16 v[96:111], a[140:143], v[56:63], v17\n"
"s_mov_b32 s85, 65536\n"
"v_accvgpr_read_b32 v6, a245\n"
"v_exp_f32 v74, v74\n"
"v_exp_f32 v75, v75\n"
"v_exp_f32 v76, v76\n"
"v_smfmac_f32_16x16x64_bf16 a[208:211], a[240:243], v[56:63], v17\n"
"ds_read_b128 a[148:151], v18 offset:37888\n"
"v_exp_f32 v77, v77\n"
"v_exp_f32 v78, v78\n"
"v_exp_f32 v79, v79\n"
"v_mfma_f32_32x32x16_bf16 v[32:47], a[128:131], a[184:187], 0\n"
"v_exp_f32 v80, v80\n"
"v_exp_f32 v81, v81\n"
"v_exp_f32 v82, v82\n"
"v_mfma_f32_32x32x16_bf16 v[32:47], a[132:135], a[188:191], v[32:47]\n"
"v_exp_f32 v83, v83\n"
"v_exp_f32 v84, v84\n"
"v_exp_f32 v85, v85\n"
"v_smfmac_f32_32x32x32_bf16 v[112:127], a[136:139], v[64:71], v17\n"
"buffer_load_dwordx4 v24, s[16:19], s12 offen offset:2048 sc1 lds\n"
"v_exp_f32 v86, v86\n"
"v_exp_f32 v87, v87\n"
"v_exp_f32 v88, v88\n"
"v_smfmac_f32_16x16x64_bf16 a[212:215], a[240:243], v[64:71], v17\n"
"ds_read_b128 a[152:155], v18 offset:38912\n"
"v_exp_f32 v89, v89\n"
"v_smfmac_f32_32x32x32_bf16 v[112:127], a[140:143], v[72:79], v17\n"
"s_mov_b32 s91, 65536\n"
"v_xor_b32 v7, 32, v6\n"
"v_xor_b32 v8, 64, v6\n"
"v_xor_b32 v9, 96, v6\n"
"v_exp_f32 v90, v90\n"
"v_exp_f32 v91, v91\n"
"v_exp_f32 v92, v92\n"
"v_smfmac_f32_16x16x64_bf16 a[212:215], a[240:243], v[72:79], v17\n"
"s_waitcnt lgkmcnt(2)\n"
"ds_read_b128 a[156:159], v18 offset:39936\n"
"v_exp_f32 v93, v93\n"
"v_exp_f32 v94, v94\n"
"v_exp_f32 v95, v95\n"
"v_mfma_f32_32x32x16_bf16 v[48:63], a[144:147], a[176:179], 0\n"
"s_waitcnt lgkmcnt(2)\n"
"v_exp_f32 v32, v32\n"
"v_exp_f32 v33, v33\n"
"v_exp_f32 v34, v34\n"
"v_mfma_f32_32x32x16_bf16 v[48:63], a[148:151], a[180:183], v[48:63]\n"
"s_waitcnt lgkmcnt(1)\n"
"v_exp_f32 v35, v35\n"
"v_exp_f32 v36, v36\n"
"v_exp_f32 v37, v37\n"
"v_smfmac_f32_32x32x32_bf16 v[96:111], a[152:155], v[80:87], v17\n"
"buffer_load_dwordx4 v24, s[16:19], s12 offen offset:3072 sc1 lds\n"
"v_exp_f32 v38, v38\n"
"v_exp_f32 v39, v39\n"
"v_exp_f32 v40, v40\n"
"v_smfmac_f32_16x16x64_bf16 a[208:211], a[240:243], v[80:87], v17\n"
"s_waitcnt lgkmcnt(0)\n"
"ds_read_b128 a[128:131], v18 offset:40960\n"
"v_exp_f32 v41, v41\n"
"v_smfmac_f32_32x32x32_bf16 v[96:111], a[156:159], v[88:95], v17\n"
"v_add_u32 v2, s85, v6\n"
"v_add_u32 v3, s85, v7\n"
"v_add_u32 v4, s85, v8\n"
"v_add_u32 v5, s85, v9\n"
"v_exp_f32 v42, v42\n"
"v_exp_f32 v43, v43\n"
"v_exp_f32 v44, v44\n"
"v_smfmac_f32_16x16x64_bf16 a[208:211], a[240:243], v[88:95], v17\n"
"ds_read_b128 a[132:135], v18 offset:41984\n"
"v_exp_f32 v45, v45\n"
"v_exp_f32 v46, v46\n"
"v_exp_f32 v47, v47\n"
"v_mfma_f32_32x32x16_bf16 v[64:79], a[144:147], a[184:187], 0\n"
"s_add_u32 s12, s12, 16384\n"
"v_exp_f32 v48, v48\n"
"v_exp_f32 v49, v49\n"
"v_exp_f32 v50, v50\n"
"v_mfma_f32_32x32x16_bf16 v[64:79], a[148:151], a[188:191], v[64:79]\n"
"v_exp_f32 v51, v51\n"
"v_exp_f32 v52, v52\n"
"v_exp_f32 v53, v53\n"
"v_smfmac_f32_32x32x32_bf16 v[112:127], a[152:155], v[32:39], v17\n"
"v_exp_f32 v54, v54\n"
"v_exp_f32 v55, v55\n"
"v_exp_f32 v56, v56\n"
"v_smfmac_f32_16x16x64_bf16 a[212:215], a[240:243], v[32:39], v17\n"
"ds_read_b128 a[136:139], v18 offset:43008\n"
"v_exp_f32 v57, v57\n"
"v_smfmac_f32_32x32x32_bf16 v[112:127], a[156:159], v[40:47], v17\n"
"s_add_u32 m0, s55, 98304\n"
"v_exp_f32 v58, v58\n"
"v_exp_f32 v59, v59\n"
"v_exp_f32 v60, v60\n"
"v_smfmac_f32_16x16x64_bf16 a[212:215], a[240:243], v[40:47], v17\n"
"s_waitcnt lgkmcnt(2)\n"
"ds_read_b128 a[140:143], v18 offset:44032\n"
"v_exp_f32 v61, v61\n"
"v_exp_f32 v62, v62\n"
"v_exp_f32 v63, v63\n"
"v_mfma_f32_32x32x16_bf16 v[80:95], a[128:131], a[176:179], 0\n"
"s_waitcnt lgkmcnt(2)\n"
"v_exp_f32 v64, v64\n"
"v_exp_f32 v65, v65\n"
"v_exp_f32 v66, v66\n"
"v_mfma_f32_32x32x16_bf16 v[80:95], a[132:135], a[180:183], v[80:95]\n"
"s_waitcnt lgkmcnt(1)\n"
"v_exp_f32 v67, v67\n"
"v_exp_f32 v68, v68\n"
"v_exp_f32 v69, v69\n"
"v_smfmac_f32_32x32x32_bf16 v[96:111], a[136:139], v[48:55], v17\n"
"buffer_load_dwordx4 v10, s[20:23], s82 offen offset:0 sc1 lds\n"
"v_exp_f32 v70, v70\n"
"v_exp_f32 v71, v71\n"
"v_exp_f32 v72, v72\n"
"v_smfmac_f32_16x16x64_bf16 a[208:211], a[240:243], v[48:55], v17\n"
"s_waitcnt lgkmcnt(0)\n"
"ds_read_b128 a[144:147], v18 offset:45056\n"
"v_exp_f32 v73, v73\n"
"v_smfmac_f32_32x32x32_bf16 v[96:111], a[140:143], v[56:63], v17\n"
"v_lshrrev_b32 v25, 1, v18\n"
"v_and_b32 v25, 0xf8, v25\n"
"buffer_load_dwordx4 v11, s[20:23], s82 offen offset:1024 sc1 lds\n"
"v_exp_f32 v74, v74\n"
"v_exp_f32 v75, v75\n"
"v_exp_f32 v76, v76\n"
"v_smfmac_f32_16x16x64_bf16 a[208:211], a[240:243], v[56:63], v17\n"
"ds_read_b128 a[148:151], v18 offset:46080\n"
"v_exp_f32 v77, v77\n"
"v_exp_f32 v78, v78\n"
"v_exp_f32 v79, v79\n"
"v_mfma_f32_32x32x16_bf16 v[32:47], a[128:131], a[184:187], 0\n"
"v_exp_f32 v80, v80\n"
"v_exp_f32 v81, v81\n"
"v_exp_f32 v82, v82\n"
"v_mfma_f32_32x32x16_bf16 v[32:47], a[132:135], a[188:191], v[32:47]\n"
"ds_read_b128 a[152:155], v18 offset:47104\n"
"v_exp_f32 v83, v83\n"
"v_exp_f32 v84, v84\n"
"v_exp_f32 v85, v85\n"
"v_smfmac_f32_32x32x32_bf16 v[112:127], a[136:139], v[64:71], v17\n"
"buffer_load_dwordx4 v12, s[20:23], s82 offen offset:2048 sc1 lds\n"
"v_exp_f32 v86, v86\n"
"v_exp_f32 v87, v87\n"
"v_exp_f32 v88, v88\n"
"v_smfmac_f32_16x16x64_bf16 a[212:215], a[240:243], v[64:71], v17\n"
"ds_read_b128 a[156:159], v18 offset:48128\n"
"v_exp_f32 v89, v89\n"
"v_smfmac_f32_32x32x32_bf16 v[112:127], a[140:143], v[72:79], v17\n"
"buffer_load_dwordx4 v13, s[20:23], s82 offen offset:3072 sc1 lds\n"
"s_add_u32 s82, s82, s53\n"
"v_exp_f32 v90, v90\n"
"v_exp_f32 v91, v91\n"
"v_exp_f32 v92, v92\n"
"v_smfmac_f32_16x16x64_bf16 a[212:215], a[240:243], v[72:79], v17\n"
"s_waitcnt lgkmcnt(3)\n"
"v_exp_f32 v93, v93\n"
"v_exp_f32 v94, v94\n"
"v_exp_f32 v95, v95\n"
"v_mfma_f32_32x32x16_bf16 v[48:63], a[144:147], a[176:179], 0\n"
"s_waitcnt vmcnt(4) lgkmcnt(0)\n"
"s_barrier\n"
"v_exp_f32 v32, v32\n"
"v_exp_f32 v33, v33\n"
"v_exp_f32 v34, v34\n"
"v_mfma_f32_32x32x16_bf16 v[48:63], a[148:151], a[180:183], v[48:63]\n"
"v_exp_f32 v35, v35\n"
"v_exp_f32 v36, v36\n"
"v_exp_f32 v37, v37\n"
"v_smfmac_f32_32x32x32_bf16 v[96:111], a[152:155], v[80:87], v17\n"
"v_exp_f32 v38, v38\n"
"v_exp_f32 v39, v39\n"
"v_exp_f32 v40, v40\n"
"v_smfmac_f32_16x16x64_bf16 a[208:211], a[240:243], v[80:87], v17\n"
"ds_read_b128 a[128:131], v18 offset:49152\n"
"v_exp_f32 v41, v41\n"
"v_smfmac_f32_32x32x32_bf16 v[96:111], a[156:159], v[88:95], v17\n"
"s_cmp_lg_u32 s48, 0\n"
"s_cbranch_scc1 Lph_%=\n"
"v_mov_b32 v28, s80\n"
"v_mov_b32 v29, s81\n"
"s_add_u32 s95, s60, 4589568\n"
"s_mov_b64 exec, 1\n"
"buffer_store_dwordx2 v[28:29], off, s[16:19], s95 sc1\n"
"s_mov_b64 exec, -1\n"
"s_cmp_lg_u32 s61, 0\n"
"s_cbranch_scc1 Lph_%=\n"
"s_mov_b32 s90, 5115904\n"
"v_mov_b32 v28, s80\n"
"v_mov_b32 v29, s81\n"
"s_mov_b64 exec, 1\n"
"buffer_store_dwordx2 v[28:29], off, s[16:19], s90 sc1\n"
"s_mov_b64 exec, -1\n"
"Lph_%=:\n"
"v_exp_f32 v42, v42\n"
"v_exp_f32 v43, v43\n"
"v_exp_f32 v44, v44\n"
"v_smfmac_f32_16x16x64_bf16 a[208:211], a[240:243], v[88:95], v17\n"
"ds_read_b128 a[132:135], v18 offset:50176\n"
"v_exp_f32 v45, v45\n"
"v_exp_f32 v46, v46\n"
"v_exp_f32 v47, v47\n"
"v_mfma_f32_32x32x16_bf16 v[64:79], a[144:147], a[184:187], 0\n"
"v_accvgpr_read_b32 v16, a246\n"
"v_exp_f32 v48, v48\n"
"v_exp_f32 v49, v49\n"
"v_exp_f32 v50, v50\n"
"v_mfma_f32_32x32x16_bf16 v[64:79], a[148:151], a[188:191], v[64:79]\n"
"v_exp_f32 v51, v51\n"
"v_exp_f32 v52, v52\n"
"v_exp_f32 v53, v53\n"
"v_mfma_f32_32x32x16_bf16 v[144:159], v[20:23], v[20:23], 0\n"
"s_add_u32 s86, s55, 131072\n"
"ds_read_b64_tr_b16 a[0:1], v2 offset:0\n"
"v_exp_f32 v54, v54\n"
"v_exp_f32 v55, v55\n"
"v_exp_f32 v56, v56\n"
"v_smfmac_f32_32x32x32_bf16 v[112:127], a[152:155], v[32:39], v17\n"
"v_exp_f32 v57, v57\n"
"v_exp_f32 v58, v58\n"
"v_exp_f32 v59, v59\n"
"v_smfmac_f32_16x16x64_bf16 a[212:215], a[240:243], v[32:39], v17\n"
"ds_read_b128 a[136:139], v18 offset:51200\n"
"v_smfmac_f32_32x32x32_bf16 v[112:127], a[156:159], v[40:47], v17\n"
"s_add_u32 m0, s55, 16384\n"
"v_exp_f32 v60, v60\n"
"v_exp_f32 v61, v61\n"
"v_exp_f32 v62, v62\n"
"v_smfmac_f32_16x16x64_bf16 a[212:215], a[240:243], v[40:47], v17\n"
"s_waitcnt lgkmcnt(3)\n"
"s_mov_b32 s90, 32768\n"
"ds_read_b128 a[140:143], v18 offset:52224\n"
"v_exp_f32 v63, v63\n"
"v_mfma_f32_32x32x16_bf16 v[80:95], a[128:131], a[176:179], 0\n"
"s_waitcnt lgkmcnt(3)\n"
"v_exp_f32 v64, v64\n"
"v_exp_f32 v65, v65\n"
"v_exp_f32 v66, v66\n"
"v_mfma_f32_32x32x16_bf16 v[80:95], a[132:135], a[180:183], v[80:95]\n"
"v_exp_f32 v67, v67\n"
"v_exp_f32 v68, v68\n"
"v_exp_f32 v69, v69\n"
"v_mfma_f32_32x32x16_bf16 v[176:191], v[20:23], v[20:23], 0\n"
"s_waitcnt lgkmcnt(1)\n"
"s_add_u32 s87, s55, 163840\n"
"ds_read_b64_tr_b16 a[2:3], v2 offset:8192\n"
"v_exp_f32 v70, v70\n"
"v_exp_f32 v71, v71\n"
"v_exp_f32 v72, v72\n"
"v_smfmac_f32_32x32x32_bf16 v[96:111], a[136:139], v[48:55], v17\n"
"buffer_load_dwordx4 v24, s[16:19], s12 offen offset:0 sc1 lds\n"
"v_exp_f32 v73, v73\n"
"v_exp_f32 v74, v74\n"
"v_exp_f32 v75, v75\n"
"v_smfmac_f32_16x16x64_bf16 a[208:211], a[240:243], v[48:55], v17\n"
"s_waitcnt lgkmcnt(1)\n"
"ds_read_b128 a[144:147], v18 offset:53248\n"
"v_smfmac_f32_32x32x32_bf16 v[96:111], a[140:143], v[56:63], v17\n"
"buffer_load_dwordx4 a[160:163], v16, s[40:43], 0 offen offset:0\n"
"v_exp_f32 v76, v76\n"
"v_exp_f32 v77, v77\n"
"v_exp_f32 v78, v78\n"
"v_smfmac_f32_16x16x64_bf16 a[208:211], a[240:243], v[56:63], v17\n"
"ds_read_b128 a[148:151], v18 offset:54272\n"
"v_exp_f32 v79, v79\n"
"v_mfma_f32_32x32x16_bf16 v[32:47], a[128:131], a[184:187], 0\n"
"v_exp_f32 v80, v80\n"
"v_exp_f32 v81, v81\n"
"v_exp_f32 v82, v82\n"
"v_mfma_f32_32x32x16_bf16 v[32:47], a[132:135], a[188:191], v[32:47]\n"
"v_exp_f32 v83, v83\n"
"v_exp_f32 v84, v84\n"
"v_exp_f32 v85, v85\n"
"v_mfma_f32_32x32x16_bf16 v[192:207], v[20:23], v[20:23], 0\n"
"s_mov_b32 s84, 6\n"
"ds_read_b64_tr_b16 a[4:5], v3 offset:0\n"
"v_exp_f32 v86, v86\n"
"v_exp_f32 v87, v87\n"
"v_exp_f32 v88, v88\n"
"v_smfmac_f32_32x32x32_bf16 v[112:127], a[136:139], v[64:71], v17\n"
"buffer_load_dwordx4 v24, s[16:19], s12 offen offset:1024 sc1 lds\n"
"v_exp_f32 v89, v89\n"
"v_exp_f32 v90, v90\n"
"v_exp_f32 v91, v91\n"
"v_smfmac_f32_16x16x64_bf16 a[212:215], a[240:243], v[64:71], v17\n"
"ds_read_b128 a[152:155], v18 offset:55296\n"
"v_smfmac_f32_32x32x32_bf16 v[112:127], a[140:143], v[72:79], v17\n"
"buffer_load_dwordx4 a[164:167], v16, s[40:43], 0 offen offset:64\n"
"v_exp_f32 v92, v92\n"
"v_exp_f32 v93, v93\n"
"v_exp_f32 v94, v94\n"
"v_smfmac_f32_16x16x64_bf16 a[212:215], a[240:243], v[72:79], v17\n"
"s_waitcnt lgkmcnt(3)\n"
"ds_read_b128 a[156:159], v18 offset:56320\n"
"v_exp_f32 v95, v95\n"
"v_mfma_f32_32x32x16_bf16 v[48:63], a[144:147], a[176:179], 0\n"
"s_waitcnt lgkmcnt(3)\n"
"v_exp_f32 v32, v32\n"
"v_exp_f32 v33, v33\n"
"v_exp_f32 v34, v34\n"
"v_mfma_f32_32x32x16_bf16 v[48:63], a[148:151], a[180:183], v[48:63]\n"
"v_exp_f32 v35, v35\n"
"v_exp_f32 v36, v36\n"
"v_exp_f32 v37, v37\n"
"v_mfma_f32_32x32x16_bf16 v[208:223], v[20:23], v[20:23], 0\n"
"s_waitcnt lgkmcnt(1)\n"
"s_mov_b32 s89, 32768\n"
"ds_read_b64_tr_b16 a[6:7], v3 offset:8192\n"
"v_exp_f32 v38, v38\n"
"v_exp_f32 v39, v39\n"
"v_exp_f32 v40, v40\n"
"v_smfmac_f32_32x32x32_bf16 v[96:111], a[152:155], v[80:87], v17\n"
"buffer_load_dwordx4 v24, s[16:19], s12 offen offset:2048 sc1 lds\n"
"v_exp_f32 v41, v41\n"
"v_exp_f32 v42, v42\n"
"v_exp_f32 v43, v43\n"
"v_smfmac_f32_16x16x64_bf16 a[208:211], a[240:243], v[80:87], v17\n"
"s_waitcnt lgkmcnt(1)\n"
"ds_read_b128 a[128:131], v18 offset:57344\n"
"v_smfmac_f32_32x32x32_bf16 v[96:111], a[156:159], v[88:95], v17\n"
"buffer_load_dwordx4 a[168:171], v16, s[40:43], s90 offen offset:0\n"
"v_exp_f32 v44, v44\n"
"v_exp_f32 v45, v45\n"
"v_exp_f32 v46, v46\n"
"v_smfmac_f32_16x16x64_bf16 a[208:211], a[240:243], v[88:95], v17\n"
"ds_read_b128 a[132:135], v18 offset:58368\n"
"v_exp_f32 v47, v47\n"
"v_mfma_f32_32x32x16_bf16 v[64:79], a[144:147], a[184:187], 0\n"
"v_exp_f32 v48, v48\n"
"v_exp_f32 v49, v49\n"
"v_exp_f32 v50, v50\n"
"v_mfma_f32_32x32x16_bf16 v[64:79], a[148:151], a[188:191], v[64:79]\n"
"v_exp_f32 v51, v51\n"
"v_exp_f32 v52, v52\n"
"v_exp_f32 v53, v53\n"
"v_mfma_f32_32x32x16_bf16 v[224:239], v[20:23], v[20:23], 0\n"
"s_lshr_b32 s95, s78, 14\n"
"ds_read_b64_tr_b16 a[8:9], v4 offset:0\n"
"v_exp_f32 v54, v54\n"
"v_exp_f32 v55, v55\n"
"v_exp_f32 v56, v56\n"
"v_smfmac_f32_32x32x32_bf16 v[112:127], a[152:155], v[32:39], v17\n"
"buffer_load_dwordx4 v24, s[16:19], s12 offen offset:3072 sc1 lds\n"
"v_exp_f32 v57, v57\n"
"v_exp_f32 v58, v58\n"
"v_exp_f32 v59, v59\n"
"v_smfmac_f32_16x16x64_bf16 a[212:215], a[240:243], v[32:39], v17\n"
"ds_read_b128 a[136:139], v18 offset:59392\n"
"v_smfmac_f32_32x32x32_bf16 v[112:127], a[156:159], v[40:47], v17\n"
"buffer_load_dwordx4 a[172:175], v16, s[40:43], s90 offen offset:64\n"
"v_exp_f32 v60, v60\n"
"v_exp_f32 v61, v61\n"
"v_exp_f32 v62, v62\n"
"v_smfmac_f32_16x16x64_bf16 a[212:215], a[240:243], v[40:47], v17\n"
"s_waitcnt lgkmcnt(3)\n"
"ds_read_b128 a[140:143], v18 offset:60416\n"
"v_exp_f32 v63, v63\n"
"v_mfma_f32_32x32x16_bf16 v[80:95], a[128:131], a[176:179], 0\n"
"s_waitcnt lgkmcnt(3)\n"
"s_add_u32 s12, s12, 16384\n"
"v_exp_f32 v64, v64\n"
"v_exp_f32 v65, v65\n"
"v_exp_f32 v66, v66\n"
"v_mfma_f32_32x32x16_bf16 v[80:95], a[132:135], a[180:183], v[80:95]\n"
"v_exp_f32 v67, v67\n"
"v_exp_f32 v68, v68\n"
"v_exp_f32 v69, v69\n"
"v_mfma_f32_32x32x16_bf16 v[240:255], v[20:23], v[20:23], 0\n"
"s_waitcnt lgkmcnt(1)\n"
"s_lshl_b32 s95, s95, 13\n"
"ds_read_b64_tr_b16 a[10:11], v4 offset:8192\n"
"v_exp_f32 v70, v70\n"
"v_exp_f32 v71, v71\n"
"v_exp_f32 v72, v72\n"
"v_smfmac_f32_32x32x32_bf16 v[96:111], a[136:139], v[48:55], v17\n"
"v_exp_f32 v73, v73\n"
"v_exp_f32 v74, v74\n"
"v_exp_f32 v75, v75\n"
"v_smfmac_f32_16x16x64_bf16 a[208:211], a[240:243], v[48:55], v17\n"
"s_waitcnt lgkmcnt(1)\n"
"ds_read_b128 a[144:147], v18 offset:61440\n"
"v_smfmac_f32_32x32x32_bf16 v[96:111], a[140:143], v[56:63], v17\n"
"s_add_u32 m0, s55, 114688\n"
"v_exp_f32 v76, v76\n"
"v_exp_f32 v77, v77\n"
"v_exp_f32 v78, v78\n"
"v_smfmac_f32_16x16x64_bf16 a[208:211], a[240:243], v[56:63], v17\n"
"ds_read_b128 a[148:151], v18 offset:62464\n"
"v_exp_f32 v79, v79\n"
"v_mfma_f32_32x32x16_bf16 v[32:47], a[128:131], a[184:187], 0\n"
"v_exp_f32 v80, v80\n"
"v_exp_f32 v81, v81\n"
"v_exp_f32 v82, v82\n"
"v_mfma_f32_32x32x16_bf16 v[32:47], a[132:135], a[188:191], v[32:47]\n"
"v_exp_f32 v83, v83\n"
"v_exp_f32 v84, v84\n"
"v_exp_f32 v85, v85\n"
"v_smfmac_f32_32x32x32_bf16 v[112:127], a[136:139], v[64:71], v17\n"
"buffer_load_dwordx4 v10, s[20:23], s82 offen offset:0 sc1 lds\n"
"v_exp_f32 v86, v86\n"
"v_exp_f32 v87, v87\n"
"v_exp_f32 v88, v88\n"
"v_smfmac_f32_16x16x64_bf16 a[212:215], a[240:243], v[64:71], v17\n"
"ds_read_b128 a[152:155], v18 offset:63488\n"
"v_exp_f32 v89, v89\n"
"v_smfmac_f32_32x32x32_bf16 v[112:127], a[140:143], v[72:79], v17\n"
"buffer_load_dwordx4 v11, s[20:23], s82 offen offset:1024 sc1 lds\n"
"v_exp_f32 v90, v90\n"
"v_exp_f32 v91, v91\n"
"v_exp_f32 v92, v92\n"
"v_smfmac_f32_16x16x64_bf16 a[212:215], a[240:243], v[72:79], v17\n"
"s_waitcnt lgkmcnt(2)\n"
"ds_read_b128 a[156:159], v18 offset:64512\n"
"v_exp_f32 v93, v93\n"
"v_exp_f32 v94, v94\n"
"v_exp_f32 v95, v95\n"
"v_mfma_f32_32x32x16_bf16 v[48:63], a[144:147], a[176:179], 0\n"
"s_waitcnt lgkmcnt(2)\n"
"v_exp_f32 v32, v32\n"
"v_exp_f32 v33, v33\n"
"v_exp_f32 v34, v34\n"
"v_mfma_f32_32x32x16_bf16 v[48:63], a[148:151], a[180:183], v[48:63]\n"
"s_waitcnt lgkmcnt(1)\n"
"v_exp_f32 v35, v35\n"
"v_exp_f32 v36, v36\n"
"v_exp_f32 v37, v37\n"
"v_smfmac_f32_32x32x32_bf16 v[96:111], a[152:155], v[80:87], v17\n"
"buffer_load_dwordx4 v12, s[20:23], s82 offen offset:2048 sc1 lds\n"
"v_exp_f32 v38, v38\n"
"v_exp_f32 v39, v39\n"
"v_exp_f32 v40, v40\n"
"v_smfmac_f32_16x16x64_bf16 a[208:211], a[240:243], v[80:87], v17\n"
"s_waitcnt lgkmcnt(0)\n"
"ds_read_b128 a[128:131], v18 offset:0\n"
"v_exp_f32 v41, v41\n"
"v_smfmac_f32_32x32x32_bf16 v[96:111], a[156:159], v[88:95], v17\n"
"buffer_load_dwordx4 v13, s[20:23], s82 offen offset:3072 sc1 lds\n"
"s_add_u32 s82, s82, s53\n"
"s_add_u32 m0, s55, 32768\n"
"v_exp_f32 v42, v42\n"
"v_exp_f32 v43, v43\n"
"v_exp_f32 v44, v44\n"
"v_smfmac_f32_16x16x64_bf16 a[208:211], a[240:243], v[88:95], v17\n"
"ds_read_b128 a[132:135], v18 offset:1024\n"
"v_exp_f32 v45, v45\n"
"v_exp_f32 v46, v46\n"
"v_exp_f32 v47, v47\n"
"v_mfma_f32_32x32x16_bf16 v[64:79], a[144:147], a[184:187], 0\n"
"v_exp_f32 v48, v48\n"
"v_exp_f32 v49, v49\n"
"v_exp_f32 v50, v50\n"
"v_mfma_f32_32x32x16_bf16 v[64:79], a[148:151], a[188:191], v[64:79]\n"
"v_exp_f32 v51, v51\n"
"v_exp_f32 v52, v52\n"
"v_exp_f32 v53, v53\n"
"v_smfmac_f32_32x32x32_bf16 v[112:127], a[152:155], v[32:39], v17\n"
"buffer_load_dwordx4 v24, s[16:19], s12 offen offset:0 sc1 lds\n"
"v_exp_f32 v54, v54\n"
"v_exp_f32 v55, v55\n"
"v_exp_f32 v56, v56\n"
"v_smfmac_f32_16x16x64_bf16 a[212:215], a[240:243], v[32:39], v17\n"
"ds_read_b128 a[136:139], v18 offset:2048\n"
"v_exp_f32 v57, v57\n"
"v_smfmac_f32_32x32x32_bf16 v[112:127], a[156:159], v[40:47], v17\n"
"buffer_load_dwordx2 v[28:29], v25, s[16:19], s64 offen sc1\n"
"v_exp_f32 v58, v58\n"
"v_exp_f32 v59, v59\n"
"v_exp_f32 v60, v60\n"
"v_smfmac_f32_16x16x64_bf16 a[212:215], a[240:243], v[40:47], v17\n"
"s_waitcnt lgkmcnt(2)\n"
"ds_read_b128 a[140:143], v18 offset:3072\n"
"v_exp_f32 v61, v61\n"
"v_exp_f32 v62, v62\n"
"v_exp_f32 v63, v63\n"
"v_mfma_f32_32x32x16_bf16 v[80:95], a[128:131], a[176:179], 0\n"
"s_waitcnt lgkmcnt(2)\n"
"v_exp_f32 v64, v64\n"
"v_exp_f32 v65, v65\n"
"v_exp_f32 v66, v66\n"
"v_mfma_f32_32x32x16_bf16 v[80:95], a[132:135], a[180:183], v[80:95]\n"
"s_waitcnt lgkmcnt(1)\n"
"v_exp_f32 v67, v67\n"
"v_exp_f32 v68, v68\n"
"v_exp_f32 v69, v69\n"
"v_smfmac_f32_32x32x32_bf16 v[96:111], a[136:139], v[48:55], v17\n"
"buffer_load_dwordx4 v24, s[16:19], s12 offen offset:1024 sc1 lds\n"
"v_exp_f32 v70, v70\n"
"v_exp_f32 v71, v71\n"
"v_exp_f32 v72, v72\n"
"v_smfmac_f32_16x16x64_bf16 a[208:211], a[240:243], v[48:55], v17\n"
"s_waitcnt lgkmcnt(0)\n"
"ds_read_b128 a[144:147], v18 offset:4096\n"
"v_exp_f32 v73, v73\n"
"v_smfmac_f32_32x32x32_bf16 v[96:111], a[140:143], v[56:63], v17\n"
"v_add_u32 v14, s66, v18\n"
"v_add_u32 v15, s67, v18\n"
"v_exp_f32 v74, v74\n"
"v_exp_f32 v75, v75\n"
"v_exp_f32 v76, v76\n"
"v_smfmac_f32_16x16x64_bf16 a[208:211], a[240:243], v[56:63], v17\n"
"ds_read_b128 a[148:151], v18 offset:5120\n"
"v_exp_f32 v77, v77\n"
"v_exp_f32 v78, v78\n"
"v_exp_f32 v79, v79\n"
"v_mfma_f32_32x32x16_bf16 v[32:47], a[128:131], a[184:187], 0\n"
"v_exp_f32 v80, v80\n"
"v_exp_f32 v81, v81\n"
"v_exp_f32 v82, v82\n"
"v_mfma_f32_32x32x16_bf16 v[32:47], a[132:135], a[188:191], v[32:47]\n"
"v_exp_f32 v83, v83\n"
"v_exp_f32 v84, v84\n"
"v_exp_f32 v85, v85\n"
"v_smfmac_f32_32x32x32_bf16 v[112:127], a[136:139], v[64:71], v17\n"
"buffer_load_dwordx4 v24, s[16:19], s12 offen offset:2048 sc1 lds\n"
"v_exp_f32 v86, v86\n"
"v_exp_f32 v87, v87\n"
"v_exp_f32 v88, v88\n"
"v_smfmac_f32_16x16x64_bf16 a[212:215], a[240:243], v[64:71], v17\n"
"ds_read_b128 a[152:155], v18 offset:6144\n"
"v_exp_f32 v89, v89\n"
"v_smfmac_f32_32x32x32_bf16 v[112:127], a[140:143], v[72:79], v17\n"
"s_mov_b32 s83, 0\n"
"v_exp_f32 v90, v90\n"
"v_exp_f32 v91, v91\n"
"v_exp_f32 v92, v92\n"
"v_smfmac_f32_16x16x64_bf16 a[212:215], a[240:243], v[72:79], v17\n"
"s_waitcnt lgkmcnt(2)\n"
"ds_read_b128 a[156:159], v18 offset:7168\n"
"v_exp_f32 v93, v93\n"
"v_exp_f32 v94, v94\n"
"v_exp_f32 v95, v95\n"
"v_mfma_f32_32x32x16_bf16 v[48:63], a[144:147], a[176:179], 0\n"
"s_waitcnt lgkmcnt(2)\n"
"v_exp_f32 v32, v32\n"
"v_exp_f32 v33, v33\n"
"v_exp_f32 v34, v34\n"
"v_mfma_f32_32x32x16_bf16 v[48:63], a[148:151], a[180:183], v[48:63]\n"
"s_waitcnt lgkmcnt(1)\n"
"v_exp_f32 v35, v35\n"
"v_exp_f32 v36, v36\n"
"v_exp_f32 v37, v37\n"
"v_smfmac_f32_32x32x32_bf16 v[96:111], a[152:155], v[80:87], v17\n"
"buffer_load_dwordx4 v24, s[16:19], s12 offen offset:3072 sc1 lds\n"
"v_exp_f32 v38, v38\n"
"v_exp_f32 v39, v39\n"
"v_exp_f32 v40, v40\n"
"v_smfmac_f32_16x16x64_bf16 a[208:211], a[240:243], v[80:87], v17\n"
"s_waitcnt lgkmcnt(0)\n"
"ds_read_b128 a[128:131], v18 offset:8192\n"
"v_exp_f32 v41, v41\n"
"v_smfmac_f32_32x32x32_bf16 v[96:111], a[156:159], v[88:95], v17\n"
"s_mov_b32 s54, 8192\n"
"v_exp_f32 v42, v42\n"
"v_exp_f32 v43, v43\n"
"v_exp_f32 v44, v44\n"
"v_smfmac_f32_16x16x64_bf16 a[208:211], a[240:243], v[88:95], v17\n"
"ds_read_b128 a[132:135], v18 offset:9216\n"
"v_exp_f32 v45, v45\n"
"v_exp_f32 v46, v46\n"
"v_exp_f32 v47, v47\n"
"v_mfma_f32_32x32x16_bf16 v[64:79], a[144:147], a[184:187], 0\n"
"s_add_u32 s12, s12, 16384\n"
"v_exp_f32 v48, v48\n"
"v_exp_f32 v49, v49\n"
"v_exp_f32 v50, v50\n"
"v_mfma_f32_32x32x16_bf16 v[64:79], a[148:151], a[188:191], v[64:79]\n"
"v_exp_f32 v51, v51\n"
"v_exp_f32 v52, v52\n"
"v_exp_f32 v53, v53\n"
"v_smfmac_f32_32x32x32_bf16 v[112:127], a[152:155], v[32:39], v17\n"
"v_exp_f32 v54, v54\n"
"v_exp_f32 v55, v55\n"
"v_exp_f32 v56, v56\n"
"v_smfmac_f32_16x16x64_bf16 a[212:215], a[240:243], v[32:39], v17\n"
"ds_read_b128 a[136:139], v18 offset:10240\n"
"v_exp_f32 v57, v57\n"
"v_smfmac_f32_32x32x32_bf16 v[112:127], a[156:159], v[40:47], v17\n"
"s_waitcnt vmcnt(3)\n"
"v_cmp_ne_u64 vcc, s[80:81], v[28:29]\n"
"s_cbranch_vccnz Lf3s_%=\n"
"Lf3ok_%=:\n"
"buffer_load_dwordx4 a[64:67], v14, s[16:19], s83 offen offset:0 sc1\n"
"v_exp_f32 v58, v58\n"
"v_exp_f32 v59, v59\n"
"v_exp_f32 v60, v60\n"
"v_smfmac_f32_16x16x64_bf16 a[212:215], a[240:243], v[40:47], v17\n"
"s_waitcnt lgkmcnt(2)\n"
"ds_read_b128 a[140:143], v18 offset:11264\n"
"v_exp_f32 v61, v61\n"
"v_exp_f32 v62, v62\n"
"v_exp_f32 v63, v63\n"
"v_mfma_f32_32x32x16_bf16 v[80:95], a[128:131], a[176:179], 0\n"
"s_waitcnt lgkmcnt(2)\n"
"v_exp_f32 v64, v64\n"
"v_exp_f32 v65, v65\n"
"v_exp_f32 v66, v66\n"
"v_mfma_f32_32x32x16_bf16 v[80:95], a[132:135], a[180:183], v[80:95]\n"
"s_waitcnt lgkmcnt(1)\n"
"v_exp_f32 v67, v67\n"
"v_exp_f32 v68, v68\n"
"v_exp_f32 v69, v69\n"
"v_smfmac_f32_32x32x32_bf16 v[96:111], a[136:139], v[48:55], v17\n"
"buffer_load_dwordx4 a[68:71], v14, s[16:19], s83 offen offset:1024 sc1\n"
"v_exp_f32 v70, v70\n"
"v_exp_f32 v71, v71\n"
"v_exp_f32 v72, v72\n"
"v_smfmac_f32_16x16x64_bf16 a[208:211], a[240:243], v[48:55], v17\n"
"s_waitcnt lgkmcnt(0)\n"
"ds_read_b128 a[144:147], v18 offset:12288\n"
"v_exp_f32 v73, v73\n"
"v_smfmac_f32_32x32x32_bf16 v[96:111], a[140:143], v[56:63], v17\n"
"s_getpc_b64 s[10:11]\n"
"Lfupc_%=:\n"
"s_add_u32 s10, s10, Lfu_%=-Lfupc_%=\n"
"s_addc_u32 s11, s11, 0\n"
"buffer_load_dwordx4 a[72:75], v15, s[16:19], s83 offen offset:0 sc1\n"
"v_exp_f32 v74, v74\n"
"v_exp_f32 v75, v75\n"
"v_exp_f32 v76, v76\n"
"v_smfmac_f32_16x16x64_bf16 a[208:211], a[240:243], v[56:63], v17\n"
"ds_read_b128 a[148:151], v18 offset:13312\n"
"v_exp_f32 v77, v77\n"
"v_exp_f32 v78, v78\n"
"v_exp_f32 v79, v79\n"
"v_mfma_f32_32x32x16_bf16 v[32:47], a[128:131], a[184:187], 0\n"
"v_exp_f32 v80, v80\n"
"v_exp_f32 v81, v81\n"
"v_exp_f32 v82, v82\n"
"v_mfma_f32_32x32x16_bf16 v[32:47], a[132:135], a[188:191], v[32:47]\n"
"ds_read_b128 a[152:155], v18 offset:14336\n"
"v_exp_f32 v83, v83\n"
"v_exp_f32 v84, v84\n"
"v_exp_f32 v85, v85\n"
"v_smfmac_f32_32x32x32_bf16 v[112:127], a[136:139], v[64:71], v17\n"
"buffer_load_dwordx4 a[76:79], v15, s[16:19], s83 offen offset:1024 sc1\n"
"s_add_u32 s83, s83, s54\n"
"v_exp_f32 v86, v86\n"
"v_exp_f32 v87, v87\n"
"v_exp_f32 v88, v88\n"
"v_smfmac_f32_16x16x64_bf16 a[212:215], a[240:243], v[64:71], v17\n"
"ds_read_b128 a[156:159], v18 offset:15360\n"
"v_exp_f32 v89, v89\n"
"v_smfmac_f32_32x32x32_bf16 v[112:127], a[140:143], v[72:79], v17\n"
"buffer_load_dwordx4 a[80:83], v14, s[16:19], s83 offen offset:0 sc1\n"
"v_exp_f32 v90, v90\n"
"v_exp_f32 v91, v91\n"
"v_exp_f32 v92, v92\n"
"v_smfmac_f32_16x16x64_bf16 a[212:215], a[240:243], v[72:79], v17\n"
"s_waitcnt lgkmcnt(3)\n"
"v_exp_f32 v93, v93\n"
"v_exp_f32 v94, v94\n"
"v_exp_f32 v95, v95\n"
"v_mfma_f32_32x32x16_bf16 v[48:63], a[144:147], a[176:179], 0\n"
"s_waitcnt vmcnt(5) lgkmcnt(0)\n"
"s_barrier\n"
"v_exp_f32 v32, v32\n"
"v_exp_f32 v33, v33\n"
"v_exp_f32 v34, v34\n"
"v_mfma_f32_32x32x16_bf16 v[48:63], a[148:151], a[180:183], v[48:63]\n"
"v_exp_f32 v35, v35\n"
"v_exp_f32 v36, v36\n"
"v_exp_f32 v37, v37\n"
"v_smfmac_f32_32x32x32_bf16 v[96:111], a[152:155], v[80:87], v17\n"
"v_exp_f32 v38, v38\n"
"v_exp_f32 v39, v39\n"
"v_exp_f32 v40, v40\n"
"v_smfmac_f32_16x16x64_bf16 a[208:211], a[240:243], v[80:87], v17\n"
"ds_read_b128 a[128:131], v18 offset:16384\n"
"v_exp_f32 v41, v41\n"
"v_smfmac_f32_32x32x32_bf16 v[96:111], a[156:159], v[88:95], v17\n"
"buffer_load_dwordx4 a[84:87], v14, s[16:19], s83 offen offset:1024 sc1\n"
"buffer_load_dwordx4 a[88:91], v15, s[16:19], s83 offen offset:0 sc1\n"
"v_exp_f32 v42, v42\n"
"v_exp_f32 v43, v43\n"
"v_exp_f32 v44, v44\n"
"v_smfmac_f32_16x16x64_bf16 a[208:211], a[240:243], v[88:95], v17\n"
"ds_read_b128 a[132:135], v18 offset:17408\n"
"v_exp_f32 v45, v45\n"
"v_exp_f32 v46, v46\n"
"v_exp_f32 v47, v47\n"
"v_mfma_f32_32x32x16_bf16 v[64:79], a[144:147], a[184:187], 0\n"
"v_exp_f32 v48, v48\n"
"v_exp_f32 v49, v49\n"
"v_exp_f32 v50, v50\n"
"v_mfma_f32_32x32x16_bf16 v[64:79], a[148:151], a[188:191], v[64:79]\n"
"v_exp_f32 v51, v51\n"
"v_exp_f32 v52, v52\n"
"v_exp_f32 v53, v53\n"
"v_mfma_f32_16x16x32_bf16 v[128:131], a[232:235], a[160:163], 0\n"
"s_sub_u32 s95, s78, s95\n"
"ds_read_b64_tr_b16 a[12:13], v5 offset:0\n"
"v_exp_f32 v54, v54\n"
"v_mfma_f32_16x16x32_bf16 v[132:135], a[236:239], a[160:163], 0\n"
"ds_read_b64_tr_b16 a[14:15], v5 offset:8192\n"
"v_exp_f32 v55, v55\n"
"v_smfmac_f32_32x32x32_bf16 v[112:127], a[152:155], v[32:39], v17\n"
"buffer_load_dwordx4 a[92:95], v15, s[16:19], s83 offen offset:1024 sc1\n"
"s_add_u32 s83, s83, s54\n"
"v_exp_f32 v56, v56\n"
"v_exp_f32 v57, v57\n"
"v_exp_f32 v58, v58\n"
"v_smfmac_f32_16x16x64_bf16 a[212:215], a[240:243], v[32:39], v17\n"
"ds_read_b128 a[136:139], v18 offset:18432\n"
"v_exp_f32 v59, v59\n"
"v_smfmac_f32_32x32x32_bf16 v[112:127], a[156:159], v[40:47], v17\n"
"s_add_u32 m0, s55, 49152\n"
"buffer_load_dwordx4 a[96:99], v14, s[16:19], s83 offen offset:0 sc1\n"
"v_exp_f32 v60, v60\n"
"v_exp_f32 v61, v61\n"
"v_exp_f32 v62, v62\n"
"v_smfmac_f32_16x16x64_bf16 a[212:215], a[240:243], v[40:47], v17\n"
"s_waitcnt lgkmcnt(4)\n"
"ds_read_b128 a[140:143], v18 offset:19456\n"
"v_exp_f32 v63, v63\n"
"v_mfma_f32_32x32x16_bf16 v[80:95], a[128:131], a[176:179], 0\n"
"s_waitcnt lgkmcnt(4)\n"
"v_exp_f32 v64, v64\n"
"v_exp_f32 v65, v65\n"
"v_exp_f32 v66, v66\n"
"v_mfma_f32_32x32x16_bf16 v[80:95], a[132:135], a[180:183], v[80:95]\n"
"v_exp_f32 v67, v67\n"
"v_exp_f32 v68, v68\n"
"v_exp_f32 v69, v69\n"
"v_mfma_f32_16x16x32_bf16 v[136:139], a[232:235], a[164:167], 0\n"
"s_add_u32 s95, s95, 6291456\n"
"ds_read_b64_tr_b16 a[16:17], v2 offset:1024\n"
"v_exp_f32 v70, v70\n"
"v_mfma_f32_16x16x32_bf16 v[140:143], a[236:239], a[164:167], 0\n"
"s_waitcnt lgkmcnt(2)\n"
"ds_read_b64_tr_b16 a[18:19], v2 offset:9216\n"
"v_exp_f32 v71, v71\n"
"v_smfmac_f32_32x32x32_bf16 v[96:111], a[136:139], v[48:55], v17\n"
"buffer_load_dwordx4 v24, s[16:19], s12 offen offset:0 sc1 lds\n"
"v_exp_f32 v72, v72\n"
"v_exp_f32 v73, v73\n"
"v_exp_f32 v74, v74\n"
"v_smfmac_f32_16x16x64_bf16 a[208:211], a[240:243], v[48:55], v17\n"
"s_waitcnt lgkmcnt(2)\n"
"ds_read_b128 a[144:147], v18 offset:20480\n"
"v_exp_f32 v75, v75\n"
"v_smfmac_f32_32x32x32_bf16 v[96:111], a[140:143], v[56:63], v17\n"
"s_getpc_b64 s[8:9]\n"
"Lfuhpc_%=:\n"
"s_add_u32 s8, s8, Lfuh_%=-Lfuhpc_%=\n"
"s_addc_u32 s9, s9, 0\n"
"buffer_load_dwordx4 a[100:103], v14, s[16:19], s83 offen offset:1024 sc1\n"
"v_exp_f32 v76, v76\n"
"v_exp_f32 v77, v77\n"
"v_exp_f32 v78, v78\n"
"v_smfmac_f32_16x16x64_bf16 a[208:211], a[240:243], v[56:63], v17\n"
"ds_read_b128 a[148:151], v18 offset:21504\n"
"v_exp_f32 v79, v79\n"
"v_mfma_f32_32x32x16_bf16 v[32:47], a[128:131], a[184:187], 0\n"
"v_exp_f32 v80, v80\n"
"v_exp_f32 v81, v81\n"
"v_exp_f32 v82, v82\n"
"v_mfma_f32_32x32x16_bf16 v[32:47], a[132:135], a[188:191], v[32:47]\n"
"v_exp_f32 v83, v83\n"
"v_exp_f32 v84, v84\n"
"v_exp_f32 v85, v85\n"
"v_mfma_f32_16x16x32_bf16 v[160:163], a[232:235], a[168:171], 0\n"
"s_add_u32 s88, s55, 65536\n"
"ds_read_b64_tr_b16 a[20:21], v3 offset:1024\n"
"v_exp_f32 v86, v86\n"
"v_mfma_f32_16x16x32_bf16 v[164:167], a[236:239], a[168:171], 0\n"
"ds_read_b64_tr_b16 a[22:23], v3 offset:9216\n"
"v_exp_f32 v87, v87\n"
"v_smfmac_f32_32x32x32_bf16 v[112:127], a[136:139], v[64:71], v17\n"
"buffer_load_dwordx4 v24, s[16:19], s12 offen offset:1024 sc1 lds\n"
"v_exp_f32 v88, v88\n"
"v_exp_f32 v89, v89\n"
"v_exp_f32 v90, v90\n"
"v_smfmac_f32_16x16x64_bf16 a[212:215], a[240:243], v[64:71], v17\n"
"ds_read_b128 a[152:155], v18 offset:22528\n"
"v_exp_f32 v91, v91\n"
"v_smfmac_f32_32x32x32_bf16 v[112:127], a[140:143], v[72:79], v17\n"
"buffer_load_dwordx4 a[104:107], v15, s[16:19], s83 offen offset:0 sc1\n"
"v_exp_f32 v92, v92\n"
"v_exp_f32 v93, v93\n"
"v_exp_f32 v94, v94\n"
"v_smfmac_f32_16x16x64_bf16 a[212:215], a[240:243], v[72:79], v17\n"
"s_waitcnt lgkmcnt(4)\n"
"ds_read_b128 a[156:159], v18 offset:23552\n"
"v_exp_f32 v95, v95\n"
"v_mfma_f32_32x32x16_bf16 v[48:63], a[144:147], a[176:179], 0\n"
"s_waitcnt lgkmcnt(4)\n"
"v_exp_f32 v32, v32\n"
"v_exp_f32 v33, v33\n"
"v_exp_f32 v34, v34\n"
"v_mfma_f32_32x32x16_bf16 v[48:63], a[148:151], a[180:183], v[48:63]\n"
"v_exp_f32 v35, v35\n"
"v_exp_f32 v36, v36\n"
"v_exp_f32 v37, v37\n"
"v_mfma_f32_16x16x32_bf16 v[168:171], a[232:235], a[172:175], 0\n"
"ds_read_b64_tr_b16 a[24:25], v4 offset:1024\n"
"v_exp_f32 v38, v38\n"
"v_mfma_f32_16x16x32_bf16 v[172:175], a[236:239], a[172:175], 0\n"
"s_waitcnt lgkmcnt(2)\n"
"ds_read_b64_tr_b16 a[26:27], v4 offset:9216\n"
"v_exp_f32 v39, v39\n"
"v_smfmac_f32_32x32x32_bf16 v[96:111], a[152:155], v[80:87], v17\n"
"buffer_load_dwordx4 v24, s[16:19], s12 offen offset:2048 sc1 lds\n"
"v_exp_f32 v40, v40\n"
"v_exp_f32 v41, v41\n"
"v_exp_f32 v42, v42\n"
"v_smfmac_f32_16x16x64_bf16 a[208:211], a[240:243], v[80:87], v17\n"
"s_waitcnt lgkmcnt(2)\n"
"ds_read_b128 a[128:131], v18 offset:24576\n"
"v_exp_f32 v43, v43\n"
"v_smfmac_f32_32x32x32_bf16 v[96:111], a[156:159], v[88:95], v17\n"
"v_add_u32 v19, 32768, v18\n"
"v_lshlrev_b32 v21, 4, v18\n"
"buffer_load_dwordx4 a[108:111], v15, s[16:19], s83 offen offset:1024 sc1\n"
"s_add_u32 s83, s83, s54\n"
"v_exp_f32 v44, v44\n"
"v_exp_f32 v45, v45\n"
"v_exp_f32 v46, v46\n"
"v_smfmac_f32_16x16x64_bf16 a[208:211], a[240:243], v[88:95], v17\n"
"ds_read_b128 a[132:135], v18 offset:25600\n"
"v_exp_f32 v47, v47\n"
"v_mfma_f32_32x32x16_bf16 v[64:79], a[144:147], a[184:187], 0\n"
"v_exp_f32 v48, v48\n"
"v_exp_f32 v49, v49\n"
"v_exp_f32 v50, v50\n"
"v_mfma_f32_32x32x16_bf16 v[64:79], a[148:151], a[188:191], v[64:79]\n"
"v_exp_f32 v51, v51\n"
"v_exp_f32 v52, v52\n"
"v_exp_f32 v53, v53\n"
"v_smfmac_f32_32x32x32_bf16 v[112:127], a[152:155], v[32:39], v17\n"
"buffer_load_dwordx4 v24, s[16:19], s12 offen offset:3072 sc1 lds\n"
"v_exp_f32 v54, v54\n"
"v_exp_f32 v55, v55\n"
"v_exp_f32 v56, v56\n"
"v_smfmac_f32_16x16x64_bf16 a[212:215], a[240:243], v[32:39], v17\n"
"ds_read_b128 a[136:139], v18 offset:26624\n"
"v_exp_f32 v57, v57\n"
"v_smfmac_f32_32x32x32_bf16 v[112:127], a[156:159], v[40:47], v17\n"
"buffer_load_dwordx4 a[112:115], v14, s[16:19], s83 offen offset:0 sc1\n"
"ds_read_b64_tr_b16 a[28:29], v5 offset:1024\n"
"v_exp_f32 v58, v58\n"
"v_exp_f32 v59, v59\n"
"v_exp_f32 v60, v60\n"
"v_smfmac_f32_16x16x64_bf16 a[212:215], a[240:243], v[40:47], v17\n"
"s_waitcnt lgkmcnt(3)\n"
"ds_read_b128 a[140:143], v18 offset:27648\n"
"v_exp_f32 v61, v61\n"
"v_exp_f32 v62, v62\n"
"v_exp_f32 v63, v63\n"
"v_mfma_f32_32x32x16_bf16 v[80:95], a[128:131], a[176:179], 0\n"
"s_waitcnt lgkmcnt(3)\n"
"s_add_u32 s12, s12, 16384\n"
"v_exp_f32 v64, v64\n"
"v_exp_f32 v65, v65\n"
"v_exp_f32 v66, v66\n"
"v_mfma_f32_32x32x16_bf16 v[80:95], a[132:135], a[180:183], v[80:95]\n"
"s_waitcnt lgkmcnt(2)\n"
"v_exp_f32 v67, v67\n"
"v_exp_f32 v68, v68\n"
"v_exp_f32 v69, v69\n"
"v_smfmac_f32_32x32x32_bf16 v[96:111], a[136:139], v[48:55], v17\n"
"v_exp_f32 v70, v70\n"
"v_exp_f32 v71, v71\n"
"v_exp_f32 v72, v72\n"
"v_smfmac_f32_16x16x64_bf16 a[208:211], a[240:243], v[48:55], v17\n"
"s_waitcnt lgkmcnt(0)\n"
"ds_read_b128 a[144:147], v18 offset:28672\n"
"v_exp_f32 v73, v73\n"
"v_smfmac_f32_32x32x32_bf16 v[96:111], a[140:143], v[56:63], v17\n"
"ds_read_b64_tr_b16 a[30:31], v5 offset:9216\n"
"buffer_load_dwordx4 a[116:119], v14, s[16:19], s83 offen offset:1024 sc1\n"
"v_exp_f32 v74, v74\n"
"v_exp_f32 v75, v75\n"
"v_exp_f32 v76, v76\n"
"v_smfmac_f32_16x16x64_bf16 a[208:211], a[240:243], v[56:63], v17\n"
"ds_read_b128 a[148:151], v18 offset:29696\n"
"v_exp_f32 v77, v77\n"
"v_exp_f32 v78, v78\n"
"v_exp_f32 v79, v79\n"
"v_mfma_f32_32x32x16_bf16 v[32:47], a[128:131], a[184:187], 0\n"
"v_exp_f32 v80, v80\n"
"v_exp_f32 v81, v81\n"
"v_exp_f32 v82, v82\n"
"v_exp_f32 v83, v83\n"
"v_cvt_pk_bf16_f32 v80, v80, v81\n"
"v_mfma_f32_32x32x16_bf16 v[32:47], a[132:135], a[188:191], v[32:47]\n"
"v_exp_f32 v84, v84\n"
"v_exp_f32 v85, v85\n"
"v_cvt_pk_bf16_f32 v81, v82, v83\n"
"v_exp_f32 v86, v86\n"
"v_exp_f32 v87, v87\n"
"v_cvt_pk_bf16_f32 v82, v84, v85\n"
"v_smfmac_f32_32x32x32_bf16 v[112:127], a[136:139], v[64:71], v17\n"
"buffer_load_dwordx4 a[120:123], v15, s[16:19], s83 offen offset:0 sc1\n"
"v_exp_f32 v88, v88\n"
"v_exp_f32 v89, v89\n"
"v_cvt_pk_bf16_f32 v83, v86, v87\n"
"v_smfmac_f32_16x16x64_bf16 a[212:215], a[240:243], v[64:71], v17\n"
"ds_read_b128 a[152:155], v18 offset:30720\n"
"v_exp_f32 v90, v90\n"
"v_exp_f32 v91, v91\n"
"v_cvt_pk_bf16_f32 v84, v88, v89\n"
"v_smfmac_f32_32x32x32_bf16 v[112:127], a[140:143], v[72:79], v17\n"
"buffer_load_dwordx4 a[124:127], v15, s[16:19], s83 offen offset:1024 sc1\n"
"v_exp_f32 v92, v92\n"
"v_exp_f32 v93, v93\n"
"v_cvt_pk_bf16_f32 v85, v90, v91\n"
"v_exp_f32 v94, v94\n"
"v_smfmac_f32_16x16x64_bf16 a[212:215], a[240:243], v[72:79], v17\n"
"ds_read_b128 a[156:159], v18 offset:31744\n"
"v_exp_f32 v95, v95\n"
"v_cvt_pk_bf16_f32 v86, v92, v93\n"
"v_cvt_pk_bf16_f32 v87, v94, v95\n"
"s_mov_b32 exec_hi, 0\n"
"buffer_load_dword v22, v21, s[32:35], s95 offen\n"
"s_mov_b64 exec, -1\n"
"s_waitcnt vmcnt(6) lgkmcnt(1)\n"
".p2align 6\n"
"Lfu_%=:\n"
"v_mfma_f32_32x32x16_bf16 v[48:63], a[144:147], a[176:179], 0\n"
"s_waitcnt vmcnt(21)\n"
"v_exp_f32 v32, v32\n"
"v_exp_f32 v33, v33\n"
"v_exp_f32 v34, v34\n"
"v_mfma_f32_16x16x32_bf16 v[128:131], a[0:3], a[64:67], v[128:131]\n"
"s_add_u32 m0, s86, 0\n"
"v_exp_f32 v35, v35\n"
"v_mfma_f32_16x16x32_bf16 v[132:135], a[4:7], a[64:67], v[132:135]\n"
"s_add_u32 s83, s83, s54\n"
"ds_read_b64_tr_b16 a[32:33], v2 offset:2048\n"
"v_cvt_pk_bf16_f32 v88, v32, v33\n"
"v_mfma_f32_16x16x32_bf16 v[136:139], a[8:11], a[64:67], v[136:139]\n"
"ds_read_b64_tr_b16 a[34:35], v2 offset:10240\n"
"v_cvt_pk_bf16_f32 v89, v34, v35\n"
"v_mfma_f32_16x16x32_bf16 v[140:143], a[12:15], a[64:67], v[140:143]\n"
"buffer_load_dwordx4 v10, s[20:23], s82 offen offset:0 sc1 lds\n"
"v_mfma_f32_32x32x16_bf16 v[48:63], a[148:151], a[180:183], v[48:63]\n"
"ds_read_b128 a[128:131], v19 offset:0\n"
"v_exp_f32 v36, v36\n"
"v_exp_f32 v37, v37\n"
"v_exp_f32 v38, v38\n"
"v_mfma_f32_16x16x32_bf16 v[144:147], a[16:19], a[64:67], v[144:147]\n"
"v_exp_f32 v39, v39\n"
"v_mfma_f32_16x16x32_bf16 v[148:151], a[20:23], a[64:67], v[148:151]\n"
"ds_read_b64_tr_b16 a[36:37], v3 offset:2048\n"
"v_cvt_pk_bf16_f32 v90, v36, v37\n"
"v_mfma_f32_16x16x32_bf16 v[152:155], a[24:27], a[64:67], v[152:155]\n"
"s_waitcnt lgkmcnt(5)\n"
"ds_read_b64_tr_b16 a[38:39], v3 offset:10240\n"
"v_cvt_pk_bf16_f32 v91, v38, v39\n"
"v_mfma_f32_16x16x32_bf16 v[156:159], a[28:31], a[64:67], v[156:159]\n"
"buffer_load_dwordx4 a[64:67], v14, s[16:19], s83 offen offset:0 sc1\n"
"v_mfma_f32_32x32x16_bf16 v[96:111], a[152:155], v[80:83], v[96:111]\n"
"v_exp_f32 v40, v40\n"
"v_exp_f32 v41, v41\n"
"v_exp_f32 v42, v42\n"
"v_mfma_f32_16x16x32_bf16 v[188:191], a[28:31], a[68:71], v[188:191]\n"
"s_xor_b32 s90, s89, 32768\n"
"v_exp_f32 v43, v43\n"
"v_mfma_f32_16x16x32_bf16 v[184:187], a[24:27], a[68:71], v[184:187]\n"
"ds_read_b64_tr_b16 a[40:41], v4 offset:2048\n"
"v_cvt_pk_bf16_f32 v92, v40, v41\n"
"v_mfma_f32_16x16x32_bf16 v[180:183], a[20:23], a[68:71], v[180:183]\n"
"ds_read_b64_tr_b16 a[42:43], v4 offset:10240\n"
"v_cvt_pk_bf16_f32 v93, v42, v43\n"
"v_mfma_f32_16x16x32_bf16 v[176:179], a[16:19], a[68:71], v[176:179]\n"
"s_waitcnt lgkmcnt(7)\n"
"buffer_load_dwordx4 v11, s[20:23], s82 offen offset:1024 sc1 lds\n"
"v_mfma_f32_32x32x16_bf16 v[96:111], a[156:159], v[84:87], v[96:111]\n"
"v_exp_f32 v44, v44\n"
"v_exp_f32 v45, v45\n"
"v_exp_f32 v46, v46\n"
"v_mfma_f32_16x16x32_bf16 v[172:175], a[12:15], a[68:71], v[172:175]\n"
"v_exp_f32 v47, v47\n"
"v_mfma_f32_16x16x32_bf16 v[168:171], a[8:11], a[68:71], v[168:171]\n"
"ds_read_b64_tr_b16 a[44:45], v5 offset:2048\n"
"v_cvt_pk_bf16_f32 v94, v44, v45\n"
"v_mfma_f32_16x16x32_bf16 v[164:167], a[4:7], a[68:71], v[164:167]\n"
"ds_read_b64_tr_b16 a[46:47], v5 offset:10240\n"
"v_cvt_pk_bf16_f32 v95, v46, v47\n"
"v_mfma_f32_16x16x32_bf16 v[160:163], a[0:3], a[68:71], v[160:163]\n"
"buffer_load_dwordx4 a[68:71], v14, s[16:19], s83 offen offset:1024 sc1\n"
"v_smfmac_f32_16x16x64_bf16 a[208:211], a[252:255], v[80:87], v30\n"
"ds_read_b128 a[132:135], v19 offset:1024\n"
"v_mfma_f32_32x32x16_bf16 v[32:47], a[144:147], a[184:187], 0\n"
"v_exp_f32 v48, v48\n"
"v_exp_f32 v49, v49\n"
"v_exp_f32 v50, v50\n"
"v_mfma_f32_16x16x32_bf16 v[192:195], a[0:3], a[72:75], v[192:195]\n"
"s_add_u32 m0, s90, s55\n"
"v_exp_f32 v51, v51\n"
"v_mfma_f32_16x16x32_bf16 v[196:199], a[4:7], a[72:75], v[196:199]\n"
"ds_read_b64_tr_b16 a[48:49], v2 offset:3072\n"
"v_cvt_pk_bf16_f32 v80, v48, v49\n"
"v_mfma_f32_16x16x32_bf16 v[200:203], a[8:11], a[72:75], v[200:203]\n"
"ds_read_b64_tr_b16 a[50:51], v2 offset:11264\n"
"v_cvt_pk_bf16_f32 v81, v50, v51\n"
"v_mfma_f32_16x16x32_bf16 v[204:207], a[12:15], a[72:75], v[204:207]\n"
"buffer_load_dwordx4 v24, s[16:19], s12 offen offset:0 sc1 lds\n"
"v_mfma_f32_32x32x16_bf16 v[32:47], a[148:151], a[188:191], v[32:47]\n"
"v_exp_f32 v52, v52\n"
"v_exp_f32 v53, v53\n"
"v_exp_f32 v54, v54\n"
"v_mfma_f32_16x16x32_bf16 v[208:211], a[16:19], a[72:75], v[208:211]\n"
"v_exp_f32 v55, v55\n"
"v_mfma_f32_16x16x32_bf16 v[212:215], a[20:23], a[72:75], v[212:215]\n"
"ds_read_b64_tr_b16 a[52:53], v3 offset:3072\n"
"v_cvt_pk_bf16_f32 v82, v52, v53\n"
"v_mfma_f32_16x16x32_bf16 v[216:219], a[24:27], a[72:75], v[216:219]\n"
"ds_read_b64_tr_b16 a[54:55], v3 offset:11264\n"
"v_cvt_pk_bf16_f32 v83, v54, v55\n"
"v_mfma_f32_16x16x32_bf16 v[220:223], a[28:31], a[72:75], v[220:223]\n"
"buffer_load_dwordx4 a[72:75], v15, s[16:19], s83 offen offset:0 sc1\n"
"v_mfma_f32_32x32x16_bf16 v[112:127], a[152:155], v[88:91], v[112:127]\n"
"v_exp_f32 v56, v56\n"
"v_exp_f32 v57, v57\n"
"v_exp_f32 v58, v58\n"
"v_mfma_f32_16x16x32_bf16 v[252:255], a[28:31], a[76:79], v[252:255]\n"
"v_exp_f32 v59, v59\n"
"v_mfma_f32_16x16x32_bf16 v[248:251], a[24:27], a[76:79], v[248:251]\n"
"ds_read_b64_tr_b16 a[56:57], v4 offset:3072\n"
"v_cvt_pk_bf16_f32 v84, v56, v57\n"
"v_mfma_f32_16x16x32_bf16 v[244:247], a[20:23], a[76:79], v[244:247]\n"
"ds_read_b64_tr_b16 a[58:59], v4 offset:11264\n"
"v_cvt_pk_bf16_f32 v85, v58, v59\n"
"v_mfma_f32_16x16x32_bf16 v[240:243], a[16:19], a[76:79], v[240:243]\n"
"ds_read_b128 a[136:139], v19 offset:2048\n"
"v_mfma_f32_32x32x16_bf16 v[112:127], a[156:159], v[92:95], v[112:127]\n"
"v_exp_f32 v60, v60\n"
"v_exp_f32 v61, v61\n"
"v_exp_f32 v62, v62\n"
"v_mfma_f32_16x16x32_bf16 v[236:239], a[12:15], a[76:79], v[236:239]\n"
"v_exp_f32 v63, v63\n"
"v_mfma_f32_16x16x32_bf16 v[232:235], a[8:11], a[76:79], v[232:235]\n"
"ds_read_b64_tr_b16 a[60:61], v5 offset:3072\n"
"v_cvt_pk_bf16_f32 v86, v60, v61\n"
"v_mfma_f32_16x16x32_bf16 v[228:231], a[4:7], a[76:79], v[228:231]\n"
"ds_read_b64_tr_b16 a[62:63], v5 offset:11264\n"
"v_cvt_pk_bf16_f32 v87, v62, v63\n"
"v_mfma_f32_16x16x32_bf16 v[224:227], a[0:3], a[76:79], v[224:227]\n"
"buffer_load_dwordx4 a[76:79], v15, s[16:19], s83 offen offset:1024 sc1\n"
"v_smfmac_f32_16x16x64_bf16 a[212:215], a[252:255], v[88:95], v30\n"
"s_waitcnt lgkmcnt(2)\n"
"ds_read_b128 a[140:143], v19 offset:3072\n"
"v_mfma_f32_32x32x16_bf16 v[48:63], a[128:131], a[176:179], 0\n"
"s_waitcnt vmcnt(21)\n"
"v_exp_f32 v32, v32\n"
"v_exp_f32 v33, v33\n"
"v_exp_f32 v34, v34\n"
"v_mfma_f32_16x16x32_bf16 v[128:131], a[32:35], a[80:83], v[128:131]\n"
"s_add_u32 m0, s86, 0\n"
"v_exp_f32 v35, v35\n"
"v_mfma_f32_16x16x32_bf16 v[132:135], a[36:39], a[80:83], v[132:135]\n"
"s_add_u32 s83, s83, s54\n"
"ds_read_b64_tr_b16 a[0:1], v2 offset:16384\n"
"v_cvt_pk_bf16_f32 v88, v32, v33\n"
"v_mfma_f32_16x16x32_bf16 v[136:139], a[40:43], a[80:83], v[136:139]\n"
"ds_read_b64_tr_b16 a[2:3], v2 offset:24576\n"
"v_cvt_pk_bf16_f32 v89, v34, v35\n"
"v_mfma_f32_16x16x32_bf16 v[140:143], a[44:47], a[80:83], v[140:143]\n"
"buffer_load_dwordx4 v12, s[20:23], s82 offen offset:2048 sc1 lds\n"
"v_mfma_f32_32x32x16_bf16 v[48:63], a[132:135], a[180:183], v[48:63]\n"
"ds_read_b128 a[144:147], v19 offset:4096\n"
"v_exp_f32 v36, v36\n"
"v_exp_f32 v37, v37\n"
"v_exp_f32 v38, v38\n"
"v_mfma_f32_16x16x32_bf16 v[144:147], a[48:51], a[80:83], v[144:147]\n"
"v_exp_f32 v39, v39\n"
"v_mfma_f32_16x16x32_bf16 v[148:151], a[52:55], a[80:83], v[148:151]\n"
"ds_read_b64_tr_b16 a[4:5], v3 offset:16384\n"
"v_cvt_pk_bf16_f32 v90, v36, v37\n"
"v_mfma_f32_16x16x32_bf16 v[152:155], a[56:59], a[80:83], v[152:155]\n"
"s_waitcnt lgkmcnt(5)\n"
"ds_read_b64_tr_b16 a[6:7], v3 offset:24576\n"
"v_cvt_pk_bf16_f32 v91, v38, v39\n"
"v_mfma_f32_16x16x32_bf16 v[156:159], a[60:63], a[80:83], v[156:159]\n"
"buffer_load_dwordx4 a[80:83], v14, s[16:19], s83 offen offset:0 sc1\n"
"v_mfma_f32_32x32x16_bf16 v[96:111], a[136:139], v[80:83], v[96:111]\n"
"v_exp_f32 v40, v40\n"
"v_exp_f32 v41, v41\n"
"v_exp_f32 v42, v42\n"
"v_mfma_f32_16x16x32_bf16 v[188:191], a[60:63], a[84:87], v[188:191]\n"
"v_exp_f32 v43, v43\n"
"v_mfma_f32_16x16x32_bf16 v[184:187], a[56:59], a[84:87], v[184:187]\n"
"ds_read_b64_tr_b16 a[8:9], v4 offset:16384\n"
"v_cvt_pk_bf16_f32 v92, v40, v41\n"
"v_mfma_f32_16x16x32_bf16 v[180:183], a[52:55], a[84:87], v[180:183]\n"
"ds_read_b64_tr_b16 a[10:11], v4 offset:24576\n"
"v_cvt_pk_bf16_f32 v93, v42, v43\n"
"v_mfma_f32_16x16x32_bf16 v[176:179], a[48:51], a[84:87], v[176:179]\n"
"s_waitcnt lgkmcnt(7)\n"
"buffer_load_dwordx4 v13, s[20:23], s82 offen offset:3072 sc1 lds\n"
"v_mfma_f32_32x32x16_bf16 v[96:111], a[140:143], v[84:87], v[96:111]\n"
"v_exp_f32 v44, v44\n"
"v_exp_f32 v45, v45\n"
"v_exp_f32 v46, v46\n"
"v_mfma_f32_16x16x32_bf16 v[172:175], a[44:47], a[84:87], v[172:175]\n"
"s_add_u32 s82, s82, s53\n"
"v_exp_f32 v47, v47\n"
"v_mfma_f32_16x16x32_bf16 v[168:171], a[40:43], a[84:87], v[168:171]\n"
"ds_read_b64_tr_b16 a[12:13], v5 offset:16384\n"
"v_cvt_pk_bf16_f32 v94, v44, v45\n"
"v_mfma_f32_16x16x32_bf16 v[164:167], a[36:39], a[84:87], v[164:167]\n"
"ds_read_b64_tr_b16 a[14:15], v5 offset:24576\n"
"v_cvt_pk_bf16_f32 v95, v46, v47\n"
"v_mfma_f32_16x16x32_bf16 v[160:163], a[32:35], a[84:87], v[160:163]\n"
"buffer_load_dwordx4 a[84:87], v14, s[16:19], s83 offen offset:1024 sc1\n"
"v_smfmac_f32_16x16x64_bf16 a[208:211], a[252:255], v[80:87], v30\n"
"ds_read_b128 a[148:151], v19 offset:5120\n"
"v_mfma_f32_32x32x16_bf16 v[32:47], a[128:131], a[184:187], 0\n"
"v_exp_f32 v48, v48\n"
"v_exp_f32 v49, v49\n"
"v_exp_f32 v50, v50\n"
"v_mfma_f32_16x16x32_bf16 v[192:195], a[32:35], a[88:91], v[192:195]\n"
"s_add_u32 m0, s90, s55\n"
"v_exp_f32 v51, v51\n"
"v_mfma_f32_16x16x32_bf16 v[196:199], a[36:39], a[88:91], v[196:199]\n"
"ds_read_b64_tr_b16 a[16:17], v2 offset:17408\n"
"v_cvt_pk_bf16_f32 v80, v48, v49\n"
"v_mfma_f32_16x16x32_bf16 v[200:203], a[40:43], a[88:91], v[200:203]\n"
"ds_read_b64_tr_b16 a[18:19], v2 offset:25600\n"
"v_cvt_pk_bf16_f32 v81, v50, v51\n"
"v_mfma_f32_16x16x32_bf16 v[204:207], a[44:47], a[88:91], v[204:207]\n"
"buffer_load_dwordx4 v24, s[16:19], s12 offen offset:1024 sc1 lds\n"
"v_mfma_f32_32x32x16_bf16 v[32:47], a[132:135], a[188:191], v[32:47]\n"
"v_exp_f32 v52, v52\n"
"v_exp_f32 v53, v53\n"
"v_exp_f32 v54, v54\n"
"v_mfma_f32_16x16x32_bf16 v[208:211], a[48:51], a[88:91], v[208:211]\n"
"s_add_u32 s89, s89, 16384\n"
"v_exp_f32 v55, v55\n"
"v_mfma_f32_16x16x32_bf16 v[212:215], a[52:55], a[88:91], v[212:215]\n"
"ds_read_b64_tr_b16 a[20:21], v3 offset:17408\n"
"v_cvt_pk_bf16_f32 v82, v52, v53\n"
"v_mfma_f32_16x16x32_bf16 v[216:219], a[56:59], a[88:91], v[216:219]\n"
"ds_read_b64_tr_b16 a[22:23], v3 offset:25600\n"
"v_cvt_pk_bf16_f32 v83, v54, v55\n"
"v_mfma_f32_16x16x32_bf16 v[220:223], a[60:63], a[88:91], v[220:223]\n"
"buffer_load_dwordx4 a[88:91], v15, s[16:19], s83 offen offset:0 sc1\n"
"v_mfma_f32_32x32x16_bf16 v[112:127], a[136:139], v[88:91], v[112:127]\n"
"v_exp_f32 v56, v56\n"
"v_exp_f32 v57, v57\n"
"v_exp_f32 v58, v58\n"
"v_mfma_f32_16x16x32_bf16 v[252:255], a[60:63], a[92:95], v[252:255]\n"
"s_and_b32 s89, s89, 65535\n"
"v_exp_f32 v59, v59\n"
"v_mfma_f32_16x16x32_bf16 v[248:251], a[56:59], a[92:95], v[248:251]\n"
"ds_read_b64_tr_b16 a[24:25], v4 offset:17408\n"
"v_cvt_pk_bf16_f32 v84, v56, v57\n"
"v_mfma_f32_16x16x32_bf16 v[244:247], a[52:55], a[92:95], v[244:247]\n"
"ds_read_b64_tr_b16 a[26:27], v4 offset:25600\n"
"v_cvt_pk_bf16_f32 v85, v58, v59\n"
"v_mfma_f32_16x16x32_bf16 v[240:243], a[48:51], a[92:95], v[240:243]\n"
"ds_read_b128 a[152:155], v19 offset:6144\n"
"v_mfma_f32_32x32x16_bf16 v[112:127], a[140:143], v[92:95], v[112:127]\n"
"v_exp_f32 v60, v60\n"
"v_exp_f32 v61, v61\n"
"v_exp_f32 v62, v62\n"
"v_mfma_f32_16x16x32_bf16 v[236:239], a[44:47], a[92:95], v[236:239]\n"
"v_exp_f32 v63, v63\n"
"v_mfma_f32_16x16x32_bf16 v[232:235], a[40:43], a[92:95], v[232:235]\n"
"ds_read_b64_tr_b16 a[28:29], v5 offset:17408\n"
"v_cvt_pk_bf16_f32 v86, v60, v61\n"
"v_mfma_f32_16x16x32_bf16 v[228:231], a[36:39], a[92:95], v[228:231]\n"
"ds_read_b64_tr_b16 a[30:31], v5 offset:25600\n"
"v_cvt_pk_bf16_f32 v87, v62, v63\n"
"v_mfma_f32_16x16x32_bf16 v[224:227], a[32:35], a[92:95], v[224:227]\n"
"buffer_load_dwordx4 a[92:95], v15, s[16:19], s83 offen offset:1024 sc1\n"
"v_smfmac_f32_16x16x64_bf16 a[212:215], a[252:255], v[88:95], v30\n"
"s_waitcnt lgkmcnt(2)\n"
"ds_read_b128 a[156:159], v19 offset:7168\n"
"v_mfma_f32_32x32x16_bf16 v[48:63], a[144:147], a[176:179], 0\n"
"s_waitcnt vmcnt(21)\n"
"v_exp_f32 v32, v32\n"
"v_exp_f32 v33, v33\n"
"v_exp_f32 v34, v34\n"
"v_mfma_f32_16x16x32_bf16 v[128:131], a[0:3], a[96:99], v[128:131]\n"
"s_add_u32 m0, s86, 16384\n"
"v_exp_f32 v35, v35\n"
"v_mfma_f32_16x16x32_bf16 v[132:135], a[4:7], a[96:99], v[132:135]\n"
"s_add_u32 s83, s83, s54\n"
"ds_read_b64_tr_b16 a[32:33], v2 offset:18432\n"
"v_cvt_pk_bf16_f32 v88, v32, v33\n"
"v_mfma_f32_16x16x32_bf16 v[136:139], a[8:11], a[96:99], v[136:139]\n"
"ds_read_b64_tr_b16 a[34:35], v2 offset:26624\n"
"v_cvt_pk_bf16_f32 v89, v34, v35\n"
"v_mfma_f32_16x16x32_bf16 v[140:143], a[12:15], a[96:99], v[140:143]\n"
"buffer_load_dwordx4 v10, s[20:23], s82 offen offset:0 sc1 lds\n"
"v_mfma_f32_32x32x16_bf16 v[48:63], a[148:151], a[180:183], v[48:63]\n"
"ds_read_b128 a[128:131], v19 offset:8192\n"
"v_exp_f32 v36, v36\n"
"v_exp_f32 v37, v37\n"
"v_exp_f32 v38, v38\n"
"v_mfma_f32_16x16x32_bf16 v[144:147], a[16:19], a[96:99], v[144:147]\n"
"v_exp_f32 v39, v39\n"
"v_mfma_f32_16x16x32_bf16 v[148:151], a[20:23], a[96:99], v[148:151]\n"
"ds_read_b64_tr_b16 a[36:37], v3 offset:18432\n"
"v_cvt_pk_bf16_f32 v90, v36, v37\n"
"v_mfma_f32_16x16x32_bf16 v[152:155], a[24:27], a[96:99], v[152:155]\n"
"s_waitcnt lgkmcnt(5)\n"
"ds_read_b64_tr_b16 a[38:39], v3 offset:26624\n"
"v_cvt_pk_bf16_f32 v91, v38, v39\n"
"v_mfma_f32_16x16x32_bf16 v[156:159], a[28:31], a[96:99], v[156:159]\n"
"buffer_load_dwordx4 a[96:99], v14, s[16:19], s83 offen offset:0 sc1\n"
"v_mfma_f32_32x32x16_bf16 v[96:111], a[152:155], v[80:83], v[96:111]\n"
"v_exp_f32 v40, v40\n"
"v_exp_f32 v41, v41\n"
"v_exp_f32 v42, v42\n"
"v_mfma_f32_16x16x32_bf16 v[188:191], a[28:31], a[100:103], v[188:191]\n"
"v_exp_f32 v43, v43\n"
"v_mfma_f32_16x16x32_bf16 v[184:187], a[24:27], a[100:103], v[184:187]\n"
"ds_read_b64_tr_b16 a[40:41], v4 offset:18432\n"
"v_cvt_pk_bf16_f32 v92, v40, v41\n"
"v_mfma_f32_16x16x32_bf16 v[180:183], a[20:23], a[100:103], v[180:183]\n"
"ds_read_b64_tr_b16 a[42:43], v4 offset:26624\n"
"v_cvt_pk_bf16_f32 v93, v42, v43\n"
"v_mfma_f32_16x16x32_bf16 v[176:179], a[16:19], a[100:103], v[176:179]\n"
"s_waitcnt lgkmcnt(7)\n"
"buffer_load_dwordx4 v11, s[20:23], s82 offen offset:1024 sc1 lds\n"
"v_mfma_f32_32x32x16_bf16 v[96:111], a[156:159], v[84:87], v[96:111]\n"
"v_exp_f32 v44, v44\n"
"v_exp_f32 v45, v45\n"
"v_exp_f32 v46, v46\n"
"v_mfma_f32_16x16x32_bf16 v[172:175], a[12:15], a[100:103], v[172:175]\n"
"v_exp_f32 v47, v47\n"
"v_mfma_f32_16x16x32_bf16 v[168:171], a[8:11], a[100:103], v[168:171]\n"
"ds_read_b64_tr_b16 a[44:45], v5 offset:18432\n"
"v_cvt_pk_bf16_f32 v94, v44, v45\n"
"v_mfma_f32_16x16x32_bf16 v[164:167], a[4:7], a[100:103], v[164:167]\n"
"ds_read_b64_tr_b16 a[46:47], v5 offset:26624\n"
"v_cvt_pk_bf16_f32 v95, v46, v47\n"
"v_mfma_f32_16x16x32_bf16 v[160:163], a[0:3], a[100:103], v[160:163]\n"
"buffer_load_dwordx4 a[100:103], v14, s[16:19], s83 offen offset:1024 sc1\n"
"v_smfmac_f32_16x16x64_bf16 a[208:211], a[252:255], v[80:87], v30\n"
"ds_read_b128 a[132:135], v19 offset:9216\n"
"v_mfma_f32_32x32x16_bf16 v[32:47], a[144:147], a[184:187], 0\n"
"v_exp_f32 v48, v48\n"
"v_exp_f32 v49, v49\n"
"v_exp_f32 v50, v50\n"
"v_mfma_f32_16x16x32_bf16 v[192:195], a[0:3], a[104:107], v[192:195]\n"
"s_add_u32 m0, s90, s55\n"
"v_exp_f32 v51, v51\n"
"v_mfma_f32_16x16x32_bf16 v[196:199], a[4:7], a[104:107], v[196:199]\n"
"s_add_u32 s85, s85, 32768\n"
"ds_read_b64_tr_b16 a[48:49], v2 offset:19456\n"
"v_cvt_pk_bf16_f32 v80, v48, v49\n"
"v_mfma_f32_16x16x32_bf16 v[200:203], a[8:11], a[104:107], v[200:203]\n"
"s_cmp_eq_u32 s85, 163840\n"
"ds_read_b64_tr_b16 a[50:51], v2 offset:27648\n"
"v_cvt_pk_bf16_f32 v81, v50, v51\n"
"v_mfma_f32_16x16x32_bf16 v[204:207], a[12:15], a[104:107], v[204:207]\n"
"buffer_load_dwordx4 v24, s[16:19], s12 offen offset:2048 sc1 lds\n"
"v_mfma_f32_32x32x16_bf16 v[32:47], a[148:151], a[188:191], v[32:47]\n"
"v_exp_f32 v52, v52\n"
"v_exp_f32 v53, v53\n"
"v_exp_f32 v54, v54\n"
"v_mfma_f32_16x16x32_bf16 v[208:211], a[16:19], a[104:107], v[208:211]\n"
"s_cselect_b32 s85, s91, s85\n"
"v_exp_f32 v55, v55\n"
"v_mfma_f32_16x16x32_bf16 v[212:215], a[20:23], a[104:107], v[212:215]\n"
"ds_read_b64_tr_b16 a[52:53], v3 offset:19456\n"
"v_cvt_pk_bf16_f32 v82, v52, v53\n"
"v_mfma_f32_16x16x32_bf16 v[216:219], a[24:27], a[104:107], v[216:219]\n"
"ds_read_b64_tr_b16 a[54:55], v3 offset:27648\n"
"v_cvt_pk_bf16_f32 v83, v54, v55\n"
"v_mfma_f32_16x16x32_bf16 v[220:223], a[28:31], a[104:107], v[220:223]\n"
"buffer_load_dwordx4 a[104:107], v15, s[16:19], s83 offen offset:0 sc1\n"
"v_mfma_f32_32x32x16_bf16 v[112:127], a[152:155], v[88:91], v[112:127]\n"
"v_exp_f32 v56, v56\n"
"v_exp_f32 v57, v57\n"
"v_exp_f32 v58, v58\n"
"v_mfma_f32_16x16x32_bf16 v[252:255], a[28:31], a[108:111], v[252:255]\n"
"v_exp_f32 v59, v59\n"
"v_mfma_f32_16x16x32_bf16 v[248:251], a[24:27], a[108:111], v[248:251]\n"
"ds_read_b64_tr_b16 a[56:57], v4 offset:19456\n"
"v_cvt_pk_bf16_f32 v84, v56, v57\n"
"v_mfma_f32_16x16x32_bf16 v[244:247], a[20:23], a[108:111], v[244:247]\n"
"ds_read_b64_tr_b16 a[58:59], v4 offset:27648\n"
"v_cvt_pk_bf16_f32 v85, v58, v59\n"
"v_mfma_f32_16x16x32_bf16 v[240:243], a[16:19], a[108:111], v[240:243]\n"
"ds_read_b128 a[136:139], v19 offset:10240\n"
"v_add_u32 v2, s85, v6\n"
"v_mfma_f32_32x32x16_bf16 v[112:127], a[156:159], v[92:95], v[112:127]\n"
"v_exp_f32 v60, v60\n"
"v_exp_f32 v61, v61\n"
"v_exp_f32 v62, v62\n"
"v_mfma_f32_16x16x32_bf16 v[236:239], a[12:15], a[108:111], v[236:239]\n"
"v_exp_f32 v63, v63\n"
"v_mfma_f32_16x16x32_bf16 v[232:235], a[8:11], a[108:111], v[232:235]\n"
"ds_read_b64_tr_b16 a[60:61], v5 offset:19456\n"
"v_cvt_pk_bf16_f32 v86, v60, v61\n"
"v_mfma_f32_16x16x32_bf16 v[228:231], a[4:7], a[108:111], v[228:231]\n"
"ds_read_b64_tr_b16 a[62:63], v5 offset:27648\n"
"v_cvt_pk_bf16_f32 v87, v62, v63\n"
"v_mfma_f32_16x16x32_bf16 v[224:227], a[0:3], a[108:111], v[224:227]\n"
"buffer_load_dwordx4 a[108:111], v15, s[16:19], s83 offen offset:1024 sc1\n"
"v_smfmac_f32_16x16x64_bf16 a[212:215], a[252:255], v[88:95], v30\n"
"s_waitcnt lgkmcnt(2)\n"
"ds_read_b128 a[140:143], v19 offset:11264\n"
"v_add_u32 v3, s85, v7\n"
"v_mfma_f32_32x32x16_bf16 v[48:63], a[128:131], a[176:179], 0\n"
"s_waitcnt vmcnt(21)\n"
"v_exp_f32 v32, v32\n"
"v_exp_f32 v33, v33\n"
"v_exp_f32 v34, v34\n"
"v_mfma_f32_16x16x32_bf16 v[128:131], a[32:35], a[112:115], v[128:131]\n"
"s_add_u32 m0, s86, 16384\n"
"v_exp_f32 v35, v35\n"
"v_mfma_f32_16x16x32_bf16 v[132:135], a[36:39], a[112:115], v[132:135]\n"
"s_add_u32 s83, s83, s54\n"
"v_add_u32 v4, s85, v8\n"
"v_cvt_pk_bf16_f32 v88, v32, v33\n"
"v_mfma_f32_16x16x32_bf16 v[136:139], a[40:43], a[112:115], v[136:139]\n"
"v_add_u32 v5, s85, v9\n"
"v_cvt_pk_bf16_f32 v89, v34, v35\n"
"v_mfma_f32_16x16x32_bf16 v[140:143], a[44:47], a[112:115], v[140:143]\n"
"buffer_load_dwordx4 v12, s[20:23], s82 offen offset:2048 sc1 lds\n"
"v_mfma_f32_32x32x16_bf16 v[48:63], a[132:135], a[180:183], v[48:63]\n"
"s_waitcnt lgkmcnt(0)\n"
"s_barrier\n"
"ds_read_b128 a[144:147], v19 offset:12288\n"
"v_exp_f32 v36, v36\n"
"v_exp_f32 v37, v37\n"
"v_exp_f32 v38, v38\n"
"v_mfma_f32_16x16x32_bf16 v[144:147], a[48:51], a[112:115], v[144:147]\n"
"v_exp_f32 v39, v39\n"
"v_mfma_f32_16x16x32_bf16 v[148:151], a[52:55], a[112:115], v[148:151]\n"
"ds_read_b64_tr_b16 a[0:1], v2 offset:0\n"
"v_cvt_pk_bf16_f32 v90, v36, v37\n"
"v_mfma_f32_16x16x32_bf16 v[152:155], a[56:59], a[112:115], v[152:155]\n"
"ds_read_b64_tr_b16 a[2:3], v2 offset:8192\n"
"v_cvt_pk_bf16_f32 v91, v38, v39\n"
"v_mfma_f32_16x16x32_bf16 v[156:159], a[60:63], a[112:115], v[156:159]\n"
"buffer_load_dwordx4 a[112:115], v14, s[16:19], s83 offen offset:0 sc1\n"
"v_mfma_f32_32x32x16_bf16 v[96:111], a[136:139], v[80:83], v[96:111]\n"
"v_exp_f32 v40, v40\n"
"v_exp_f32 v41, v41\n"
"v_exp_f32 v42, v42\n"
"v_mfma_f32_16x16x32_bf16 v[188:191], a[60:63], a[116:119], v[188:191]\n"
"v_exp_f32 v43, v43\n"
"v_mfma_f32_16x16x32_bf16 v[184:187], a[56:59], a[116:119], v[184:187]\n"
"ds_read_b64_tr_b16 a[4:5], v3 offset:0\n"
"v_cvt_pk_bf16_f32 v92, v40, v41\n"
"v_mfma_f32_16x16x32_bf16 v[180:183], a[52:55], a[116:119], v[180:183]\n"
"ds_read_b64_tr_b16 a[6:7], v3 offset:8192\n"
"v_cvt_pk_bf16_f32 v93, v42, v43\n"
"v_mfma_f32_16x16x32_bf16 v[176:179], a[48:51], a[116:119], v[176:179]\n"
"buffer_load_dwordx4 v13, s[20:23], s82 offen offset:3072 sc1 lds\n"
"v_mfma_f32_32x32x16_bf16 v[96:111], a[140:143], v[84:87], v[96:111]\n"
"v_exp_f32 v44, v44\n"
"v_exp_f32 v45, v45\n"
"v_exp_f32 v46, v46\n"
"v_mfma_f32_16x16x32_bf16 v[172:175], a[44:47], a[116:119], v[172:175]\n"
"s_add_u32 s82, s82, s53\n"
"v_exp_f32 v47, v47\n"
"v_mfma_f32_16x16x32_bf16 v[168:171], a[40:43], a[116:119], v[168:171]\n"
"ds_read_b64_tr_b16 a[8:9], v4 offset:0\n"
"v_cvt_pk_bf16_f32 v94, v44, v45\n"
"v_mfma_f32_16x16x32_bf16 v[164:167], a[36:39], a[116:119], v[164:167]\n"
"ds_read_b64_tr_b16 a[10:11], v4 offset:8192\n"
"v_cvt_pk_bf16_f32 v95, v46, v47\n"
"v_mfma_f32_16x16x32_bf16 v[160:163], a[32:35], a[116:119], v[160:163]\n"
"buffer_load_dwordx4 a[116:119], v14, s[16:19], s83 offen offset:1024 sc1\n"
"v_smfmac_f32_16x16x64_bf16 a[208:211], a[252:255], v[80:87], v30\n"
"ds_read_b128 a[148:151], v19 offset:13312\n"
"v_mfma_f32_32x32x16_bf16 v[32:47], a[128:131], a[184:187], 0\n"
"v_exp_f32 v48, v48\n"
"v_exp_f32 v49, v49\n"
"v_exp_f32 v50, v50\n"
"v_mfma_f32_16x16x32_bf16 v[192:195], a[32:35], a[120:123], v[192:195]\n"
"s_add_u32 m0, s90, s55\n"
"v_exp_f32 v51, v51\n"
"v_mfma_f32_16x16x32_bf16 v[196:199], a[36:39], a[120:123], v[196:199]\n"
"s_add_u32 s86, s86, 32768\n"
"ds_read_b64_tr_b16 a[12:13], v5 offset:0\n"
"v_cvt_pk_bf16_f32 v80, v48, v49\n"
"v_mfma_f32_16x16x32_bf16 v[200:203], a[40:43], a[120:123], v[200:203]\n"
"s_cmp_eq_u32 s86, s87\n"
"ds_read_b64_tr_b16 a[14:15], v5 offset:8192\n"
"v_cvt_pk_bf16_f32 v81, v50, v51\n"
"v_mfma_f32_16x16x32_bf16 v[204:207], a[44:47], a[120:123], v[204:207]\n"
"buffer_load_dwordx4 v24, s[16:19], s12 offen offset:3072 sc1 lds\n"
"v_mfma_f32_32x32x16_bf16 v[32:47], a[132:135], a[188:191], v[32:47]\n"
"ds_read_b128 a[152:155], v19 offset:14336\n"
"v_exp_f32 v52, v52\n"
"v_exp_f32 v53, v53\n"
"v_exp_f32 v54, v54\n"
"v_mfma_f32_16x16x32_bf16 v[208:211], a[48:51], a[120:123], v[208:211]\n"
"s_cselect_b32 s86, s88, s86\n"
"v_exp_f32 v55, v55\n"
"v_mfma_f32_16x16x32_bf16 v[212:215], a[52:55], a[120:123], v[212:215]\n"
"s_add_u32 s12, s12, 16384\n"
"ds_read_b64_tr_b16 a[16:17], v2 offset:1024\n"
"v_cvt_pk_bf16_f32 v82, v52, v53\n"
"v_mfma_f32_16x16x32_bf16 v[216:219], a[56:59], a[120:123], v[216:219]\n"
"ds_read_b64_tr_b16 a[18:19], v2 offset:9216\n"
"v_cvt_pk_bf16_f32 v83, v54, v55\n"
"v_mfma_f32_16x16x32_bf16 v[220:223], a[60:63], a[120:123], v[220:223]\n"
"buffer_load_dwordx4 a[120:123], v15, s[16:19], s83 offen offset:0 sc1\n"
"v_mfma_f32_32x32x16_bf16 v[112:127], a[136:139], v[88:91], v[112:127]\n"
"v_exp_f32 v56, v56\n"
"v_exp_f32 v57, v57\n"
"v_exp_f32 v58, v58\n"
"v_mfma_f32_16x16x32_bf16 v[252:255], a[60:63], a[124:127], v[252:255]\n"
"s_add_u32 s84, s84, 1\n"
"v_exp_f32 v59, v59\n"
"v_mfma_f32_16x16x32_bf16 v[248:251], a[56:59], a[124:127], v[248:251]\n"
"ds_read_b64_tr_b16 a[20:21], v3 offset:1024\n"
"v_cvt_pk_bf16_f32 v84, v56, v57\n"
"v_mfma_f32_16x16x32_bf16 v[244:247], a[52:55], a[124:127], v[244:247]\n"
"ds_read_b64_tr_b16 a[22:23], v3 offset:9216\n"
"v_cvt_pk_bf16_f32 v85, v58, v59\n"
"v_mfma_f32_16x16x32_bf16 v[240:243], a[48:51], a[124:127], v[240:243]\n"
"ds_read_b64_tr_b16 a[24:25], v4 offset:1024\n"
"ds_read_b64_tr_b16 a[26:27], v4 offset:9216\n"
"v_mfma_f32_32x32x16_bf16 v[112:127], a[140:143], v[92:95], v[112:127]\n"
"v_exp_f32 v60, v60\n"
"v_exp_f32 v61, v61\n"
"v_exp_f32 v62, v62\n"
"v_mfma_f32_16x16x32_bf16 v[236:239], a[44:47], a[124:127], v[236:239]\n"
"s_bitcmp0_b32 130174, s84\n"
"v_exp_f32 v63, v63\n"
"v_mfma_f32_16x16x32_bf16 v[232:235], a[40:43], a[124:127], v[232:235]\n"
"ds_read_b64_tr_b16 a[28:29], v5 offset:1024\n"
"v_cvt_pk_bf16_f32 v86, v60, v61\n"
"v_mfma_f32_16x16x32_bf16 v[228:231], a[36:39], a[124:127], v[228:231]\n"
"ds_read_b64_tr_b16 a[30:31], v5 offset:9216\n"
"v_cvt_pk_bf16_f32 v87, v62, v63\n"
"v_mfma_f32_16x16x32_bf16 v[224:227], a[32:35], a[124:127], v[224:227]\n"
"buffer_load_dwordx4 a[124:127], v15, s[16:19], s83 offen offset:1024 sc1\n"
"v_smfmac_f32_16x16x64_bf16 a[212:215], a[252:255], v[88:95], v30\n"
"s_waitcnt lgkmcnt(2)\n"
"ds_read_b128 a[156:159], v19 offset:15360\n"
"v_add_u32 v19, s89, v18\n"
"s_cbranch_scc1 Lfu_%=\n"
"s_setpc_b64 s[8:9]\n"
"Lfuh_%=:\n"
"s_mov_b32 s82, 6291456\n"
"s_mov_b32 s53, 262144\n"
"v_add_u32 v11, 64512, v10\n"
"v_add_u32 v12, 129024, v10\n"
"v_add_u32 v13, 193536, v10\n"
"buffer_load_dword v22, v21, s[36:39], s78 offen\n"
"s_swappc_b64 s[8:9], s[10:11]\n"
"s_mov_b32 s83, 1032192\n"
"s_mov_b32 s54, 16384\n"
"v_add_u32 v14, s68, v18\n"
"v_add_u32 v15, 8192, v14\n"
"s_getpc_b64 s[92:93]\n"
"Lct2_%=:\n"
"s_and_b32 s93, s93, 0xffff\n"
"s_mov_b32 s94, Lkend0-Lct2_%=\n"
"s_mov_b32 s95, 0x20000\n"
"s_lshr_b32 s90, s78, 14\n"
"s_lshl_b32 s90, s90, 8\n"
"v_lshlrev_b32 v21, 3, v18\n"
"s_mov_b64 exec, 3\n"
"buffer_load_dword v22, v21, s[92:95], s90 offen\n"
"s_mov_b64 exec, -1\n"
"s_swappc_b64 s[8:9], s[10:11]\n"
"s_waitcnt lgkmcnt(1)\n"
"v_mfma_f32_32x32x16_bf16 v[48:63], a[144:147], a[176:179], 0\n"
"s_mov_b32 s20, s36\n"
"v_exp_f32 v32, v32\n"
"v_exp_f32 v33, v33\n"
"v_exp_f32 v34, v34\n"
"v_exp_f32 v35, v35\n"
"v_cvt_pk_bf16_f32 v88, v32, v33\n"
"v_mfma_f32_32x32x16_bf16 v[48:63], a[148:151], a[180:183], v[48:63]\n"
"v_cvt_pk_bf16_f32 v208, v208, v209\n"
"v_cvt_pk_bf16_f32 v209, v210, v211\n"
"v_cvt_pk_bf16_f32 v210, v212, v213\n"
"v_cvt_pk_bf16_f32 v211, v214, v215\n"
"v_exp_f32 v36, v36\n"
"v_exp_f32 v37, v37\n"
"v_cvt_pk_bf16_f32 v89, v34, v35\n"
"v_exp_f32 v38, v38\n"
"v_exp_f32 v39, v39\n"
"v_cvt_pk_bf16_f32 v90, v36, v37\n"
"v_mfma_f32_32x32x16_bf16 v[96:111], a[152:155], v[80:83], v[96:111]\n"
"s_waitcnt lgkmcnt(0)\n"
"ds_read_b128 a[128:131], v18 offset:0\n"
"v_exp_f32 v40, v40\n"
"v_exp_f32 v41, v41\n"
"v_cvt_pk_bf16_f32 v91, v38, v39\n"
"v_exp_f32 v42, v42\n"
"v_exp_f32 v43, v43\n"
"v_cvt_pk_bf16_f32 v92, v40, v41\n"
"v_mfma_f32_32x32x16_bf16 v[96:111], a[156:159], v[84:87], v[96:111]\n"
"buffer_store_dwordx4 v[208:211], v18, s[16:19], s73 offen offset:0 sc1\n"
"s_add_u32 m0, s55, 32768\n"
"v_exp_f32 v44, v44\n"
"v_exp_f32 v45, v45\n"
"v_cvt_pk_bf16_f32 v93, v42, v43\n"
"v_exp_f32 v46, v46\n"
"v_smfmac_f32_16x16x64_bf16 a[208:211], a[252:255], v[80:87], v30\n"
"ds_read_b128 a[132:135], v18 offset:1024\n"
"v_exp_f32 v47, v47\n"
"v_cvt_pk_bf16_f32 v94, v44, v45\n"
"v_cvt_pk_bf16_f32 v95, v46, v47\n"
"v_mfma_f32_32x32x16_bf16 v[64:79], a[144:147], a[184:187], 0\n"
"v_exp_f32 v48, v48\n"
"v_exp_f32 v49, v49\n"
"v_exp_f32 v50, v50\n"
"v_exp_f32 v51, v51\n"
"v_mfma_f32_32x32x16_bf16 v[64:79], a[148:151], a[188:191], v[64:79]\n"
"v_cvt_pk_bf16_f32 v216, v216, v217\n"
"v_cvt_pk_bf16_f32 v217, v218, v219\n"
"v_cvt_pk_bf16_f32 v218, v220, v221\n"
"v_cvt_pk_bf16_f32 v219, v222, v223\n"
"s_add_u32 s83, s83, s54\n"
"v_exp_f32 v52, v52\n"
"v_exp_f32 v53, v53\n"
"v_exp_f32 v54, v54\n"
"v_exp_f32 v55, v55\n"
"v_mfma_f32_32x32x16_bf16 v[112:127], a[152:155], v[88:91], v[112:127]\n"
"buffer_load_dwordx4 v24, s[16:19], s12 offen offset:0 sc1 lds\n"
"ds_read_b128 a[136:139], v18 offset:2048\n"
"v_exp_f32 v56, v56\n"
"v_exp_f32 v57, v57\n"
"v_exp_f32 v58, v58\n"
"v_mfma_f32_32x32x16_bf16 v[112:127], a[156:159], v[92:95], v[112:127]\n"
"buffer_store_dwordx4 v[216:219], v18, s[16:19], s73 offen offset:1024 sc1\n"
"v_exp_f32 v59, v59\n"
"v_exp_f32 v60, v60\n"
"v_exp_f32 v61, v61\n"
"v_smfmac_f32_16x16x64_bf16 a[212:215], a[252:255], v[88:95], v30\n"
"s_waitcnt lgkmcnt(2)\n"
"ds_read_b128 a[140:143], v18 offset:3072\n"
"v_exp_f32 v62, v62\n"
"v_exp_f32 v63, v63\n"
"v_mfma_f32_32x32x16_bf16 v[80:95], a[128:131], a[176:179], 0\n"
"s_waitcnt lgkmcnt(2)\n"
"v_exp_f32 v64, v64\n"
"v_exp_f32 v65, v65\n"
"v_exp_f32 v66, v66\n"
"v_mfma_f32_32x32x16_bf16 v[80:95], a[132:135], a[180:183], v[80:95]\n"
"s_waitcnt lgkmcnt(1)\n"
"v_exp_f32 v67, v67\n"
"v_exp_f32 v68, v68\n"
"v_exp_f32 v69, v69\n"
"v_smfmac_f32_32x32x32_bf16 v[96:111], a[136:139], v[48:55], v17\n"
"buffer_load_dwordx4 v24, s[16:19], s12 offen offset:1024 sc1 lds\n"
"v_exp_f32 v70, v70\n"
"v_exp_f32 v71, v71\n"
"v_exp_f32 v72, v72\n"
"v_smfmac_f32_16x16x64_bf16 a[208:211], a[240:243], v[48:55], v17\n"
"s_waitcnt lgkmcnt(0)\n"
"ds_read_b128 a[144:147], v18 offset:4096\n"
"v_exp_f32 v73, v73\n"
"v_smfmac_f32_32x32x32_bf16 v[96:111], a[140:143], v[56:63], v17\n"
"s_waitcnt vmcnt(4)\n"
"s_barrier\n"
"v_exp_f32 v74, v74\n"
"v_exp_f32 v75, v75\n"
"v_exp_f32 v76, v76\n"
"v_smfmac_f32_16x16x64_bf16 a[208:211], a[240:243], v[56:63], v17\n"
"ds_read_b128 a[148:151], v18 offset:5120\n"
"v_exp_f32 v77, v77\n"
"v_exp_f32 v78, v78\n"
"v_exp_f32 v79, v79\n"
"v_mfma_f32_32x32x16_bf16 v[32:47], a[128:131], a[184:187], 0\n"
"v_exp_f32 v80, v80\n"
"v_exp_f32 v81, v81\n"
"v_exp_f32 v82, v82\n"
"v_mfma_f32_32x32x16_bf16 v[32:47], a[132:135], a[188:191], v[32:47]\n"
"v_cvt_pk_bf16_f32 v240, v240, v241\n"
"v_cvt_pk_bf16_f32 v241, v242, v243\n"
"v_cvt_pk_bf16_f32 v242, v244, v245\n"
"v_cvt_pk_bf16_f32 v243, v246, v247\n"
"s_mov_b32 s21, s37\n"
"v_exp_f32 v83, v83\n"
"v_exp_f32 v84, v84\n"
"v_exp_f32 v85, v85\n"
"v_smfmac_f32_32x32x32_bf16 v[112:127], a[136:139], v[64:71], v17\n"
"buffer_load_dwordx4 v24, s[16:19], s12 offen offset:2048 sc1 lds\n"
"v_exp_f32 v86, v86\n"
"v_exp_f32 v87, v87\n"
"v_exp_f32 v88, v88\n"
"v_smfmac_f32_16x16x64_bf16 a[212:215], a[240:243], v[64:71], v17\n"
"ds_read_b128 a[152:155], v18 offset:6144\n"
"v_exp_f32 v89, v89\n"
"v_smfmac_f32_32x32x32_bf16 v[112:127], a[140:143], v[72:79], v17\n"
"buffer_store_dwordx4 v[240:243], v18, s[16:19], s73 offen offset:2048 sc1\n"
"v_exp_f32 v90, v90\n"
"v_exp_f32 v91, v91\n"
"v_exp_f32 v92, v92\n"
"v_smfmac_f32_16x16x64_bf16 a[212:215], a[240:243], v[72:79], v17\n"
"s_waitcnt lgkmcnt(2)\n"
"ds_read_b128 a[156:159], v18 offset:7168\n"
"v_exp_f32 v93, v93\n"
"v_exp_f32 v94, v94\n"
"v_exp_f32 v95, v95\n"
"v_mfma_f32_32x32x16_bf16 v[48:63], a[144:147], a[176:179], 0\n"
"s_waitcnt lgkmcnt(2)\n"
"v_exp_f32 v32, v32\n"
"v_exp_f32 v33, v33\n"
"v_exp_f32 v34, v34\n"
"v_mfma_f32_32x32x16_bf16 v[48:63], a[148:151], a[180:183], v[48:63]\n"
"s_waitcnt lgkmcnt(1)\n"
"v_cvt_pk_bf16_f32 v248, v248, v249\n"
"v_cvt_pk_bf16_f32 v249, v250, v251\n"
"v_cvt_pk_bf16_f32 v250, v252, v253\n"
"v_cvt_pk_bf16_f32 v251, v254, v255\n"
"v_exp_f32 v35, v35\n"
"v_exp_f32 v36, v36\n"
"v_exp_f32 v37, v37\n"
"v_smfmac_f32_32x32x32_bf16 v[96:111], a[152:155], v[80:87], v17\n"
"buffer_load_dwordx4 v24, s[16:19], s12 offen offset:3072 sc1 lds\n"
"v_exp_f32 v38, v38\n"
"v_exp_f32 v39, v39\n"
"v_exp_f32 v40, v40\n"
"v_smfmac_f32_16x16x64_bf16 a[208:211], a[240:243], v[80:87], v17\n"
"s_waitcnt lgkmcnt(0)\n"
"ds_read_b128 a[128:131], v18 offset:8192\n"
"v_exp_f32 v41, v41\n"
"v_smfmac_f32_32x32x32_bf16 v[96:111], a[156:159], v[88:95], v17\n"
"buffer_store_dwordx4 v[248:251], v18, s[16:19], s73 offen offset:3072 sc1\n"
"v_exp_f32 v42, v42\n"
"v_exp_f32 v43, v43\n"
"v_exp_f32 v44, v44\n"
"v_smfmac_f32_16x16x64_bf16 a[208:211], a[240:243], v[88:95], v17\n"
"ds_read_b128 a[132:135], v18 offset:9216\n"
"v_exp_f32 v45, v45\n"
"v_exp_f32 v46, v46\n"
"v_exp_f32 v47, v47\n"
"v_mfma_f32_32x32x16_bf16 v[64:79], a[144:147], a[184:187], 0\n"
"s_add_u32 s12, s12, 16384\n"
"v_exp_f32 v48, v48\n"
"v_exp_f32 v49, v49\n"
"v_exp_f32 v50, v50\n"
"v_mfma_f32_32x32x16_bf16 v[64:79], a[148:151], a[188:191], v[64:79]\n"
"s_mov_b32 s22, s38\n"
"v_exp_f32 v51, v51\n"
"v_exp_f32 v52, v52\n"
"v_exp_f32 v53, v53\n"
"v_smfmac_f32_32x32x32_bf16 v[112:127], a[152:155], v[32:39], v17\n"
"v_exp_f32 v54, v54\n"
"v_exp_f32 v55, v55\n"
"v_exp_f32 v56, v56\n"
"v_smfmac_f32_16x16x64_bf16 a[212:215], a[240:243], v[32:39], v17\n"
"ds_read_b128 a[136:139], v18 offset:10240\n"
"v_exp_f32 v57, v57\n"
"v_smfmac_f32_32x32x32_bf16 v[112:127], a[156:159], v[40:47], v17\n"
"s_add_u32 m0, s55, 49152\n"
"v_exp_f32 v58, v58\n"
"v_exp_f32 v59, v59\n"
"v_exp_f32 v60, v60\n"
"v_smfmac_f32_16x16x64_bf16 a[212:215], a[240:243], v[40:47], v17\n"
"s_waitcnt lgkmcnt(2)\n"
"ds_read_b128 a[140:143], v18 offset:11264\n"
"v_exp_f32 v61, v61\n"
"v_exp_f32 v62, v62\n"
"v_exp_f32 v63, v63\n"
"v_mfma_f32_32x32x16_bf16 v[80:95], a[128:131], a[176:179], 0\n"
"s_waitcnt lgkmcnt(2)\n"
"v_exp_f32 v64, v64\n"
"v_exp_f32 v65, v65\n"
"v_exp_f32 v66, v66\n"
"v_mfma_f32_32x32x16_bf16 v[80:95], a[132:135], a[180:183], v[80:95]\n"
"s_waitcnt lgkmcnt(1)\n"
"v_cvt_pk_bf16_f32 v192, v192, v193\n"
"v_cvt_pk_bf16_f32 v193, v194, v195\n"
"v_cvt_pk_bf16_f32 v194, v196, v197\n"
"v_cvt_pk_bf16_f32 v195, v198, v199\n"
"v_exp_f32 v67, v67\n"
"v_exp_f32 v68, v68\n"
"v_exp_f32 v69, v69\n"
"v_smfmac_f32_32x32x32_bf16 v[96:111], a[136:139], v[48:55], v17\n"
"buffer_load_dwordx4 v24, s[16:19], s12 offen offset:0 sc1 lds\n"
"v_exp_f32 v70, v70\n"
"v_exp_f32 v71, v71\n"
"v_exp_f32 v72, v72\n"
"v_smfmac_f32_16x16x64_bf16 a[208:211], a[240:243], v[48:55], v17\n"
"s_waitcnt lgkmcnt(0)\n"
"ds_read_b128 a[144:147], v18 offset:12288\n"
"v_exp_f32 v73, v73\n"
"v_smfmac_f32_32x32x32_bf16 v[96:111], a[140:143], v[56:63], v17\n"
"buffer_store_dwordx4 v[192:195], v18, s[16:19], s72 offen offset:0 sc1\n"
"v_exp_f32 v74, v74\n"
"v_exp_f32 v75, v75\n"
"v_exp_f32 v76, v76\n"
"v_smfmac_f32_16x16x64_bf16 a[208:211], a[240:243], v[56:63], v17\n"
"ds_read_b128 a[148:151], v18 offset:13312\n"
"v_exp_f32 v77, v77\n"
"v_exp_f32 v78, v78\n"
"v_exp_f32 v79, v79\n"
"v_mfma_f32_32x32x16_bf16 v[32:47], a[128:131], a[184:187], 0\n"
"v_exp_f32 v80, v80\n"
"v_exp_f32 v81, v81\n"
"v_exp_f32 v82, v82\n"
"v_mfma_f32_32x32x16_bf16 v[32:47], a[132:135], a[188:191], v[32:47]\n"
"v_cvt_pk_bf16_f32 v200, v200, v201\n"
"v_cvt_pk_bf16_f32 v201, v202, v203\n"
"v_cvt_pk_bf16_f32 v202, v204, v205\n"
"v_cvt_pk_bf16_f32 v203, v206, v207\n"
"v_exp_f32 v83, v83\n"
"v_exp_f32 v84, v84\n"
"v_exp_f32 v85, v85\n"
"v_smfmac_f32_32x32x32_bf16 v[112:127], a[136:139], v[64:71], v17\n"
"buffer_load_dwordx4 v24, s[16:19], s12 offen offset:1024 sc1 lds\n"
"v_exp_f32 v86, v86\n"
"v_exp_f32 v87, v87\n"
"v_exp_f32 v88, v88\n"
"v_smfmac_f32_16x16x64_bf16 a[212:215], a[240:243], v[64:71], v17\n"
"ds_read_b128 a[152:155], v18 offset:14336\n"
"v_exp_f32 v89, v89\n"
"v_smfmac_f32_32x32x32_bf16 v[112:127], a[140:143], v[72:79], v17\n"
"buffer_store_dwordx4 v[200:203], v18, s[16:19], s72 offen offset:1024 sc1\n"
"v_exp_f32 v90, v90\n"
"v_exp_f32 v91, v91\n"
"v_exp_f32 v92, v92\n"
"v_smfmac_f32_16x16x64_bf16 a[212:215], a[240:243], v[72:79], v17\n"
"s_waitcnt lgkmcnt(2)\n"
"ds_read_b128 a[156:159], v18 offset:15360\n"
"v_exp_f32 v93, v93\n"
"v_exp_f32 v94, v94\n"
"v_exp_f32 v95, v95\n"
"v_mfma_f32_32x32x16_bf16 v[48:63], a[144:147], a[176:179], 0\n"
"s_waitcnt lgkmcnt(2)\n"
"v_exp_f32 v32, v32\n"
"v_exp_f32 v33, v33\n"
"v_exp_f32 v34, v34\n"
"v_mfma_f32_32x32x16_bf16 v[48:63], a[148:151], a[180:183], v[48:63]\n"
"s_waitcnt lgkmcnt(1)\n"
"v_exp_f32 v35, v35\n"
"v_exp_f32 v36, v36\n"
"v_exp_f32 v37, v37\n"
"v_smfmac_f32_32x32x32_bf16 v[96:111], a[152:155], v[80:87], v17\n"
"buffer_load_dwordx4 v24, s[16:19], s12 offen offset:2048 sc1 lds\n"
"v_exp_f32 v38, v38\n"
"v_exp_f32 v39, v39\n"
"v_exp_f32 v40, v40\n"
"v_smfmac_f32_16x16x64_bf16 a[208:211], a[240:243], v[80:87], v17\n"
"s_waitcnt lgkmcnt(0)\n"
"ds_read_b128 a[128:131], v18 offset:16384\n"
"v_exp_f32 v41, v41\n"
"v_smfmac_f32_32x32x32_bf16 v[96:111], a[156:159], v[88:95], v17\n"
"s_mov_b32 s82, 0\n"
"v_exp_f32 v42, v42\n"
"v_exp_f32 v43, v43\n"
"v_exp_f32 v44, v44\n"
"v_smfmac_f32_16x16x64_bf16 a[208:211], a[240:243], v[88:95], v17\n"
"ds_read_b128 a[132:135], v18 offset:17408\n"
"v_exp_f32 v45, v45\n"
"v_exp_f32 v46, v46\n"
"v_exp_f32 v47, v47\n"
"v_mfma_f32_32x32x16_bf16 v[64:79], a[144:147], a[184:187], 0\n"
"v_exp_f32 v48, v48\n"
"v_exp_f32 v49, v49\n"
"v_exp_f32 v50, v50\n"
"v_mfma_f32_32x32x16_bf16 v[64:79], a[148:151], a[188:191], v[64:79]\n"
"v_cvt_pk_bf16_f32 v224, v224, v225\n"
"v_cvt_pk_bf16_f32 v225, v226, v227\n"
"v_cvt_pk_bf16_f32 v226, v228, v229\n"
"v_cvt_pk_bf16_f32 v227, v230, v231\n"
"v_exp_f32 v51, v51\n"
"v_exp_f32 v52, v52\n"
"v_exp_f32 v53, v53\n"
"v_smfmac_f32_32x32x32_bf16 v[112:127], a[152:155], v[32:39], v17\n"
"buffer_load_dwordx4 v24, s[16:19], s12 offen offset:3072 sc1 lds\n"
"v_exp_f32 v54, v54\n"
"v_exp_f32 v55, v55\n"
"v_exp_f32 v56, v56\n"
"v_smfmac_f32_16x16x64_bf16 a[212:215], a[240:243], v[32:39], v17\n"
"ds_read_b128 a[136:139], v18 offset:18432\n"
"v_exp_f32 v57, v57\n"
"v_smfmac_f32_32x32x32_bf16 v[112:127], a[156:159], v[40:47], v17\n"
"buffer_store_dwordx4 v[224:227], v18, s[16:19], s72 offen offset:2048 sc1\n"
"v_exp_f32 v58, v58\n"
"v_exp_f32 v59, v59\n"
"v_exp_f32 v60, v60\n"
"v_smfmac_f32_16x16x64_bf16 a[212:215], a[240:243], v[40:47], v17\n"
"s_waitcnt lgkmcnt(2)\n"
"ds_read_b128 a[140:143], v18 offset:19456\n"
"v_exp_f32 v61, v61\n"
"v_exp_f32 v62, v62\n"
"v_exp_f32 v63, v63\n"
"v_mfma_f32_32x32x16_bf16 v[80:95], a[128:131], a[176:179], 0\n"
"s_waitcnt lgkmcnt(2)\n"
"s_add_u32 s12, s12, 16384\n"
"v_exp_f32 v64, v64\n"
"v_exp_f32 v65, v65\n"
"v_exp_f32 v66, v66\n"
"v_mfma_f32_32x32x16_bf16 v[80:95], a[132:135], a[180:183], v[80:95]\n"
"s_waitcnt lgkmcnt(1)\n"
"v_cvt_pk_bf16_f32 v232, v232, v233\n"
"v_cvt_pk_bf16_f32 v233, v234, v235\n"
"v_cvt_pk_bf16_f32 v234, v236, v237\n"
"v_cvt_pk_bf16_f32 v235, v238, v239\n"
"v_exp_f32 v67, v67\n"
"v_exp_f32 v68, v68\n"
"v_exp_f32 v69, v69\n"
"v_smfmac_f32_32x32x32_bf16 v[96:111], a[136:139], v[48:55], v17\n"
"v_exp_f32 v70, v70\n"
"v_exp_f32 v71, v71\n"
"v_exp_f32 v72, v72\n"
"v_smfmac_f32_16x16x64_bf16 a[208:211], a[240:243], v[48:55], v17\n"
"s_waitcnt lgkmcnt(0)\n"
"ds_read_b128 a[144:147], v18 offset:20480\n"
"v_exp_f32 v73, v73\n"
"v_smfmac_f32_32x32x32_bf16 v[96:111], a[140:143], v[56:63], v17\n"
"buffer_store_dwordx4 v[232:235], v18, s[16:19], s72 offen offset:3072 sc1\n"
"v_exp_f32 v74, v74\n"
"v_exp_f32 v75, v75\n"
"v_exp_f32 v76, v76\n"
"v_smfmac_f32_16x16x64_bf16 a[208:211], a[240:243], v[56:63], v17\n"
"s_add_u32 m0, s55, 131072\n"
"ds_read_b128 a[148:151], v18 offset:21504\n"
"v_exp_f32 v77, v77\n"
"v_exp_f32 v78, v78\n"
"v_exp_f32 v79, v79\n"
"v_mfma_f32_32x32x16_bf16 v[32:47], a[128:131], a[184:187], 0\n"
"v_exp_f32 v80, v80\n"
"v_exp_f32 v81, v81\n"
"v_exp_f32 v82, v82\n"
"v_mfma_f32_32x32x16_bf16 v[32:47], a[132:135], a[188:191], v[32:47]\n"
"v_exp_f32 v83, v83\n"
"v_exp_f32 v84, v84\n"
"v_exp_f32 v85, v85\n"
"v_smfmac_f32_32x32x32_bf16 v[112:127], a[136:139], v[64:71], v17\n"
"buffer_load_dwordx4 v10, s[20:23], s82 offen offset:0 sc1 lds\n"
"v_exp_f32 v86, v86\n"
"v_exp_f32 v87, v87\n"
"v_exp_f32 v88, v88\n"
"v_smfmac_f32_16x16x64_bf16 a[212:215], a[240:243], v[64:71], v17\n"
"ds_read_b128 a[152:155], v18 offset:22528\n"
"v_exp_f32 v89, v89\n"
"v_smfmac_f32_32x32x32_bf16 v[112:127], a[140:143], v[72:79], v17\n"
"s_waitcnt vmcnt(9)\n"
"v_mov_b32 v22, s80\n"
"v_mov_b32 v23, s81\n"
"s_add_u32 s95, s75, 262144\n"
"s_mov_b64 exec, 1\n"
"buffer_store_dwordx2 v[22:23], off, s[16:19], s95 sc1\n"
"s_mov_b64 exec, -1\n"
"v_exp_f32 v90, v90\n"
"v_exp_f32 v91, v91\n"
"v_exp_f32 v92, v92\n"
"v_smfmac_f32_16x16x64_bf16 a[212:215], a[240:243], v[72:79], v17\n"
"s_waitcnt lgkmcnt(2)\n"
"ds_read_b128 a[156:159], v18 offset:23552\n"
"v_exp_f32 v93, v93\n"
"v_exp_f32 v94, v94\n"
"v_exp_f32 v95, v95\n"
"v_mfma_f32_32x32x16_bf16 v[48:63], a[144:147], a[176:179], 0\n"
"s_waitcnt lgkmcnt(2)\n"
"v_exp_f32 v32, v32\n"
"v_exp_f32 v33, v33\n"
"v_exp_f32 v34, v34\n"
"v_mfma_f32_32x32x16_bf16 v[48:63], a[148:151], a[180:183], v[48:63]\n"
"s_waitcnt lgkmcnt(1)\n"
"v_cvt_pk_bf16_f32 v144, v144, v145\n"
"v_cvt_pk_bf16_f32 v145, v146, v147\n"
"v_cvt_pk_bf16_f32 v146, v148, v149\n"
"v_cvt_pk_bf16_f32 v147, v150, v151\n"
"v_exp_f32 v35, v35\n"
"v_exp_f32 v36, v36\n"
"v_exp_f32 v37, v37\n"
"v_smfmac_f32_32x32x32_bf16 v[96:111], a[152:155], v[80:87], v17\n"
"buffer_load_dwordx4 v11, s[20:23], s82 offen offset:1024 sc1 lds\n"
"v_exp_f32 v38, v38\n"
"v_exp_f32 v39, v39\n"
"v_exp_f32 v40, v40\n"
"v_smfmac_f32_16x16x64_bf16 a[208:211], a[240:243], v[80:87], v17\n"
"s_waitcnt lgkmcnt(0)\n"
"ds_read_b128 a[128:131], v18 offset:24576\n"
"v_exp_f32 v41, v41\n"
"v_smfmac_f32_32x32x32_bf16 v[96:111], a[156:159], v[88:95], v17\n"
"buffer_store_dwordx4 v[144:147], v18, s[16:19], s71 offen offset:0 sc1\n"
"v_exp_f32 v42, v42\n"
"v_exp_f32 v43, v43\n"
"v_exp_f32 v44, v44\n"
"v_smfmac_f32_16x16x64_bf16 a[208:211], a[240:243], v[88:95], v17\n"
"ds_read_b128 a[132:135], v18 offset:25600\n"
"v_exp_f32 v45, v45\n"
"v_exp_f32 v46, v46\n"
"v_exp_f32 v47, v47\n"
"v_mfma_f32_32x32x16_bf16 v[64:79], a[144:147], a[184:187], 0\n"
"v_exp_f32 v48, v48\n"
"v_exp_f32 v49, v49\n"
"v_exp_f32 v50, v50\n"
"v_mfma_f32_32x32x16_bf16 v[64:79], a[148:151], a[188:191], v[64:79]\n"
"v_cvt_pk_bf16_f32 v152, v152, v153\n"
"v_cvt_pk_bf16_f32 v153, v154, v155\n"
"v_cvt_pk_bf16_f32 v154, v156, v157\n"
"v_cvt_pk_bf16_f32 v155, v158, v159\n"
"v_exp_f32 v51, v51\n"
"v_exp_f32 v52, v52\n"
"v_exp_f32 v53, v53\n"
"v_smfmac_f32_32x32x32_bf16 v[112:127], a[152:155], v[32:39], v17\n"
"buffer_load_dwordx4 v12, s[20:23], s82 offen offset:2048 sc1 lds\n"
"v_exp_f32 v54, v54\n"
"v_exp_f32 v55, v55\n"
"v_exp_f32 v56, v56\n"
"v_smfmac_f32_16x16x64_bf16 a[212:215], a[240:243], v[32:39], v17\n"
"ds_read_b128 a[136:139], v18 offset:26624\n"
"v_exp_f32 v57, v57\n"
"v_smfmac_f32_32x32x32_bf16 v[112:127], a[156:159], v[40:47], v17\n"
"buffer_store_dwordx4 v[152:155], v18, s[16:19], s71 offen offset:1024 sc1\n"
"v_exp_f32 v58, v58\n"
"v_exp_f32 v59, v59\n"
"v_exp_f32 v60, v60\n"
"v_smfmac_f32_16x16x64_bf16 a[212:215], a[240:243], v[40:47], v17\n"
"s_waitcnt lgkmcnt(2)\n"
"ds_read_b128 a[140:143], v18 offset:27648\n"
"v_exp_f32 v61, v61\n"
"v_exp_f32 v62, v62\n"
"v_exp_f32 v63, v63\n"
"v_mfma_f32_32x32x16_bf16 v[80:95], a[128:131], a[176:179], 0\n"
"s_waitcnt lgkmcnt(2)\n"
"v_exp_f32 v64, v64\n"
"v_exp_f32 v65, v65\n"
"v_exp_f32 v66, v66\n"
"v_mfma_f32_32x32x16_bf16 v[80:95], a[132:135], a[180:183], v[80:95]\n"
"s_waitcnt lgkmcnt(1)\n"
"v_exp_f32 v67, v67\n"
"v_exp_f32 v68, v68\n"
"v_exp_f32 v69, v69\n"
"v_smfmac_f32_32x32x32_bf16 v[96:111], a[136:139], v[48:55], v17\n"
"buffer_load_dwordx4 v13, s[20:23], s82 offen offset:3072 sc1 lds\n"
"v_exp_f32 v70, v70\n"
"v_exp_f32 v71, v71\n"
"v_exp_f32 v72, v72\n"
"v_smfmac_f32_16x16x64_bf16 a[208:211], a[240:243], v[48:55], v17\n"
"s_waitcnt lgkmcnt(0)\n"
"s_add_u32 s82, s82, s53\n"
"ds_read_b128 a[144:147], v18 offset:28672\n"
"v_exp_f32 v73, v73\n"
"v_smfmac_f32_32x32x32_bf16 v[96:111], a[140:143], v[56:63], v17\n"
"buffer_load_dwordx4 v[192:195], v14, s[16:19], s83 offen offset:0 sc1\n"
"v_exp_f32 v74, v74\n"
"v_exp_f32 v75, v75\n"
"v_exp_f32 v76, v76\n"
"v_smfmac_f32_16x16x64_bf16 a[208:211], a[240:243], v[56:63], v17\n"
"ds_read_b128 a[148:151], v18 offset:29696\n"
"v_exp_f32 v77, v77\n"
"v_exp_f32 v78, v78\n"
"v_exp_f32 v79, v79\n"
"v_mfma_f32_32x32x16_bf16 v[32:47], a[128:131], a[184:187], 0\n"
"v_exp_f32 v80, v80\n"
"v_exp_f32 v81, v81\n"
"v_exp_f32 v82, v82\n"
"v_mfma_f32_32x32x16_bf16 v[32:47], a[132:135], a[188:191], v[32:47]\n"
"ds_read_b128 a[152:155], v18 offset:30720\n"
"v_cvt_pk_bf16_f32 v176, v176, v177\n"
"v_cvt_pk_bf16_f32 v177, v178, v179\n"
"v_cvt_pk_bf16_f32 v178, v180, v181\n"
"v_cvt_pk_bf16_f32 v179, v182, v183\n"
"v_exp_f32 v83, v83\n"
"v_exp_f32 v84, v84\n"
"v_exp_f32 v85, v85\n"
"v_smfmac_f32_32x32x32_bf16 v[112:127], a[136:139], v[64:71], v17\n"
"buffer_load_dwordx4 v[196:199], v14, s[16:19], s83 offen offset:1024 sc1\n"
"v_exp_f32 v86, v86\n"
"v_exp_f32 v87, v87\n"
"v_exp_f32 v88, v88\n"
"v_smfmac_f32_16x16x64_bf16 a[212:215], a[240:243], v[64:71], v17\n"
"ds_read_b128 a[156:159], v18 offset:31744\n"
"v_exp_f32 v89, v89\n"
"v_smfmac_f32_32x32x32_bf16 v[112:127], a[140:143], v[72:79], v17\n"
"buffer_store_dwordx4 v[176:179], v18, s[16:19], s71 offen offset:2048 sc1\n"
"v_exp_f32 v90, v90\n"
"v_exp_f32 v91, v91\n"
"v_exp_f32 v92, v92\n"
"v_smfmac_f32_16x16x64_bf16 a[212:215], a[240:243], v[72:79], v17\n"
"s_waitcnt lgkmcnt(3)\n"
"v_exp_f32 v93, v93\n"
"v_exp_f32 v94, v94\n"
"v_exp_f32 v95, v95\n"
"v_mfma_f32_32x32x16_bf16 v[48:63], a[144:147], a[176:179], 0\n"
"s_waitcnt vmcnt(12) lgkmcnt(0)\n"
"s_barrier\n"
"v_exp_f32 v32, v32\n"
"v_exp_f32 v33, v33\n"
"v_exp_f32 v34, v34\n"
"v_mfma_f32_32x32x16_bf16 v[48:63], a[148:151], a[180:183], v[48:63]\n"
"v_cvt_pk_bf16_f32 v184, v184, v185\n"
"v_cvt_pk_bf16_f32 v185, v186, v187\n"
"v_cvt_pk_bf16_f32 v186, v188, v189\n"
"v_cvt_pk_bf16_f32 v187, v190, v191\n"
"v_exp_f32 v35, v35\n"
"v_exp_f32 v36, v36\n"
"v_exp_f32 v37, v37\n"
"v_smfmac_f32_32x32x32_bf16 v[96:111], a[152:155], v[80:87], v17\n"
"v_exp_f32 v38, v38\n"
"v_exp_f32 v39, v39\n"
"v_exp_f32 v40, v40\n"
"v_smfmac_f32_16x16x64_bf16 a[208:211], a[240:243], v[80:87], v17\n"
"ds_read_b128 a[128:131], v18 offset:32768\n"
"v_exp_f32 v41, v41\n"
"v_smfmac_f32_32x32x32_bf16 v[96:111], a[156:159], v[88:95], v17\n"
"s_xor_b32 s88, s75, 24\n"
"s_add_u32 s88, s88, 262144\n"
"buffer_load_dwordx2 v[28:29], off, s[16:19], s88 sc1\n"
"buffer_store_dwordx4 v[184:187], v18, s[16:19], s71 offen offset:3072 sc1\n"
"v_exp_f32 v42, v42\n"
"v_exp_f32 v43, v43\n"
"v_exp_f32 v44, v44\n"
"v_smfmac_f32_16x16x64_bf16 a[208:211], a[240:243], v[88:95], v17\n"
"ds_read_b128 a[132:135], v18 offset:33792\n"
"v_exp_f32 v45, v45\n"
"v_exp_f32 v46, v46\n"
"v_exp_f32 v47, v47\n"
"v_mfma_f32_32x32x16_bf16 v[64:79], a[144:147], a[184:187], 0\n"
"v_exp_f32 v48, v48\n"
"v_exp_f32 v49, v49\n"
"v_exp_f32 v50, v50\n"
"v_mfma_f32_32x32x16_bf16 v[64:79], a[148:151], a[188:191], v[64:79]\n"
"v_exp_f32 v51, v51\n"
"v_exp_f32 v52, v52\n"
"v_exp_f32 v53, v53\n"
"v_smfmac_f32_32x32x32_bf16 v[112:127], a[152:155], v[32:39], v17\n"
"s_waitcnt vmcnt(12)\n"
"v_mov_b32 v22, s80\n"
"v_mov_b32 v23, s81\n"
"s_add_u32 s95, s75, 131072\n"
"s_mov_b64 exec, 1\n"
"buffer_store_dwordx2 v[22:23], off, s[16:19], s95 sc1\n"
"s_mov_b64 exec, -1\n"
"v_exp_f32 v54, v54\n"
"v_exp_f32 v55, v55\n"
"v_exp_f32 v56, v56\n"
"v_smfmac_f32_16x16x64_bf16 a[212:215], a[240:243], v[32:39], v17\n"
"ds_read_b128 a[136:139], v18 offset:34816\n"
"v_exp_f32 v57, v57\n"
"v_smfmac_f32_32x32x32_bf16 v[112:127], a[156:159], v[40:47], v17\n"
"buffer_load_dwordx4 v[200:203], v15, s[16:19], s83 offen offset:0 sc1\n"
"v_exp_f32 v58, v58\n"
"v_exp_f32 v59, v59\n"
"v_exp_f32 v60, v60\n"
"v_smfmac_f32_16x16x64_bf16 a[212:215], a[240:243], v[40:47], v17\n"
"s_waitcnt lgkmcnt(2)\n"
"s_add_u32 m0, s55, 147456\n"
"ds_read_b128 a[140:143], v18 offset:35840\n"
"v_exp_f32 v61, v61\n"
"v_exp_f32 v62, v62\n"
"v_exp_f32 v63, v63\n"
"v_mfma_f32_32x32x16_bf16 v[80:95], a[128:131], a[176:179], 0\n"
"s_waitcnt lgkmcnt(2)\n"
"v_exp_f32 v64, v64\n"
"v_exp_f32 v65, v65\n"
"v_exp_f32 v66, v66\n"
"v_mfma_f32_32x32x16_bf16 v[80:95], a[132:135], a[180:183], v[80:95]\n"
"s_waitcnt lgkmcnt(1)\n"
"v_exp_f32 v67, v67\n"
"v_exp_f32 v68, v68\n"
"v_exp_f32 v69, v69\n"
"v_smfmac_f32_32x32x32_bf16 v[96:111], a[136:139], v[48:55], v17\n"
"buffer_load_dwordx4 v10, s[20:23], s82 offen offset:0 sc1 lds\n"
"v_exp_f32 v70, v70\n"
"v_exp_f32 v71, v71\n"
"v_exp_f32 v72, v72\n"
"v_smfmac_f32_16x16x64_bf16 a[208:211], a[240:243], v[48:55], v17\n"
"s_waitcnt lgkmcnt(0)\n"
"ds_read_b128 a[144:147], v18 offset:36864\n"
"v_exp_f32 v73, v73\n"
"v_smfmac_f32_32x32x32_bf16 v[96:111], a[140:143], v[56:63], v17\n"
"buffer_load_dwordx4 v[204:207], v15, s[16:19], s83 offen offset:1024 sc1\n"
"s_add_u32 s83, s83, s54\n"
"v_exp_f32 v74, v74\n"
"v_exp_f32 v75, v75\n"
"v_exp_f32 v76, v76\n"
"v_smfmac_f32_16x16x64_bf16 a[208:211], a[240:243], v[56:63], v17\n"
"ds_read_b128 a[148:151], v18 offset:37888\n"
"v_exp_f32 v77, v77\n"
"v_exp_f32 v78, v78\n"
"v_exp_f32 v79, v79\n"
"v_mfma_f32_32x32x16_bf16 v[32:47], a[128:131], a[184:187], 0\n"
"v_exp_f32 v80, v80\n"
"v_exp_f32 v81, v81\n"
"v_exp_f32 v82, v82\n"
"v_mfma_f32_32x32x16_bf16 v[32:47], a[132:135], a[188:191], v[32:47]\n"
"v_exp_f32 v83, v83\n"
"v_exp_f32 v84, v84\n"
"v_exp_f32 v85, v85\n"
"v_smfmac_f32_32x32x32_bf16 v[112:127], a[136:139], v[64:71], v17\n"
"buffer_load_dwordx4 v11, s[20:23], s82 offen offset:1024 sc1 lds\n"
"v_exp_f32 v86, v86\n"
"v_exp_f32 v87, v87\n"
"v_exp_f32 v88, v88\n"
"v_smfmac_f32_16x16x64_bf16 a[212:215], a[240:243], v[64:71], v17\n"
"ds_read_b128 a[152:155], v18 offset:38912\n"
"v_exp_f32 v89, v89\n"
"v_smfmac_f32_32x32x32_bf16 v[112:127], a[140:143], v[72:79], v17\n"
"buffer_load_dwordx4 v[208:211], v14, s[16:19], s83 offen offset:0 sc1\n"
"v_exp_f32 v90, v90\n"
"v_exp_f32 v91, v91\n"
"v_exp_f32 v92, v92\n"
"v_smfmac_f32_16x16x64_bf16 a[212:215], a[240:243], v[72:79], v17\n"
"s_waitcnt lgkmcnt(2)\n"
"ds_read_b128 a[156:159], v18 offset:39936\n"
"v_exp_f32 v93, v93\n"
"v_exp_f32 v94, v94\n"
"v_exp_f32 v95, v95\n"
"v_mfma_f32_32x32x16_bf16 v[48:63], a[144:147], a[176:179], 0\n"
"s_waitcnt lgkmcnt(2)\n"
"v_exp_f32 v32, v32\n"
"v_exp_f32 v33, v33\n"
"v_exp_f32 v34, v34\n"
"v_mfma_f32_32x32x16_bf16 v[48:63], a[148:151], a[180:183], v[48:63]\n"
"s_waitcnt lgkmcnt(1)\n"
"v_exp_f32 v35, v35\n"
"v_exp_f32 v36, v36\n"
"v_exp_f32 v37, v37\n"
"v_smfmac_f32_32x32x32_bf16 v[96:111], a[152:155], v[80:87], v17\n"
"buffer_load_dwordx4 v12, s[20:23], s82 offen offset:2048 sc1 lds\n"
"v_exp_f32 v38, v38\n"
"v_exp_f32 v39, v39\n"
"v_exp_f32 v40, v40\n"
"v_smfmac_f32_16x16x64_bf16 a[208:211], a[240:243], v[80:87], v17\n"
"s_waitcnt lgkmcnt(0)\n"
"ds_read_b128 a[128:131], v18 offset:40960\n"
"v_exp_f32 v41, v41\n"
"v_smfmac_f32_32x32x32_bf16 v[96:111], a[156:159], v[88:95], v17\n"
"s_waitcnt vmcnt(8)\n"
"v_cmp_ne_u64 vcc, s[80:81], v[28:29]\n"
"s_cbranch_vccnz Lf5as_%=\n"
"Lf5aok_%=:\n"
"s_add_u32 s95, s74, 32768\n"
"buffer_load_dwordx4 a[160:163], v18, s[16:19], s95 offen offset:0 sc1\n"
"v_exp_f32 v42, v42\n"
"v_exp_f32 v43, v43\n"
"v_exp_f32 v44, v44\n"
"v_smfmac_f32_16x16x64_bf16 a[208:211], a[240:243], v[88:95], v17\n"
"ds_read_b128 a[132:135], v18 offset:41984\n"
"buffer_load_dwordx4 a[164:167], v18, s[16:19], s95 offen offset:1024 sc1\n"
"v_exp_f32 v45, v45\n"
"v_exp_f32 v46, v46\n"
"v_exp_f32 v47, v47\n"
"v_mfma_f32_32x32x16_bf16 v[64:79], a[144:147], a[184:187], 0\n"
"v_exp_f32 v48, v48\n"
"v_exp_f32 v49, v49\n"
"v_exp_f32 v50, v50\n"
"v_mfma_f32_32x32x16_bf16 v[64:79], a[148:151], a[188:191], v[64:79]\n"
"v_exp_f32 v51, v51\n"
"v_exp_f32 v52, v52\n"
"v_exp_f32 v53, v53\n"
"v_smfmac_f32_32x32x32_bf16 v[112:127], a[152:155], v[32:39], v17\n"
"buffer_load_dwordx4 v13, s[20:23], s82 offen offset:3072 sc1 lds\n"
"v_exp_f32 v54, v54\n"
"v_exp_f32 v55, v55\n"
"v_exp_f32 v56, v56\n"
"v_smfmac_f32_16x16x64_bf16 a[212:215], a[240:243], v[32:39], v17\n"
"s_add_u32 s82, s82, s53\n"
"ds_read_b128 a[136:139], v18 offset:43008\n"
"v_exp_f32 v57, v57\n"
"v_smfmac_f32_32x32x32_bf16 v[112:127], a[156:159], v[40:47], v17\n"
"buffer_load_dwordx4 a[168:171], v18, s[16:19], s95 offen offset:2048 sc1\n"
"buffer_load_dwordx4 a[172:175], v18, s[16:19], s95 offen offset:3072 sc1\n"
"v_exp_f32 v58, v58\n"
"v_exp_f32 v59, v59\n"
"v_exp_f32 v60, v60\n"
"v_smfmac_f32_16x16x64_bf16 a[212:215], a[240:243], v[40:47], v17\n"
"s_waitcnt lgkmcnt(2)\n"
"ds_read_b128 a[140:143], v18 offset:44032\n"
"v_exp_f32 v61, v61\n"
"v_exp_f32 v62, v62\n"
"v_exp_f32 v63, v63\n"
"v_mfma_f32_32x32x16_bf16 v[80:95], a[128:131], a[176:179], 0\n"
"s_waitcnt lgkmcnt(2)\n"
"v_exp_f32 v64, v64\n"
"v_exp_f32 v65, v65\n"
"v_exp_f32 v66, v66\n"
"v_mfma_f32_32x32x16_bf16 v[80:95], a[132:135], a[180:183], v[80:95]\n"
"s_waitcnt lgkmcnt(1)\n"
"v_exp_f32 v67, v67\n"
"v_exp_f32 v68, v68\n"
"v_exp_f32 v69, v69\n"
"v_smfmac_f32_32x32x32_bf16 v[96:111], a[136:139], v[48:55], v17\n"
"buffer_load_dwordx4 v[212:215], v14, s[16:19], s83 offen offset:1024 sc1\n"
"v_exp_f32 v70, v70\n"
"v_exp_f32 v71, v71\n"
"v_exp_f32 v72, v72\n"
"v_smfmac_f32_16x16x64_bf16 a[208:211], a[240:243], v[48:55], v17\n"
"s_waitcnt lgkmcnt(0)\n"
"ds_read_b128 a[144:147], v18 offset:45056\n"
"v_exp_f32 v73, v73\n"
"v_smfmac_f32_32x32x32_bf16 v[96:111], a[140:143], v[56:63], v17\n"
"s_xor_b32 s88, s75, 16\n"
"s_add_u32 s88, s88, 131072\n"
"buffer_load_dwordx2 v[28:29], off, s[16:19], s88 sc1\n"
"buffer_load_dwordx4 v[216:219], v15, s[16:19], s83 offen offset:0 sc1\n"
"v_exp_f32 v74, v74\n"
"v_exp_f32 v75, v75\n"
"v_exp_f32 v76, v76\n"
"v_smfmac_f32_16x16x64_bf16 a[208:211], a[240:243], v[56:63], v17\n"
"ds_read_b128 a[148:151], v18 offset:46080\n"
"v_exp_f32 v77, v77\n"
"v_exp_f32 v78, v78\n"
"v_exp_f32 v79, v79\n"
"v_mfma_f32_32x32x16_bf16 v[32:47], a[128:131], a[184:187], 0\n"
"v_exp_f32 v80, v80\n"
"v_exp_f32 v81, v81\n"
"v_exp_f32 v82, v82\n"
"v_mfma_f32_32x32x16_bf16 v[32:47], a[132:135], a[188:191], v[32:47]\n"
"v_exp_f32 v83, v83\n"
"v_exp_f32 v84, v84\n"
"v_exp_f32 v85, v85\n"
"v_smfmac_f32_32x32x32_bf16 v[112:127], a[136:139], v[64:71], v17\n"
"buffer_load_dwordx4 v[220:223], v15, s[16:19], s83 offen offset:1024 sc1\n"
"s_add_u32 s83, s83, s54\n"
"v_exp_f32 v86, v86\n"
"v_exp_f32 v87, v87\n"
"v_exp_f32 v88, v88\n"
"v_smfmac_f32_16x16x64_bf16 a[212:215], a[240:243], v[64:71], v17\n"
"ds_read_b128 a[152:155], v18 offset:47104\n"
"v_exp_f32 v89, v89\n"
"v_smfmac_f32_32x32x32_bf16 v[112:127], a[140:143], v[72:79], v17\n"
"s_waitcnt vmcnt(16)\n"
"v_mov_b32 v22, s80\n"
"v_mov_b32 v23, s81\n"
"s_mov_b64 exec, 1\n"
"buffer_store_dwordx2 v[22:23], off, s[16:19], s75 sc1\n"
"s_mov_b64 exec, -1\n"
"v_exp_f32 v90, v90\n"
"v_exp_f32 v91, v91\n"
"v_exp_f32 v92, v92\n"
"v_smfmac_f32_16x16x64_bf16 a[212:215], a[240:243], v[72:79], v17\n"
"s_waitcnt lgkmcnt(2)\n"
"s_add_u32 m0, s55, 0\n"
"ds_read_b128 a[156:159], v18 offset:48128\n"
"v_exp_f32 v93, v93\n"
"v_exp_f32 v94, v94\n"
"v_exp_f32 v95, v95\n"
"v_mfma_f32_32x32x16_bf16 v[48:63], a[144:147], a[176:179], 0\n"
"s_waitcnt lgkmcnt(2)\n"
"v_exp_f32 v32, v32\n"
"v_exp_f32 v33, v33\n"
"v_exp_f32 v34, v34\n"
"v_mfma_f32_32x32x16_bf16 v[48:63], a[148:151], a[180:183], v[48:63]\n"
"s_waitcnt lgkmcnt(1)\n"
"v_exp_f32 v35, v35\n"
"v_exp_f32 v36, v36\n"
"v_exp_f32 v37, v37\n"
"v_smfmac_f32_32x32x32_bf16 v[96:111], a[152:155], v[80:87], v17\n"
"buffer_load_dwordx4 v10, s[20:23], s82 offen offset:0 sc1 lds\n"
"v_exp_f32 v38, v38\n"
"v_exp_f32 v39, v39\n"
"v_exp_f32 v40, v40\n"
"v_smfmac_f32_16x16x64_bf16 a[208:211], a[240:243], v[80:87], v17\n"
"s_waitcnt lgkmcnt(0)\n"
"ds_read_b128 a[128:131], v18 offset:49152\n"
"v_exp_f32 v41, v41\n"
"v_smfmac_f32_32x32x32_bf16 v[96:111], a[156:159], v[88:95], v17\n"
"buffer_load_dwordx4 v[224:227], v14, s[16:19], s83 offen offset:0 sc1\n"
"buffer_load_dwordx4 v[228:231], v14, s[16:19], s83 offen offset:1024 sc1\n"
"v_exp_f32 v42, v42\n"
"v_exp_f32 v43, v43\n"
"v_exp_f32 v44, v44\n"
"v_smfmac_f32_16x16x64_bf16 a[208:211], a[240:243], v[88:95], v17\n"
"ds_read_b128 a[132:135], v18 offset:50176\n"
"v_exp_f32 v45, v45\n"
"v_exp_f32 v46, v46\n"
"v_exp_f32 v47, v47\n"
"v_mfma_f32_32x32x16_bf16 v[64:79], a[144:147], a[184:187], 0\n"
"v_exp_f32 v48, v48\n"
"v_exp_f32 v49, v49\n"
"v_exp_f32 v50, v50\n"
"v_mfma_f32_32x32x16_bf16 v[64:79], a[148:151], a[188:191], v[64:79]\n"
"v_exp_f32 v51, v51\n"
"v_exp_f32 v52, v52\n"
"v_exp_f32 v53, v53\n"
"v_smfmac_f32_32x32x32_bf16 v[112:127], a[152:155], v[32:39], v17\n"
"buffer_load_dwordx4 v11, s[20:23], s82 offen offset:1024 sc1 lds\n"
"v_exp_f32 v54, v54\n"
"v_exp_f32 v55, v55\n"
"v_exp_f32 v56, v56\n"
"v_smfmac_f32_16x16x64_bf16 a[212:215], a[240:243], v[32:39], v17\n"
"ds_read_b128 a[136:139], v18 offset:51200\n"
"v_exp_f32 v57, v57\n"
"v_smfmac_f32_32x32x32_bf16 v[112:127], a[156:159], v[40:47], v17\n"
"buffer_load_dwordx4 v[232:235], v15, s[16:19], s83 offen offset:0 sc1\n"
"buffer_load_dwordx4 v[236:239], v15, s[16:19], s83 offen offset:1024 sc1\n"
"s_add_u32 s83, s83, s54\n"
"v_exp_f32 v58, v58\n"
"v_exp_f32 v59, v59\n"
"v_exp_f32 v60, v60\n"
"v_smfmac_f32_16x16x64_bf16 a[212:215], a[240:243], v[40:47], v17\n"
"s_waitcnt lgkmcnt(2)\n"
"ds_read_b128 a[140:143], v18 offset:52224\n"
"v_exp_f32 v61, v61\n"
"v_exp_f32 v62, v62\n"
"v_exp_f32 v63, v63\n"
"v_mfma_f32_32x32x16_bf16 v[80:95], a[128:131], a[176:179], 0\n"
"s_waitcnt lgkmcnt(2)\n"
"v_exp_f32 v64, v64\n"
"v_exp_f32 v65, v65\n"
"v_exp_f32 v66, v66\n"
"v_mfma_f32_32x32x16_bf16 v[80:95], a[132:135], a[180:183], v[80:95]\n"
"s_waitcnt lgkmcnt(1)\n"
"v_exp_f32 v67, v67\n"
"v_exp_f32 v68, v68\n"
"v_exp_f32 v69, v69\n"
"v_smfmac_f32_32x32x32_bf16 v[96:111], a[136:139], v[48:55], v17\n"
"buffer_load_dwordx4 v12, s[20:23], s82 offen offset:2048 sc1 lds\n"
"v_exp_f32 v70, v70\n"
"v_exp_f32 v71, v71\n"
"v_exp_f32 v72, v72\n"
"v_smfmac_f32_16x16x64_bf16 a[208:211], a[240:243], v[48:55], v17\n"
"s_waitcnt lgkmcnt(0)\n"
"ds_read_b128 a[144:147], v18 offset:53248\n"
"v_exp_f32 v73, v73\n"
"v_smfmac_f32_32x32x32_bf16 v[96:111], a[140:143], v[56:63], v17\n"
"s_waitcnt vmcnt(10)\n"
"v_cmp_ne_u64 vcc, s[80:81], v[28:29]\n"
"s_cbranch_vccnz Lf5bs_%=\n"
"Lf5bok_%=:\n"
"s_add_u32 s95, s74, 16384\n"
"buffer_load_dwordx4 v[144:147], v18, s[16:19], s95 offen offset:0 sc1\n"
"v_exp_f32 v74, v74\n"
"v_exp_f32 v75, v75\n"
"v_exp_f32 v76, v76\n"
"v_smfmac_f32_16x16x64_bf16 a[208:211], a[240:243], v[56:63], v17\n"
"ds_read_b128 a[148:151], v18 offset:54272\n"
"buffer_load_dwordx4 v[148:151], v18, s[16:19], s95 offen offset:1024 sc1\n"
"v_exp_f32 v77, v77\n"
"v_exp_f32 v78, v78\n"
"v_exp_f32 v79, v79\n"
"v_mfma_f32_32x32x16_bf16 v[32:47], a[128:131], a[184:187], 0\n"
"v_exp_f32 v80, v80\n"
"v_exp_f32 v81, v81\n"
"v_exp_f32 v82, v82\n"
"v_mfma_f32_32x32x16_bf16 v[32:47], a[132:135], a[188:191], v[32:47]\n"
"v_exp_f32 v83, v83\n"
"v_exp_f32 v84, v84\n"
"v_exp_f32 v85, v85\n"
"v_smfmac_f32_32x32x32_bf16 v[112:127], a[136:139], v[64:71], v17\n"
"buffer_load_dwordx4 v13, s[20:23], s82 offen offset:3072 sc1 lds\n"
"v_exp_f32 v86, v86\n"
"v_exp_f32 v87, v87\n"
"v_exp_f32 v88, v88\n"
"v_smfmac_f32_16x16x64_bf16 a[212:215], a[240:243], v[64:71], v17\n"
"s_add_u32 s82, s82, s53\n"
"ds_read_b128 a[152:155], v18 offset:55296\n"
"v_exp_f32 v89, v89\n"
"v_smfmac_f32_32x32x32_bf16 v[112:127], a[140:143], v[72:79], v17\n"
"buffer_load_dwordx4 v[152:155], v18, s[16:19], s95 offen offset:2048 sc1\n"
"buffer_load_dwordx4 v[156:159], v18, s[16:19], s95 offen offset:3072 sc1\n"
"v_exp_f32 v90, v90\n"
"v_exp_f32 v91, v91\n"
"v_exp_f32 v92, v92\n"
"v_smfmac_f32_16x16x64_bf16 a[212:215], a[240:243], v[72:79], v17\n"
"s_waitcnt lgkmcnt(2)\n"
"s_add_u32 m0, s55, 16384\n"
"ds_read_b128 a[156:159], v18 offset:56320\n"
"v_exp_f32 v93, v93\n"
"v_exp_f32 v94, v94\n"
"v_exp_f32 v95, v95\n"
"v_mfma_f32_32x32x16_bf16 v[48:63], a[144:147], a[176:179], 0\n"
"s_waitcnt lgkmcnt(2)\n"
"v_exp_f32 v32, v32\n"
"v_exp_f32 v33, v33\n"
"v_exp_f32 v34, v34\n"
"v_mfma_f32_32x32x16_bf16 v[48:63], a[148:151], a[180:183], v[48:63]\n"
"s_waitcnt lgkmcnt(1)\n"
"v_exp_f32 v35, v35\n"
"v_exp_f32 v36, v36\n"
"v_exp_f32 v37, v37\n"
"v_smfmac_f32_32x32x32_bf16 v[96:111], a[152:155], v[80:87], v17\n"
"buffer_load_dwordx4 v10, s[20:23], s82 offen offset:0 sc1 lds\n"
"v_exp_f32 v38, v38\n"
"v_exp_f32 v39, v39\n"
"v_exp_f32 v40, v40\n"
"v_smfmac_f32_16x16x64_bf16 a[208:211], a[240:243], v[80:87], v17\n"
"s_waitcnt lgkmcnt(0)\n"
"ds_read_b128 a[128:131], v18 offset:57344\n"
"v_exp_f32 v41, v41\n"
"v_smfmac_f32_32x32x32_bf16 v[96:111], a[156:159], v[88:95], v17\n"
"s_xor_b32 s88, s75, 8\n"
"s_add_u32 s88, s88, 0\n"
"buffer_load_dwordx2 v[28:29], off, s[16:19], s88 sc1\n"
"buffer_load_dwordx4 v[240:243], v14, s[16:19], s83 offen offset:0 sc1\n"
"v_exp_f32 v42, v42\n"
"v_exp_f32 v43, v43\n"
"v_exp_f32 v44, v44\n"
"v_smfmac_f32_16x16x64_bf16 a[208:211], a[240:243], v[88:95], v17\n"
"ds_read_b128 a[132:135], v18 offset:58368\n"
"v_exp_f32 v45, v45\n"
"v_exp_f32 v46, v46\n"
"v_exp_f32 v47, v47\n"
"v_mfma_f32_32x32x16_bf16 v[64:79], a[144:147], a[184:187], 0\n"
"v_exp_f32 v48, v48\n"
"v_exp_f32 v49, v49\n"
"v_exp_f32 v50, v50\n"
"v_mfma_f32_32x32x16_bf16 v[64:79], a[148:151], a[188:191], v[64:79]\n"
"v_exp_f32 v51, v51\n"
"v_exp_f32 v52, v52\n"
"v_exp_f32 v53, v53\n"
"v_smfmac_f32_32x32x32_bf16 v[112:127], a[152:155], v[32:39], v17\n"
"buffer_load_dwordx4 v11, s[20:23], s82 offen offset:1024 sc1 lds\n"
"v_exp_f32 v54, v54\n"
"v_exp_f32 v55, v55\n"
"v_exp_f32 v56, v56\n"
"v_smfmac_f32_16x16x64_bf16 a[212:215], a[240:243], v[32:39], v17\n"
"ds_read_b128 a[136:139], v18 offset:59392\n"
"v_exp_f32 v57, v57\n"
"v_smfmac_f32_32x32x32_bf16 v[112:127], a[156:159], v[40:47], v17\n"
"buffer_load_dwordx4 v[244:247], v14, s[16:19], s83 offen offset:1024 sc1\n"
"v_exp_f32 v58, v58\n"
"v_exp_f32 v59, v59\n"
"v_exp_f32 v60, v60\n"
"v_smfmac_f32_16x16x64_bf16 a[212:215], a[240:243], v[40:47], v17\n"
"s_waitcnt lgkmcnt(2)\n"
"ds_read_b128 a[140:143], v18 offset:60416\n"
"v_exp_f32 v61, v61\n"
"v_exp_f32 v62, v62\n"
"v_exp_f32 v63, v63\n"
"v_mfma_f32_32x32x16_bf16 v[80:95], a[128:131], a[176:179], 0\n"
"s_waitcnt lgkmcnt(2)\n"
"v_exp_f32 v64, v64\n"
"v_exp_f32 v65, v65\n"
"v_exp_f32 v66, v66\n"
"v_mfma_f32_32x32x16_bf16 v[80:95], a[132:135], a[180:183], v[80:95]\n"
"s_waitcnt lgkmcnt(1)\n"
"v_exp_f32 v67, v67\n"
"v_exp_f32 v68, v68\n"
"v_exp_f32 v69, v69\n"
"v_smfmac_f32_32x32x32_bf16 v[96:111], a[136:139], v[48:55], v17\n"
"buffer_load_dwordx4 v12, s[20:23], s82 offen offset:2048 sc1 lds\n"
"v_exp_f32 v70, v70\n"
"v_exp_f32 v71, v71\n"
"v_exp_f32 v72, v72\n"
"v_smfmac_f32_16x16x64_bf16 a[208:211], a[240:243], v[48:55], v17\n"
"s_waitcnt lgkmcnt(0)\n"
"ds_read_b128 a[144:147], v18 offset:61440\n"
"v_exp_f32 v73, v73\n"
"v_smfmac_f32_32x32x32_bf16 v[96:111], a[140:143], v[56:63], v17\n"
"buffer_load_dwordx4 v[248:251], v15, s[16:19], s83 offen offset:0 sc1\n"
"v_exp_f32 v74, v74\n"
"v_exp_f32 v75, v75\n"
"v_exp_f32 v76, v76\n"
"v_smfmac_f32_16x16x64_bf16 a[208:211], a[240:243], v[56:63], v17\n"
"ds_read_b128 a[148:151], v18 offset:62464\n"
"v_exp_f32 v77, v77\n"
"v_exp_f32 v78, v78\n"
"v_exp_f32 v79, v79\n"
"v_mfma_f32_32x32x16_bf16 v[32:47], a[128:131], a[184:187], 0\n"
"v_exp_f32 v80, v80\n"
"v_exp_f32 v81, v81\n"
"v_exp_f32 v82, v82\n"
"v_mfma_f32_32x32x16_bf16 v[32:47], a[132:135], a[188:191], v[32:47]\n"
"v_exp_f32 v83, v83\n"
"v_exp_f32 v84, v84\n"
"v_exp_f32 v85, v85\n"
"v_smfmac_f32_32x32x32_bf16 v[112:127], a[136:139], v[64:71], v17\n"
"buffer_load_dwordx4 v13, s[20:23], s82 offen offset:3072 sc1 lds\n"
"v_exp_f32 v86, v86\n"
"v_exp_f32 v87, v87\n"
"v_exp_f32 v88, v88\n"
"v_smfmac_f32_16x16x64_bf16 a[212:215], a[240:243], v[64:71], v17\n"
"s_add_u32 s82, s82, s53\n"
"ds_read_b128 a[152:155], v18 offset:63488\n"
"v_exp_f32 v89, v89\n"
"v_smfmac_f32_32x32x32_bf16 v[112:127], a[140:143], v[72:79], v17\n"
"buffer_load_dwordx4 v[252:255], v15, s[16:19], s83 offen offset:1024 sc1\n"
"s_add_u32 s83, s83, s54\n"
"v_exp_f32 v90, v90\n"
"v_exp_f32 v91, v91\n"
"v_exp_f32 v92, v92\n"
"v_smfmac_f32_16x16x64_bf16 a[212:215], a[240:243], v[72:79], v17\n"
"ds_read_b128 a[156:159], v18 offset:64512\n"
"v_exp_f32 v93, v93\n"
"v_exp_f32 v94, v94\n"
"v_exp_f32 v95, v95\n"
"v_exp_f32 v32, v32\n"
"v_exp_f32 v33, v33\n"
"v_smfmac_f32_16x16x64_bf16 a[208:211], a[240:243], v[80:87], v17\n"
"s_waitcnt vmcnt(7)\n"
"v_cmp_ne_u64 vcc, s[80:81], v[28:29]\n"
"s_cbranch_vccnz Lf5cs_%=\n"
"Lf5cok_%=:\n"
"s_add_u32 s95, s74, 0\n"
"v_exp_f32 v34, v34\n"
"v_exp_f32 v35, v35\n"
"v_exp_f32 v36, v36\n"
"v_smfmac_f32_16x16x64_bf16 a[208:211], a[240:243], v[88:95], v17\n"
"s_waitcnt lgkmcnt(1)\n"
"buffer_load_dwordx4 v[176:179], v18, s[16:19], s95 offen offset:0 sc1\n"
"v_exp_f32 v37, v37\n"
"v_exp_f32 v38, v38\n"
"v_exp_f32 v39, v39\n"
"v_smfmac_f32_32x32x32_bf16 v[96:111], a[152:155], v[80:87], v17\n"
"s_waitcnt lgkmcnt(0)\n"
"buffer_load_dwordx4 v[180:183], v18, s[16:19], s95 offen offset:1024 sc1\n"
"v_exp_f32 v40, v40\n"
"v_exp_f32 v41, v41\n"
"v_exp_f32 v42, v42\n"
"v_exp_f32 v43, v43\n"
"v_smfmac_f32_32x32x32_bf16 v[96:111], a[156:159], v[88:95], v17\n"
"buffer_load_dwordx4 v[184:187], v18, s[16:19], s95 offen offset:2048 sc1\n"
"v_exp_f32 v44, v44\n"
"v_exp_f32 v45, v45\n"
"v_exp_f32 v46, v46\n"
"v_exp_f32 v47, v47\n"
"buffer_load_dwordx4 v[188:191], v18, s[16:19], s95 offen offset:3072 sc1\n"
"s_cmp_lg_u32 s15, 0\n"
"s_cbranch_scc1 Lslow_%=\n"
"v_smfmac_f32_32x32x32_bf16 v[112:127], a[152:155], v[32:39], v17\n"
"s_mov_b32 s83, 0\n"
"v_accvgpr_read_b32 v64, a208\n"
"v_accvgpr_read_b32 v65, a209\n"
"v_add_f32 v64, v64, v65\n"
"v_rcp_f32 v64, v64\n"
"v_accvgpr_read_b32 v21, a248\n"
"v_smfmac_f32_32x32x32_bf16 v[112:127], a[156:159], v[40:47], v17\n"
"v_mul_f32 v96, v96, v64\n"
"v_mul_f32 v97, v97, v64\n"
"v_mul_f32 v98, v98, v64\n"
"v_mul_f32 v99, v99, v64\n"
"v_mul_f32 v104, v104, v64\n"
"v_smfmac_f32_16x16x64_bf16 a[212:215], a[240:243], v[32:39], v17\n"
"v_mul_f32 v105, v105, v64\n"
"v_smfmac_f32_16x16x64_bf16 a[212:215], a[240:243], v[40:47], v17\n"
"v_mul_f32 v106, v106, v64\n"
"v_mul_f32 v107, v107, v64\n"
"v_cvt_pk_bf16_f32 v48, v96, v97\n"
"v_cvt_pk_bf16_f32 v49, v98, v99\n"
"v_cvt_pk_bf16_f32 v50, v104, v105\n"
"v_cvt_pk_bf16_f32 v51, v106, v107\n"
"buffer_store_dwordx4 v[48:51], v21, s[16:19], s76 offen offset:0 sc1\n"
"v_pk_mul_f32 v[100:101], v[100:101], v[64:65] op_sel_hi:[1,0]\n"
"v_pk_mul_f32 v[102:103], v[102:103], v[64:65] op_sel_hi:[1,0]\n"
"v_pk_mul_f32 v[108:109], v[108:109], v[64:65] op_sel_hi:[1,0]\n"
"v_pk_mul_f32 v[110:111], v[110:111], v[64:65] op_sel_hi:[1,0]\n"
"v_cvt_pk_bf16_f32 v52, v100, v101\n"
"v_cvt_pk_bf16_f32 v53, v102, v103\n"
"v_cvt_pk_bf16_f32 v54, v108, v109\n"
"v_cvt_pk_bf16_f32 v55, v110, v111\n"
"buffer_store_dwordx4 v[52:55], v21, s[16:19], s76 offen offset:512 sc1\n"
"v_accvgpr_read_b32 v68, a212\n"
"v_accvgpr_read_b32 v69, a213\n"
"Lrest_%=:\n"
"v_add_f32 v68, v68, v69\n"
"v_rcp_f32 v68, v68\n"
"v_accvgpr_read_b32 v21, a248\n"
"v_pk_mul_f32 v[112:113], v[112:113], v[68:69] op_sel_hi:[1,0]\n"
"v_pk_mul_f32 v[114:115], v[114:115], v[68:69] op_sel_hi:[1,0]\n"
"v_pk_mul_f32 v[120:121], v[120:121], v[68:69] op_sel_hi:[1,0]\n"
"v_pk_mul_f32 v[122:123], v[122:123], v[68:69] op_sel_hi:[1,0]\n"
"v_cvt_pk_bf16_f32 v56, v112, v113\n"
"v_cvt_pk_bf16_f32 v57, v114, v115\n"
"v_cvt_pk_bf16_f32 v58, v120, v121\n"
"v_cvt_pk_bf16_f32 v59, v122, v123\n"
"buffer_store_dwordx4 v[56:59], v21, s[16:19], s76 offen offset:2048 sc1\n"
"v_pk_mul_f32 v[116:117], v[116:117], v[68:69] op_sel_hi:[1,0]\n"
"v_pk_mul_f32 v[118:119], v[118:119], v[68:69] op_sel_hi:[1,0]\n"
"v_pk_mul_f32 v[124:125], v[124:125], v[68:69] op_sel_hi:[1,0]\n"
"v_pk_mul_f32 v[126:127], v[126:127], v[68:69] op_sel_hi:[1,0]\n"
"v_cvt_pk_bf16_f32 v60, v116, v117\n"
"v_cvt_pk_bf16_f32 v61, v118, v119\n"
"v_cvt_pk_bf16_f32 v62, v124, v125\n"
"v_cvt_pk_bf16_f32 v63, v126, v127\n"
"buffer_store_dwordx4 v[60:63], v21, s[16:19], s76 offen offset:2560 sc1\n"
"v_accvgpr_read_b32 v16, a247\n"
"v_accvgpr_read_b32 v26, a249\n"
"v_add_u32 v24, 98304, v24\n"
"v_xor_b32 v27, 64, v26\n"
"v_lshlrev_b32 v25, 0, v25\n"
"v_mfma_f32_16x16x32_bf16 v[128:131], a[0:3], a[64:67], v[128:131]\n"
"ds_read_b64_tr_b16 a[32:33], v2 offset:2048\n"
"v_mfma_f32_16x16x32_bf16 v[132:135], a[4:7], a[64:67], v[132:135]\n"
"ds_read_b64_tr_b16 a[34:35], v2 offset:10240\n"
"v_mfma_f32_16x16x32_bf16 v[136:139], a[8:11], a[64:67], v[136:139]\n"
"ds_read_b64_tr_b16 a[36:37], v3 offset:2048\n"
"v_mfma_f32_16x16x32_bf16 v[140:143], a[12:15], a[64:67], v[140:143]\n"
"ds_read_b64_tr_b16 a[38:39], v3 offset:10240\n"
"v_mfma_f32_16x16x32_bf16 v[160:163], a[0:3], a[68:71], v[160:163]\n"
"ds_read_b64_tr_b16 a[40:41], v4 offset:2048\n"
"v_mfma_f32_16x16x32_bf16 v[164:167], a[4:7], a[68:71], v[164:167]\n"
"ds_read_b64_tr_b16 a[42:43], v4 offset:10240\n"
"v_mfma_f32_16x16x32_bf16 v[168:171], a[8:11], a[68:71], v[168:171]\n"
"ds_read_b64_tr_b16 a[44:45], v5 offset:2048\n"
"v_mfma_f32_16x16x32_bf16 v[172:175], a[12:15], a[68:71], v[172:175]\n"
"ds_read_b64_tr_b16 a[46:47], v5 offset:10240\n"
"v_mfma_f32_16x16x32_bf16 v[128:131], a[16:19], a[72:75], v[128:131]\n"
"ds_read_b64_tr_b16 a[48:49], v2 offset:3072\n"
"v_mfma_f32_16x16x32_bf16 v[132:135], a[20:23], a[72:75], v[132:135]\n"
"ds_read_b64_tr_b16 a[50:51], v2 offset:11264\n"
"v_mfma_f32_16x16x32_bf16 v[136:139], a[24:27], a[72:75], v[136:139]\n"
"ds_read_b64_tr_b16 a[52:53], v3 offset:3072\n"
"v_mfma_f32_16x16x32_bf16 v[140:143], a[28:31], a[72:75], v[140:143]\n"
"ds_read_b64_tr_b16 a[54:55], v3 offset:11264\n"
"v_mfma_f32_16x16x32_bf16 v[160:163], a[16:19], a[76:79], v[160:163]\n"
"ds_read_b64_tr_b16 a[56:57], v4 offset:3072\n"
"v_mfma_f32_16x16x32_bf16 v[164:167], a[20:23], a[76:79], v[164:167]\n"
"ds_read_b64_tr_b16 a[58:59], v4 offset:11264\n"
"v_mfma_f32_16x16x32_bf16 v[168:171], a[24:27], a[76:79], v[168:171]\n"
"ds_read_b64_tr_b16 a[60:61], v5 offset:3072\n"
"v_mfma_f32_16x16x32_bf16 v[172:175], a[28:31], a[76:79], v[172:175]\n"
"s_waitcnt lgkmcnt(7)\n"
"ds_read_b64_tr_b16 a[62:63], v5 offset:11264\n"
"v_mfma_f32_16x16x32_bf16 v[128:131], a[32:35], a[80:83], v[128:131]\n"
"ds_read_b64_tr_b16 a[0:1], v2 offset:16384\n"
"v_mfma_f32_16x16x32_bf16 v[132:135], a[36:39], a[80:83], v[132:135]\n"
"ds_read_b64_tr_b16 a[2:3], v2 offset:24576\n"
"v_mfma_f32_16x16x32_bf16 v[136:139], a[40:43], a[80:83], v[136:139]\n"
"ds_read_b64_tr_b16 a[4:5], v3 offset:16384\n"
"v_mfma_f32_16x16x32_bf16 v[140:143], a[44:47], a[80:83], v[140:143]\n"
"ds_read_b64_tr_b16 a[6:7], v3 offset:24576\n"
"v_mfma_f32_16x16x32_bf16 v[160:163], a[32:35], a[84:87], v[160:163]\n"
"ds_read_b64_tr_b16 a[8:9], v4 offset:16384\n"
"v_mfma_f32_16x16x32_bf16 v[164:167], a[36:39], a[84:87], v[164:167]\n"
"ds_read_b64_tr_b16 a[10:11], v4 offset:24576\n"
"v_mfma_f32_16x16x32_bf16 v[168:171], a[40:43], a[84:87], v[168:171]\n"
"ds_read_b64_tr_b16 a[12:13], v5 offset:16384\n"
"v_mfma_f32_16x16x32_bf16 v[172:175], a[44:47], a[84:87], v[172:175]\n"
"s_waitcnt lgkmcnt(7)\n"
"ds_read_b64_tr_b16 a[14:15], v5 offset:24576\n"
"v_mfma_f32_16x16x32_bf16 v[128:131], a[48:51], a[88:91], v[128:131]\n"
"ds_read_b64_tr_b16 a[16:17], v2 offset:17408\n"
"v_mfma_f32_16x16x32_bf16 v[132:135], a[52:55], a[88:91], v[132:135]\n"
"ds_read_b64_tr_b16 a[18:19], v2 offset:25600\n"
"v_mfma_f32_16x16x32_bf16 v[136:139], a[56:59], a[88:91], v[136:139]\n"
"ds_read_b64_tr_b16 a[20:21], v3 offset:17408\n"
"v_mfma_f32_16x16x32_bf16 v[140:143], a[60:63], a[88:91], v[140:143]\n"
"ds_read_b64_tr_b16 a[22:23], v3 offset:25600\n"
"v_mfma_f32_16x16x32_bf16 v[160:163], a[48:51], a[92:95], v[160:163]\n"
"ds_read_b64_tr_b16 a[24:25], v4 offset:17408\n"
"v_mfma_f32_16x16x32_bf16 v[164:167], a[52:55], a[92:95], v[164:167]\n"
"ds_read_b64_tr_b16 a[26:27], v4 offset:25600\n"
"v_mfma_f32_16x16x32_bf16 v[168:171], a[56:59], a[92:95], v[168:171]\n"
"ds_read_b64_tr_b16 a[28:29], v5 offset:17408\n"
"v_mfma_f32_16x16x32_bf16 v[172:175], a[60:63], a[92:95], v[172:175]\n"
"s_waitcnt lgkmcnt(7)\n"
"ds_read_b64_tr_b16 a[30:31], v5 offset:25600\n"
"v_mfma_f32_16x16x32_bf16 v[128:131], a[0:3], a[96:99], v[128:131]\n"
"ds_read_b64_tr_b16 a[32:33], v2 offset:18432\n"
"v_mfma_f32_16x16x32_bf16 v[132:135], a[4:7], a[96:99], v[132:135]\n"
"ds_read_b64_tr_b16 a[34:35], v2 offset:26624\n"
"v_mfma_f32_16x16x32_bf16 v[136:139], a[8:11], a[96:99], v[136:139]\n"
"ds_read_b64_tr_b16 a[36:37], v3 offset:18432\n"
"v_mfma_f32_16x16x32_bf16 v[140:143], a[12:15], a[96:99], v[140:143]\n"
"ds_read_b64_tr_b16 a[38:39], v3 offset:26624\n"
"v_mfma_f32_16x16x32_bf16 v[160:163], a[0:3], a[100:103], v[160:163]\n"
"ds_read_b64_tr_b16 a[40:41], v4 offset:18432\n"
"v_mfma_f32_16x16x32_bf16 v[164:167], a[4:7], a[100:103], v[164:167]\n"
"ds_read_b64_tr_b16 a[42:43], v4 offset:26624\n"
"v_mfma_f32_16x16x32_bf16 v[168:171], a[8:11], a[100:103], v[168:171]\n"
"ds_read_b64_tr_b16 a[44:45], v5 offset:18432\n"
"v_mov_b32 v22, s80\n"
"v_mfma_f32_16x16x32_bf16 v[172:175], a[12:15], a[100:103], v[172:175]\n"
"s_waitcnt lgkmcnt(7)\n"
"ds_read_b64_tr_b16 a[46:47], v5 offset:26624\n"
"v_mov_b32 v23, s81\n"
"v_mfma_f32_16x16x32_bf16 v[128:131], a[16:19], a[104:107], v[128:131]\n"
"s_waitcnt vmcnt(0)\n"
"s_mov_b64 exec, 1\n"
"buffer_store_dwordx2 v[22:23], off, s[16:19], s63 sc1\n"
"s_mov_b64 exec, -1\n"
"ds_read_b64_tr_b16 a[48:49], v2 offset:19456\n"
"v_mfma_f32_16x16x32_bf16 v[132:135], a[20:23], a[104:107], v[132:135]\n"
"ds_read_b64_tr_b16 a[50:51], v2 offset:27648\n"
"v_mfma_f32_16x16x32_bf16 v[136:139], a[24:27], a[104:107], v[136:139]\n"
"ds_read_b64_tr_b16 a[52:53], v3 offset:19456\n"
"v_add_u32 v2, 98304, v6\n"
"v_mfma_f32_16x16x32_bf16 v[140:143], a[28:31], a[104:107], v[140:143]\n"
"ds_read_b64_tr_b16 a[54:55], v3 offset:27648\n"
"v_mfma_f32_16x16x32_bf16 v[160:163], a[16:19], a[108:111], v[160:163]\n"
"ds_read_b64_tr_b16 a[56:57], v4 offset:19456\n"
"v_add_u32 v3, 98304, v7\n"
"v_mfma_f32_16x16x32_bf16 v[164:167], a[20:23], a[108:111], v[164:167]\n"
"ds_read_b64_tr_b16 a[58:59], v4 offset:27648\n"
"v_mfma_f32_16x16x32_bf16 v[168:171], a[24:27], a[108:111], v[168:171]\n"
"ds_read_b64_tr_b16 a[60:61], v5 offset:19456\n"
"v_add_u32 v4, 98304, v8\n"
"v_mfma_f32_16x16x32_bf16 v[172:175], a[28:31], a[108:111], v[172:175]\n"
"s_waitcnt lgkmcnt(7)\n"
"ds_read_b64_tr_b16 a[62:63], v5 offset:27648\n"
"v_mfma_f32_16x16x32_bf16 v[128:131], a[32:35], a[112:115], v[128:131]\n"
"ds_read_b64_tr_b16 a[0:1], v2 offset:0\n"
"v_add_u32 v5, 98304, v9\n"
"v_mfma_f32_16x16x32_bf16 v[132:135], a[36:39], a[112:115], v[132:135]\n"
"ds_read_b64_tr_b16 a[2:3], v2 offset:8192\n"
"v_mfma_f32_16x16x32_bf16 v[136:139], a[40:43], a[112:115], v[136:139]\n"
"ds_read_b64_tr_b16 a[4:5], v3 offset:0\n"
"v_mfma_f32_16x16x32_bf16 v[140:143], a[44:47], a[112:115], v[140:143]\n"
"ds_read_b64_tr_b16 a[6:7], v3 offset:8192\n"
"v_mfma_f32_16x16x32_bf16 v[160:163], a[32:35], a[116:119], v[160:163]\n"
"ds_read_b64_tr_b16 a[8:9], v4 offset:0\n"
"v_mfma_f32_16x16x32_bf16 v[164:167], a[36:39], a[116:119], v[164:167]\n"
"ds_read_b64_tr_b16 a[10:11], v4 offset:8192\n"
"v_mfma_f32_16x16x32_bf16 v[168:171], a[40:43], a[116:119], v[168:171]\n"
"ds_read_b64_tr_b16 a[12:13], v5 offset:0\n"
"v_mfma_f32_16x16x32_bf16 v[172:175], a[44:47], a[116:119], v[172:175]\n"
"s_waitcnt lgkmcnt(7)\n"
"ds_read_b64_tr_b16 a[14:15], v5 offset:8192\n"
"v_mfma_f32_16x16x32_bf16 v[128:131], a[48:51], a[120:123], v[128:131]\n"
"s_barrier\n"
"ds_read_b64_tr_b16 a[16:17], v2 offset:1024\n"
"v_mfma_f32_16x16x32_bf16 v[132:135], a[52:55], a[120:123], v[132:135]\n"
"s_add_u32 m0, s55, 32768\n"
"ds_read_b64_tr_b16 a[18:19], v2 offset:9216\n"
"v_mfma_f32_16x16x32_bf16 v[136:139], a[56:59], a[120:123], v[136:139]\n"
"ds_read_b64_tr_b16 a[20:21], v3 offset:1024\n"
"buffer_load_dwordx4 v10, s[20:23], s82 offen offset:0 sc1 lds\n"
"v_mfma_f32_16x16x32_bf16 v[140:143], a[60:63], a[120:123], v[140:143]\n"
"ds_read_b64_tr_b16 a[22:23], v3 offset:9216\n"
"v_mfma_f32_16x16x32_bf16 v[160:163], a[48:51], a[124:127], v[160:163]\n"
"ds_read_b64_tr_b16 a[24:25], v4 offset:1024\n"
"v_mfma_f32_16x16x32_bf16 v[164:167], a[52:55], a[124:127], v[164:167]\n"
"ds_read_b64_tr_b16 a[26:27], v4 offset:9216\n"
"v_mfma_f32_16x16x32_bf16 v[168:171], a[56:59], a[124:127], v[168:171]\n"
"ds_read_b64_tr_b16 a[28:29], v5 offset:1024\n"
"buffer_load_dwordx4 v11, s[20:23], s82 offen offset:1024 sc1 lds\n"
"v_mfma_f32_16x16x32_bf16 v[172:175], a[60:63], a[124:127], v[172:175]\n"
"s_waitcnt lgkmcnt(7)\n"
"ds_read_b64_tr_b16 a[30:31], v5 offset:9216\n"
"v_mfma_f32_16x16x32_bf16 v[128:131], a[0:3], v[192:195], v[128:131]\n"
"ds_read_b64_tr_b16 a[32:33], v2 offset:2048\n"
"v_mfma_f32_16x16x32_bf16 v[132:135], a[4:7], v[192:195], v[132:135]\n"
"ds_read_b64_tr_b16 a[34:35], v2 offset:10240\n"
"v_mfma_f32_16x16x32_bf16 v[136:139], a[8:11], v[192:195], v[136:139]\n"
"ds_read_b64_tr_b16 a[36:37], v3 offset:2048\n"
"buffer_load_dwordx4 v12, s[20:23], s82 offen offset:2048 sc1 lds\n"
"v_mfma_f32_16x16x32_bf16 v[140:143], a[12:15], v[192:195], v[140:143]\n"
"ds_read_b64_tr_b16 a[38:39], v3 offset:10240\n"
"v_mfma_f32_16x16x32_bf16 v[160:163], a[0:3], v[196:199], v[160:163]\n"
"ds_read_b64_tr_b16 a[40:41], v4 offset:2048\n"
"v_mfma_f32_16x16x32_bf16 v[164:167], a[4:7], v[196:199], v[164:167]\n"
"ds_read_b64_tr_b16 a[42:43], v4 offset:10240\n"
"v_mfma_f32_16x16x32_bf16 v[168:171], a[8:11], v[196:199], v[168:171]\n"
"ds_read_b64_tr_b16 a[44:45], v5 offset:2048\n"
"buffer_load_dwordx4 v13, s[20:23], s82 offen offset:3072 sc1 lds\n"
"v_mfma_f32_16x16x32_bf16 v[172:175], a[12:15], v[196:199], v[172:175]\n"
"s_waitcnt lgkmcnt(7)\n"
"s_add_u32 s82, s82, s53\n"
"ds_read_b64_tr_b16 a[46:47], v5 offset:10240\n"
"v_mfma_f32_16x16x32_bf16 v[128:131], a[16:19], v[200:203], v[128:131]\n"
"ds_read_b64_tr_b16 a[48:49], v2 offset:3072\n"
"v_mfma_f32_16x16x32_bf16 v[132:135], a[20:23], v[200:203], v[132:135]\n"
"s_add_u32 m0, s55, 49152\n"
"ds_read_b64_tr_b16 a[50:51], v2 offset:11264\n"
"v_mfma_f32_16x16x32_bf16 v[136:139], a[24:27], v[200:203], v[136:139]\n"
"ds_read_b64_tr_b16 a[52:53], v3 offset:3072\n"
"buffer_load_dwordx4 v10, s[20:23], s82 offen offset:0 sc1 lds\n"
"v_mfma_f32_16x16x32_bf16 v[140:143], a[28:31], v[200:203], v[140:143]\n"
"ds_read_b64_tr_b16 a[54:55], v3 offset:11264\n"
"v_mfma_f32_16x16x32_bf16 v[160:163], a[16:19], v[204:207], v[160:163]\n"
"ds_read_b64_tr_b16 a[56:57], v4 offset:3072\n"
"v_mfma_f32_16x16x32_bf16 v[164:167], a[20:23], v[204:207], v[164:167]\n"
"ds_read_b64_tr_b16 a[58:59], v4 offset:11264\n"
"v_mfma_f32_16x16x32_bf16 v[168:171], a[24:27], v[204:207], v[168:171]\n"
"ds_read_b64_tr_b16 a[60:61], v5 offset:3072\n"
"buffer_load_dwordx4 v11, s[20:23], s82 offen offset:1024 sc1 lds\n"
"v_mfma_f32_16x16x32_bf16 v[172:175], a[28:31], v[204:207], v[172:175]\n"
"s_waitcnt lgkmcnt(7)\n"
"ds_read_b64_tr_b16 a[62:63], v5 offset:11264\n"
"v_mfma_f32_16x16x32_bf16 v[128:131], a[32:35], v[208:211], v[128:131]\n"
"ds_read_b64_tr_b16 a[0:1], v2 offset:16384\n"
"v_mfma_f32_16x16x32_bf16 v[132:135], a[36:39], v[208:211], v[132:135]\n"
"ds_read_b64_tr_b16 a[2:3], v2 offset:24576\n"
"v_mfma_f32_16x16x32_bf16 v[136:139], a[40:43], v[208:211], v[136:139]\n"
"ds_read_b64_tr_b16 a[4:5], v3 offset:16384\n"
"buffer_load_dwordx4 v12, s[20:23], s82 offen offset:2048 sc1 lds\n"
"v_mfma_f32_16x16x32_bf16 v[140:143], a[44:47], v[208:211], v[140:143]\n"
"ds_read_b64_tr_b16 a[6:7], v3 offset:24576\n"
"v_mfma_f32_16x16x32_bf16 v[160:163], a[32:35], v[212:215], v[160:163]\n"
"ds_read_b64_tr_b16 a[8:9], v4 offset:16384\n"
"v_mfma_f32_16x16x32_bf16 v[164:167], a[36:39], v[212:215], v[164:167]\n"
"ds_read_b64_tr_b16 a[10:11], v4 offset:24576\n"
"v_mfma_f32_16x16x32_bf16 v[168:171], a[40:43], v[212:215], v[168:171]\n"
"ds_read_b64_tr_b16 a[12:13], v5 offset:16384\n"
"buffer_load_dwordx4 v13, s[20:23], s82 offen offset:3072 sc1 lds\n"
"v_mfma_f32_16x16x32_bf16 v[172:175], a[44:47], v[212:215], v[172:175]\n"
"s_waitcnt lgkmcnt(7)\n"
"s_add_u32 s82, s82, s53\n"
"ds_read_b64_tr_b16 a[14:15], v5 offset:24576\n"
"v_mfma_f32_16x16x32_bf16 v[128:131], a[48:51], v[216:219], v[128:131]\n"
"ds_read_b64_tr_b16 a[16:17], v2 offset:17408\n"
"v_mfma_f32_16x16x32_bf16 v[132:135], a[52:55], v[216:219], v[132:135]\n"
"s_add_u32 m0, s55, 65536\n"
"ds_read_b64_tr_b16 a[18:19], v2 offset:25600\n"
"v_mfma_f32_16x16x32_bf16 v[136:139], a[56:59], v[216:219], v[136:139]\n"
"ds_read_b64_tr_b16 a[20:21], v3 offset:17408\n"
"buffer_load_dwordx4 v10, s[20:23], s82 offen offset:0 sc1 lds\n"
"v_mfma_f32_16x16x32_bf16 v[140:143], a[60:63], v[216:219], v[140:143]\n"
"ds_read_b64_tr_b16 a[22:23], v3 offset:25600\n"
"v_mfma_f32_16x16x32_bf16 v[160:163], a[48:51], v[220:223], v[160:163]\n"
"ds_read_b64_tr_b16 a[24:25], v4 offset:17408\n"
"v_mfma_f32_16x16x32_bf16 v[164:167], a[52:55], v[220:223], v[164:167]\n"
"ds_read_b64_tr_b16 a[26:27], v4 offset:25600\n"
"v_mfma_f32_16x16x32_bf16 v[168:171], a[56:59], v[220:223], v[168:171]\n"
"ds_read_b64_tr_b16 a[28:29], v5 offset:17408\n"
"buffer_load_dwordx4 v11, s[20:23], s82 offen offset:1024 sc1 lds\n"
"v_mfma_f32_16x16x32_bf16 v[172:175], a[60:63], v[220:223], v[172:175]\n"
"s_waitcnt lgkmcnt(7)\n"
"ds_read_b64_tr_b16 a[30:31], v5 offset:25600\n"
"v_mfma_f32_16x16x32_bf16 v[128:131], a[0:3], v[224:227], v[128:131]\n"
"ds_read_b64_tr_b16 a[32:33], v2 offset:18432\n"
"v_mfma_f32_16x16x32_bf16 v[132:135], a[4:7], v[224:227], v[132:135]\n"
"ds_read_b64_tr_b16 a[34:35], v2 offset:26624\n"
"v_mfma_f32_16x16x32_bf16 v[136:139], a[8:11], v[224:227], v[136:139]\n"
"ds_read_b64_tr_b16 a[36:37], v3 offset:18432\n"
"buffer_load_dwordx4 v12, s[20:23], s82 offen offset:2048 sc1 lds\n"
"v_mfma_f32_16x16x32_bf16 v[140:143], a[12:15], v[224:227], v[140:143]\n"
"ds_read_b64_tr_b16 a[38:39], v3 offset:26624\n"
"v_mfma_f32_16x16x32_bf16 v[160:163], a[0:3], v[228:231], v[160:163]\n"
"ds_read_b64_tr_b16 a[40:41], v4 offset:18432\n"
"v_mfma_f32_16x16x32_bf16 v[164:167], a[4:7], v[228:231], v[164:167]\n"
"ds_read_b64_tr_b16 a[42:43], v4 offset:26624\n"
"v_mfma_f32_16x16x32_bf16 v[168:171], a[8:11], v[228:231], v[168:171]\n"
"ds_read_b64_tr_b16 a[44:45], v5 offset:18432\n"
"buffer_load_dwordx4 v13, s[20:23], s82 offen offset:3072 sc1 lds\n"
"v_mfma_f32_16x16x32_bf16 v[172:175], a[12:15], v[228:231], v[172:175]\n"
"s_waitcnt lgkmcnt(7)\n"
"s_add_u32 s82, s82, s53\n"
"ds_read_b64_tr_b16 a[46:47], v5 offset:26624\n"
"v_mfma_f32_16x16x32_bf16 v[128:131], a[16:19], v[232:235], v[128:131]\n"
"ds_read_b64_tr_b16 a[48:49], v2 offset:19456\n"
"buffer_load_dwordx2 v[22:23], v25, s[16:19], s65 offen sc1\n"
"v_mfma_f32_16x16x32_bf16 v[132:135], a[20:23], v[232:235], v[132:135]\n"
"s_add_u32 m0, s55, 81920\n"
"ds_read_b64_tr_b16 a[50:51], v2 offset:27648\n"
"v_mfma_f32_16x16x32_bf16 v[136:139], a[24:27], v[232:235], v[136:139]\n"
"ds_read_b64_tr_b16 a[52:53], v3 offset:19456\n"
"v_add_u32 v2, 131072, v6\n"
"buffer_load_dwordx4 v10, s[20:23], s82 offen offset:0 sc1 lds\n"
"v_mfma_f32_16x16x32_bf16 v[140:143], a[28:31], v[232:235], v[140:143]\n"
"ds_read_b64_tr_b16 a[54:55], v3 offset:27648\n"
"v_mfma_f32_16x16x32_bf16 v[160:163], a[16:19], v[236:239], v[160:163]\n"
"ds_read_b64_tr_b16 a[56:57], v4 offset:19456\n"
"v_add_u32 v3, 131072, v7\n"
"v_mfma_f32_16x16x32_bf16 v[164:167], a[20:23], v[236:239], v[164:167]\n"
"ds_read_b64_tr_b16 a[58:59], v4 offset:27648\n"
"v_mfma_f32_16x16x32_bf16 v[168:171], a[24:27], v[236:239], v[168:171]\n"
"ds_read_b64_tr_b16 a[60:61], v5 offset:19456\n"
"v_add_u32 v4, 131072, v8\n"
"buffer_load_dwordx4 v11, s[20:23], s82 offen offset:1024 sc1 lds\n"
"v_mfma_f32_16x16x32_bf16 v[172:175], a[28:31], v[236:239], v[172:175]\n"
"s_waitcnt lgkmcnt(7)\n"
"ds_read_b64_tr_b16 a[62:63], v5 offset:27648\n"
"v_mfma_f32_16x16x32_bf16 v[128:131], a[32:35], v[240:243], v[128:131]\n"
"ds_read_b64_tr_b16 a[0:1], v2 offset:0\n"
"v_add_u32 v5, 131072, v9\n"
"v_mfma_f32_16x16x32_bf16 v[132:135], a[36:39], v[240:243], v[132:135]\n"
"ds_read_b64_tr_b16 a[2:3], v2 offset:8192\n"
"v_mfma_f32_16x16x32_bf16 v[136:139], a[40:43], v[240:243], v[136:139]\n"
"ds_read_b64_tr_b16 a[4:5], v3 offset:0\n"
"buffer_load_dwordx4 v12, s[20:23], s82 offen offset:2048 sc1 lds\n"
"v_mfma_f32_16x16x32_bf16 v[140:143], a[44:47], v[240:243], v[140:143]\n"
"ds_read_b64_tr_b16 a[6:7], v3 offset:8192\n"
"v_mfma_f32_16x16x32_bf16 v[160:163], a[32:35], v[244:247], v[160:163]\n"
"ds_read_b64_tr_b16 a[8:9], v4 offset:0\n"
"v_mfma_f32_16x16x32_bf16 v[164:167], a[36:39], v[244:247], v[164:167]\n"
"ds_read_b64_tr_b16 a[10:11], v4 offset:8192\n"
"v_mfma_f32_16x16x32_bf16 v[168:171], a[40:43], v[244:247], v[168:171]\n"
"ds_read_b64_tr_b16 a[12:13], v5 offset:0\n"
"buffer_load_dwordx4 v13, s[20:23], s82 offen offset:3072 sc1 lds\n"
"v_mfma_f32_16x16x32_bf16 v[172:175], a[44:47], v[244:247], v[172:175]\n"
"s_waitcnt lgkmcnt(7)\n"
"s_add_u32 s82, s82, s53\n"
"ds_read_b64_tr_b16 a[14:15], v5 offset:8192\n"
"v_mfma_f32_16x16x32_bf16 v[128:131], a[48:51], v[248:251], v[128:131]\n"
"ds_read_b64_tr_b16 a[16:17], v2 offset:1024\n"
"v_mfma_f32_16x16x32_bf16 v[132:135], a[52:55], v[248:251], v[132:135]\n"
"ds_read_b64_tr_b16 a[18:19], v2 offset:9216\n"
"v_mfma_f32_16x16x32_bf16 v[136:139], a[56:59], v[248:251], v[136:139]\n"
"ds_read_b64_tr_b16 a[20:21], v3 offset:1024\n"
"v_mfma_f32_16x16x32_bf16 v[140:143], a[60:63], v[248:251], v[140:143]\n"
"ds_read_b64_tr_b16 a[22:23], v3 offset:9216\n"
"v_mfma_f32_16x16x32_bf16 v[160:163], a[48:51], v[252:255], v[160:163]\n"
"ds_read_b64_tr_b16 a[24:25], v4 offset:1024\n"
"v_mfma_f32_16x16x32_bf16 v[164:167], a[52:55], v[252:255], v[164:167]\n"
"ds_read_b64_tr_b16 a[26:27], v4 offset:9216\n"
"v_mfma_f32_16x16x32_bf16 v[168:171], a[56:59], v[252:255], v[168:171]\n"
"ds_read_b64_tr_b16 a[28:29], v5 offset:1024\n"
"v_mfma_f32_16x16x32_bf16 v[172:175], a[60:63], v[252:255], v[172:175]\n"
"ds_read_b64_tr_b16 a[30:31], v5 offset:9216\n"
"s_waitcnt vmcnt(4)\n"
"v_cmp_ne_u64 vcc, s[80:81], v[22:23]\n"
"s_cbranch_vccnz Lf4s_%=\n"
"Lf4ok_%=:\n"
"buffer_load_dwordx4 a[64:67], v14, s[16:19], s83 offen offset:0 sc1\n"
"v_mfma_f32_16x16x32_bf16 v[128:131], a[224:227], a[160:163], v[128:131]\n"
"buffer_load_dwordx4 a[68:71], v14, s[16:19], s83 offen offset:1024 sc1\n"
"v_mfma_f32_16x16x32_bf16 v[132:135], a[228:231], a[160:163], v[132:135]\n"
"buffer_load_dwordx4 a[72:75], v15, s[16:19], s83 offen offset:0 sc1\n"
"v_mfma_f32_16x16x32_bf16 v[136:139], a[224:227], a[164:167], v[136:139]\n"
"buffer_load_dwordx4 a[76:79], v15, s[16:19], s83 offen offset:1024 sc1\n"
"v_mfma_f32_16x16x32_bf16 v[140:143], a[228:231], a[164:167], v[140:143]\n"
"s_add_u32 s83, s83, s54\n"
"v_mfma_f32_16x16x32_bf16 v[160:163], a[224:227], a[168:171], v[160:163]\n"
"buffer_load_dwordx4 a[80:83], v14, s[16:19], s83 offen offset:0 sc1\n"
"v_mfma_f32_16x16x32_bf16 v[164:167], a[228:231], a[168:171], v[164:167]\n"
"buffer_load_dwordx4 a[84:87], v14, s[16:19], s83 offen offset:1024 sc1\n"
"v_mfma_f32_16x16x32_bf16 v[168:171], a[224:227], a[172:175], v[168:171]\n"
"buffer_load_dwordx4 a[88:91], v15, s[16:19], s83 offen offset:0 sc1\n"
"v_mfma_f32_16x16x32_bf16 v[172:175], a[228:231], a[172:175], v[172:175]\n"
"buffer_load_dwordx4 a[92:95], v15, s[16:19], s83 offen offset:1024 sc1\n"
"v_mfma_f32_16x16x32_bf16 v[128:131], a[224:227], v[144:147], v[128:131]\n"
"s_add_u32 s83, s83, s54\n"
"v_mfma_f32_16x16x32_bf16 v[132:135], a[228:231], v[144:147], v[132:135]\n"
"buffer_load_dwordx4 a[96:99], v14, s[16:19], s83 offen offset:0 sc1\n"
"v_mfma_f32_16x16x32_bf16 v[136:139], a[224:227], v[148:151], v[136:139]\n"
"buffer_load_dwordx4 a[100:103], v14, s[16:19], s83 offen offset:1024 sc1\n"
"v_mfma_f32_16x16x32_bf16 v[140:143], a[228:231], v[148:151], v[140:143]\n"
"buffer_load_dwordx4 a[104:107], v15, s[16:19], s83 offen offset:0 sc1\n"
"v_mfma_f32_16x16x32_bf16 v[160:163], a[224:227], v[152:155], v[160:163]\n"
"buffer_load_dwordx4 a[108:111], v15, s[16:19], s83 offen offset:1024 sc1\n"
"v_mfma_f32_16x16x32_bf16 v[164:167], a[228:231], v[152:155], v[164:167]\n"
"s_add_u32 s83, s83, s54\n"
"v_mfma_f32_16x16x32_bf16 v[168:171], a[224:227], v[156:159], v[168:171]\n"
"buffer_load_dwordx4 a[112:115], v14, s[16:19], s83 offen offset:0 sc1\n"
"v_mfma_f32_16x16x32_bf16 v[172:175], a[228:231], v[156:159], v[172:175]\n"
"buffer_load_dwordx4 a[116:119], v14, s[16:19], s83 offen offset:1024 sc1\n"
"v_mfma_f32_16x16x32_bf16 v[128:131], a[224:227], v[176:179], v[128:131]\n"
"buffer_load_dwordx4 a[120:123], v15, s[16:19], s83 offen offset:0 sc1\n"
"v_mfma_f32_16x16x32_bf16 v[132:135], a[228:231], v[176:179], v[132:135]\n"
"buffer_load_dwordx4 a[124:127], v15, s[16:19], s83 offen offset:1024 sc1\n"
"v_mfma_f32_16x16x32_bf16 v[136:139], a[224:227], v[180:183], v[136:139]\n"
"s_add_u32 s83, s83, s54\n"
"v_mfma_f32_16x16x32_bf16 v[140:143], a[228:231], v[180:183], v[140:143]\n"
"v_mfma_f32_16x16x32_bf16 v[160:163], a[224:227], v[184:187], v[160:163]\n"
"v_mfma_f32_16x16x32_bf16 v[164:167], a[228:231], v[184:187], v[164:167]\n"
"v_mfma_f32_16x16x32_bf16 v[168:171], a[224:227], v[188:191], v[168:171]\n"
"v_mfma_f32_16x16x32_bf16 v[172:175], a[228:231], v[188:191], v[172:175]\n"
"s_waitcnt vmcnt(15) lgkmcnt(7)\n"
"v_mfma_f32_16x16x32_bf16 v[128:131], a[0:3], a[64:67], v[128:131]\n"
"ds_read_b64_tr_b16 a[32:33], v2 offset:2048\n"
"v_mfma_f32_16x16x32_bf16 v[132:135], a[4:7], a[64:67], v[132:135]\n"
"ds_read_b64_tr_b16 a[34:35], v2 offset:10240\n"
"v_mfma_f32_16x16x32_bf16 v[136:139], a[8:11], a[64:67], v[136:139]\n"
"ds_read_b64_tr_b16 a[36:37], v3 offset:2048\n"
"v_mfma_f32_16x16x32_bf16 v[140:143], a[12:15], a[64:67], v[140:143]\n"
"s_waitcnt vmcnt(14)\n"
"ds_read_b64_tr_b16 a[38:39], v3 offset:10240\n"
"buffer_load_dwordx4 a[64:67], v14, s[16:19], s83 offen offset:0 sc1\n"
"v_mfma_f32_16x16x32_bf16 v[160:163], a[0:3], a[68:71], v[160:163]\n"
"ds_read_b64_tr_b16 a[40:41], v4 offset:2048\n"
"v_mfma_f32_16x16x32_bf16 v[164:167], a[4:7], a[68:71], v[164:167]\n"
"ds_read_b64_tr_b16 a[42:43], v4 offset:10240\n"
"v_mfma_f32_16x16x32_bf16 v[168:171], a[8:11], a[68:71], v[168:171]\n"
"ds_read_b64_tr_b16 a[44:45], v5 offset:2048\n"
"v_mfma_f32_16x16x32_bf16 v[172:175], a[12:15], a[68:71], v[172:175]\n"
"s_waitcnt vmcnt(14) lgkmcnt(7)\n"
"ds_read_b64_tr_b16 a[46:47], v5 offset:10240\n"
"buffer_load_dwordx4 a[68:71], v14, s[16:19], s83 offen offset:1024 sc1\n"
"v_mfma_f32_16x16x32_bf16 v[128:131], a[16:19], a[72:75], v[128:131]\n"
"ds_read_b64_tr_b16 a[48:49], v2 offset:3072\n"
"v_mfma_f32_16x16x32_bf16 v[132:135], a[20:23], a[72:75], v[132:135]\n"
"ds_read_b64_tr_b16 a[50:51], v2 offset:11264\n"
"v_mfma_f32_16x16x32_bf16 v[136:139], a[24:27], a[72:75], v[136:139]\n"
"ds_read_b64_tr_b16 a[52:53], v3 offset:3072\n"
"v_mfma_f32_16x16x32_bf16 v[140:143], a[28:31], a[72:75], v[140:143]\n"
"s_waitcnt vmcnt(14)\n"
"ds_read_b64_tr_b16 a[54:55], v3 offset:11264\n"
"buffer_load_dwordx4 a[72:75], v15, s[16:19], s83 offen offset:0 sc1\n"
"v_mfma_f32_16x16x32_bf16 v[160:163], a[16:19], a[76:79], v[160:163]\n"
"ds_read_b64_tr_b16 a[56:57], v4 offset:3072\n"
"v_mfma_f32_16x16x32_bf16 v[164:167], a[20:23], a[76:79], v[164:167]\n"
"ds_read_b64_tr_b16 a[58:59], v4 offset:11264\n"
"v_mfma_f32_16x16x32_bf16 v[168:171], a[24:27], a[76:79], v[168:171]\n"
"ds_read_b64_tr_b16 a[60:61], v5 offset:3072\n"
"v_mfma_f32_16x16x32_bf16 v[172:175], a[28:31], a[76:79], v[172:175]\n"
"s_waitcnt vmcnt(14) lgkmcnt(7)\n"
"ds_read_b64_tr_b16 a[62:63], v5 offset:11264\n"
"buffer_load_dwordx4 a[76:79], v15, s[16:19], s83 offen offset:1024 sc1\n"
"v_mfma_f32_16x16x32_bf16 v[128:131], a[32:35], a[80:83], v[128:131]\n"
"s_add_u32 s83, s83, s54\n"
"ds_read_b64_tr_b16 a[0:1], v2 offset:16384\n"
"v_mfma_f32_16x16x32_bf16 v[132:135], a[36:39], a[80:83], v[132:135]\n"
"s_waitcnt vmcnt(20)\n"
"s_barrier\n"
"ds_read_b64_tr_b16 a[2:3], v2 offset:24576\n"
"v_mfma_f32_16x16x32_bf16 v[136:139], a[40:43], a[80:83], v[136:139]\n"
"ds_read_b64_tr_b16 a[4:5], v3 offset:16384\n"
"v_mfma_f32_16x16x32_bf16 v[140:143], a[44:47], a[80:83], v[140:143]\n"
"s_waitcnt vmcnt(14)\n"
"ds_read_b64_tr_b16 a[6:7], v3 offset:24576\n"
"buffer_load_dwordx4 a[80:83], v14, s[16:19], s83 offen offset:0 sc1\n"
"v_mfma_f32_16x16x32_bf16 v[160:163], a[32:35], a[84:87], v[160:163]\n"
"ds_read_b64_tr_b16 a[8:9], v4 offset:16384\n"
"v_mfma_f32_16x16x32_bf16 v[164:167], a[36:39], a[84:87], v[164:167]\n"
"ds_read_b64_tr_b16 a[10:11], v4 offset:24576\n"
"v_mfma_f32_16x16x32_bf16 v[168:171], a[40:43], a[84:87], v[168:171]\n"
"ds_read_b64_tr_b16 a[12:13], v5 offset:16384\n"
"v_mfma_f32_16x16x32_bf16 v[172:175], a[44:47], a[84:87], v[172:175]\n"
"s_waitcnt vmcnt(14) lgkmcnt(7)\n"
"ds_read_b64_tr_b16 a[14:15], v5 offset:24576\n"
"buffer_load_dwordx4 a[84:87], v14, s[16:19], s83 offen offset:1024 sc1\n"
"v_mfma_f32_16x16x32_bf16 v[128:131], a[48:51], a[88:91], v[128:131]\n"
"ds_read_b64_tr_b16 a[16:17], v2 offset:17408\n"
"v_mfma_f32_16x16x32_bf16 v[132:135], a[52:55], a[88:91], v[132:135]\n"
"ds_read_b64_tr_b16 a[18:19], v2 offset:25600\n"
"v_mfma_f32_16x16x32_bf16 v[136:139], a[56:59], a[88:91], v[136:139]\n"
"ds_read_b64_tr_b16 a[20:21], v3 offset:17408\n"
"v_mfma_f32_16x16x32_bf16 v[140:143], a[60:63], a[88:91], v[140:143]\n"
"s_waitcnt vmcnt(14)\n"
"ds_read_b64_tr_b16 a[22:23], v3 offset:25600\n"
"buffer_load_dwordx4 a[88:91], v15, s[16:19], s83 offen offset:0 sc1\n"
"v_mfma_f32_16x16x32_bf16 v[160:163], a[48:51], a[92:95], v[160:163]\n"
"ds_read_b64_tr_b16 a[24:25], v4 offset:17408\n"
"v_mfma_f32_16x16x32_bf16 v[164:167], a[52:55], a[92:95], v[164:167]\n"
"ds_read_b64_tr_b16 a[26:27], v4 offset:25600\n"
"v_mfma_f32_16x16x32_bf16 v[168:171], a[56:59], a[92:95], v[168:171]\n"
"ds_read_b64_tr_b16 a[28:29], v5 offset:17408\n"
"v_mfma_f32_16x16x32_bf16 v[172:175], a[60:63], a[92:95], v[172:175]\n"
"s_waitcnt vmcnt(14) lgkmcnt(7)\n"
"ds_read_b64_tr_b16 a[30:31], v5 offset:25600\n"
"buffer_load_dwordx4 a[92:95], v15, s[16:19], s83 offen offset:1024 sc1\n"
"v_mfma_f32_16x16x32_bf16 v[128:131], a[0:3], a[96:99], v[128:131]\n"
"s_add_u32 s83, s83, s54\n"
"ds_read_b64_tr_b16 a[32:33], v2 offset:18432\n"
"v_mfma_f32_16x16x32_bf16 v[132:135], a[4:7], a[96:99], v[132:135]\n"
"ds_read_b64_tr_b16 a[34:35], v2 offset:26624\n"
"v_mfma_f32_16x16x32_bf16 v[136:139], a[8:11], a[96:99], v[136:139]\n"
"ds_read_b64_tr_b16 a[36:37], v3 offset:18432\n"
"v_mfma_f32_16x16x32_bf16 v[140:143], a[12:15], a[96:99], v[140:143]\n"
"s_waitcnt vmcnt(14)\n"
"ds_read_b64_tr_b16 a[38:39], v3 offset:26624\n"
"buffer_load_dwordx4 a[96:99], v14, s[16:19], s83 offen offset:0 sc1\n"
"v_mfma_f32_16x16x32_bf16 v[160:163], a[0:3], a[100:103], v[160:163]\n"
"ds_read_b64_tr_b16 a[40:41], v4 offset:18432\n"
"v_mfma_f32_16x16x32_bf16 v[164:167], a[4:7], a[100:103], v[164:167]\n"
"ds_read_b64_tr_b16 a[42:43], v4 offset:26624\n"
"v_mfma_f32_16x16x32_bf16 v[168:171], a[8:11], a[100:103], v[168:171]\n"
"ds_read_b64_tr_b16 a[44:45], v5 offset:18432\n"
"v_mfma_f32_16x16x32_bf16 v[172:175], a[12:15], a[100:103], v[172:175]\n"
"s_waitcnt vmcnt(14) lgkmcnt(7)\n"
"ds_read_b64_tr_b16 a[46:47], v5 offset:26624\n"
"buffer_load_dwordx4 a[100:103], v14, s[16:19], s83 offen offset:1024 sc1\n"
"v_mfma_f32_16x16x32_bf16 v[128:131], a[16:19], a[104:107], v[128:131]\n"
"ds_read_b64_tr_b16 a[48:49], v2 offset:19456\n"
"v_mfma_f32_16x16x32_bf16 v[132:135], a[20:23], a[104:107], v[132:135]\n"
"ds_read_b64_tr_b16 a[50:51], v2 offset:27648\n"
"v_mfma_f32_16x16x32_bf16 v[136:139], a[24:27], a[104:107], v[136:139]\n"
"ds_read_b64_tr_b16 a[52:53], v3 offset:19456\n"
"v_add_u32 v2, 0, v6\n"
"v_mfma_f32_16x16x32_bf16 v[140:143], a[28:31], a[104:107], v[140:143]\n"
"s_waitcnt vmcnt(14)\n"
"ds_read_b64_tr_b16 a[54:55], v3 offset:27648\n"
"buffer_load_dwordx4 a[104:107], v15, s[16:19], s83 offen offset:0 sc1\n"
"v_mfma_f32_16x16x32_bf16 v[160:163], a[16:19], a[108:111], v[160:163]\n"
"ds_read_b64_tr_b16 a[56:57], v4 offset:19456\n"
"v_add_u32 v3, 0, v7\n"
"v_mfma_f32_16x16x32_bf16 v[164:167], a[20:23], a[108:111], v[164:167]\n"
"ds_read_b64_tr_b16 a[58:59], v4 offset:27648\n"
"v_mfma_f32_16x16x32_bf16 v[168:171], a[24:27], a[108:111], v[168:171]\n"
"ds_read_b64_tr_b16 a[60:61], v5 offset:19456\n"
"v_add_u32 v4, 0, v8\n"
"v_mfma_f32_16x16x32_bf16 v[172:175], a[28:31], a[108:111], v[172:175]\n"
"s_waitcnt vmcnt(14) lgkmcnt(7)\n"
"ds_read_b64_tr_b16 a[62:63], v5 offset:27648\n"
"buffer_load_dwordx4 a[108:111], v15, s[16:19], s83 offen offset:1024 sc1\n"
"v_mfma_f32_16x16x32_bf16 v[128:131], a[32:35], a[112:115], v[128:131]\n"
"s_add_u32 s83, s83, s54\n"
"ds_read_b64_tr_b16 a[0:1], v2 offset:0\n"
"v_add_u32 v5, 0, v9\n"
"v_mfma_f32_16x16x32_bf16 v[132:135], a[36:39], a[112:115], v[132:135]\n"
"ds_read_b64_tr_b16 a[2:3], v2 offset:8192\n"
"v_mfma_f32_16x16x32_bf16 v[136:139], a[40:43], a[112:115], v[136:139]\n"
"ds_read_b64_tr_b16 a[4:5], v3 offset:0\n"
"v_mfma_f32_16x16x32_bf16 v[140:143], a[44:47], a[112:115], v[140:143]\n"
"s_waitcnt vmcnt(14)\n"
"ds_read_b64_tr_b16 a[6:7], v3 offset:8192\n"
"buffer_load_dwordx4 a[112:115], v14, s[16:19], s83 offen offset:0 sc1\n"
"v_mfma_f32_16x16x32_bf16 v[160:163], a[32:35], a[116:119], v[160:163]\n"
"ds_read_b64_tr_b16 a[8:9], v4 offset:0\n"
"v_mfma_f32_16x16x32_bf16 v[164:167], a[36:39], a[116:119], v[164:167]\n"
"ds_read_b64_tr_b16 a[10:11], v4 offset:8192\n"
"v_mfma_f32_16x16x32_bf16 v[168:171], a[40:43], a[116:119], v[168:171]\n"
"ds_read_b64_tr_b16 a[12:13], v5 offset:0\n"
"v_mfma_f32_16x16x32_bf16 v[172:175], a[44:47], a[116:119], v[172:175]\n"
"s_waitcnt vmcnt(14) lgkmcnt(7)\n"
"ds_read_b64_tr_b16 a[14:15], v5 offset:8192\n"
"buffer_load_dwordx4 a[116:119], v14, s[16:19], s83 offen offset:1024 sc1\n"
"v_mfma_f32_16x16x32_bf16 v[128:131], a[48:51], a[120:123], v[128:131]\n"
"ds_read_b64_tr_b16 a[16:17], v2 offset:1024\n"
"v_mfma_f32_16x16x32_bf16 v[132:135], a[52:55], a[120:123], v[132:135]\n"
"ds_read_b64_tr_b16 a[18:19], v2 offset:9216\n"
"v_mfma_f32_16x16x32_bf16 v[136:139], a[56:59], a[120:123], v[136:139]\n"
"ds_read_b64_tr_b16 a[20:21], v3 offset:1024\n"
"v_mfma_f32_16x16x32_bf16 v[140:143], a[60:63], a[120:123], v[140:143]\n"
"s_waitcnt vmcnt(14)\n"
"ds_read_b64_tr_b16 a[22:23], v3 offset:9216\n"
"buffer_load_dwordx4 a[120:123], v15, s[16:19], s83 offen offset:0 sc1\n"
"v_mfma_f32_16x16x32_bf16 v[160:163], a[48:51], a[124:127], v[160:163]\n"
"ds_read_b64_tr_b16 a[24:25], v4 offset:1024\n"
"v_mfma_f32_16x16x32_bf16 v[164:167], a[52:55], a[124:127], v[164:167]\n"
"ds_read_b64_tr_b16 a[26:27], v4 offset:9216\n"
"v_mfma_f32_16x16x32_bf16 v[168:171], a[56:59], a[124:127], v[168:171]\n"
"ds_read_b64_tr_b16 a[28:29], v5 offset:1024\n"
"v_mfma_f32_16x16x32_bf16 v[172:175], a[60:63], a[124:127], v[172:175]\n"
"s_waitcnt vmcnt(14) lgkmcnt(7)\n"
"ds_read_b64_tr_b16 a[30:31], v5 offset:9216\n"
"buffer_load_dwordx4 a[124:127], v15, s[16:19], s83 offen offset:1024 sc1\n"
"v_mfma_f32_16x16x32_bf16 v[128:131], a[0:3], a[64:67], v[128:131]\n"
"s_add_u32 s83, s83, s54\n"
"ds_read_b64_tr_b16 a[32:33], v2 offset:2048\n"
"v_mfma_f32_16x16x32_bf16 v[132:135], a[4:7], a[64:67], v[132:135]\n"
"ds_read_b64_tr_b16 a[34:35], v2 offset:10240\n"
"v_mfma_f32_16x16x32_bf16 v[136:139], a[8:11], a[64:67], v[136:139]\n"
"ds_read_b64_tr_b16 a[36:37], v3 offset:2048\n"
"v_mfma_f32_16x16x32_bf16 v[140:143], a[12:15], a[64:67], v[140:143]\n"
"s_waitcnt vmcnt(14)\n"
"ds_read_b64_tr_b16 a[38:39], v3 offset:10240\n"
"buffer_load_dwordx4 a[64:67], v14, s[16:19], s83 offen offset:0 sc1\n"
"v_mfma_f32_16x16x32_bf16 v[160:163], a[0:3], a[68:71], v[160:163]\n"
"ds_read_b64_tr_b16 a[40:41], v4 offset:2048\n"
"v_mfma_f32_16x16x32_bf16 v[164:167], a[4:7], a[68:71], v[164:167]\n"
"ds_read_b64_tr_b16 a[42:43], v4 offset:10240\n"
"v_mfma_f32_16x16x32_bf16 v[168:171], a[8:11], a[68:71], v[168:171]\n"
"ds_read_b64_tr_b16 a[44:45], v5 offset:2048\n"
"v_mfma_f32_16x16x32_bf16 v[172:175], a[12:15], a[68:71], v[172:175]\n"
"s_waitcnt vmcnt(14) lgkmcnt(7)\n"
"ds_read_b64_tr_b16 a[46:47], v5 offset:10240\n"
"buffer_load_dwordx4 a[68:71], v14, s[16:19], s83 offen offset:1024 sc1\n"
"v_mfma_f32_16x16x32_bf16 v[128:131], a[16:19], a[72:75], v[128:131]\n"
"ds_read_b64_tr_b16 a[48:49], v2 offset:3072\n"
"v_mfma_f32_16x16x32_bf16 v[132:135], a[20:23], a[72:75], v[132:135]\n"
"ds_read_b64_tr_b16 a[50:51], v2 offset:11264\n"
"v_mfma_f32_16x16x32_bf16 v[136:139], a[24:27], a[72:75], v[136:139]\n"
"ds_read_b64_tr_b16 a[52:53], v3 offset:3072\n"
"v_mfma_f32_16x16x32_bf16 v[140:143], a[28:31], a[72:75], v[140:143]\n"
"s_waitcnt vmcnt(14)\n"
"ds_read_b64_tr_b16 a[54:55], v3 offset:11264\n"
"buffer_load_dwordx4 a[72:75], v15, s[16:19], s83 offen offset:0 sc1\n"
"v_mfma_f32_16x16x32_bf16 v[160:163], a[16:19], a[76:79], v[160:163]\n"
"ds_read_b64_tr_b16 a[56:57], v4 offset:3072\n"
"v_mfma_f32_16x16x32_bf16 v[164:167], a[20:23], a[76:79], v[164:167]\n"
"ds_read_b64_tr_b16 a[58:59], v4 offset:11264\n"
"v_mfma_f32_16x16x32_bf16 v[168:171], a[24:27], a[76:79], v[168:171]\n"
"ds_read_b64_tr_b16 a[60:61], v5 offset:3072\n"
"v_mfma_f32_16x16x32_bf16 v[172:175], a[28:31], a[76:79], v[172:175]\n"
"s_waitcnt vmcnt(14) lgkmcnt(7)\n"
"ds_read_b64_tr_b16 a[62:63], v5 offset:11264\n"
"buffer_load_dwordx4 a[76:79], v15, s[16:19], s83 offen offset:1024 sc1\n"
"v_mfma_f32_16x16x32_bf16 v[128:131], a[32:35], a[80:83], v[128:131]\n"
"s_add_u32 s83, s83, s54\n"
"ds_read_b64_tr_b16 a[0:1], v2 offset:16384\n"
"v_mfma_f32_16x16x32_bf16 v[132:135], a[36:39], a[80:83], v[132:135]\n"
"ds_read_b64_tr_b16 a[2:3], v2 offset:24576\n"
"v_mfma_f32_16x16x32_bf16 v[136:139], a[40:43], a[80:83], v[136:139]\n"
"ds_read_b64_tr_b16 a[4:5], v3 offset:16384\n"
"v_mfma_f32_16x16x32_bf16 v[140:143], a[44:47], a[80:83], v[140:143]\n"
"s_waitcnt vmcnt(14)\n"
"ds_read_b64_tr_b16 a[6:7], v3 offset:24576\n"
"buffer_load_dwordx4 a[80:83], v14, s[16:19], s83 offen offset:0 sc1\n"
"v_mfma_f32_16x16x32_bf16 v[160:163], a[32:35], a[84:87], v[160:163]\n"
"ds_read_b64_tr_b16 a[8:9], v4 offset:16384\n"
"v_mfma_f32_16x16x32_bf16 v[164:167], a[36:39], a[84:87], v[164:167]\n"
"ds_read_b64_tr_b16 a[10:11], v4 offset:24576\n"
"v_mfma_f32_16x16x32_bf16 v[168:171], a[40:43], a[84:87], v[168:171]\n"
"ds_read_b64_tr_b16 a[12:13], v5 offset:16384\n"
"v_mfma_f32_16x16x32_bf16 v[172:175], a[44:47], a[84:87], v[172:175]\n"
"s_waitcnt vmcnt(14) lgkmcnt(7)\n"
"ds_read_b64_tr_b16 a[14:15], v5 offset:24576\n"
"buffer_load_dwordx4 a[84:87], v14, s[16:19], s83 offen offset:1024 sc1\n"
"v_mfma_f32_16x16x32_bf16 v[128:131], a[48:51], a[88:91], v[128:131]\n"
"ds_read_b64_tr_b16 a[16:17], v2 offset:17408\n"
"v_mfma_f32_16x16x32_bf16 v[132:135], a[52:55], a[88:91], v[132:135]\n"
"ds_read_b64_tr_b16 a[18:19], v2 offset:25600\n"
"v_mfma_f32_16x16x32_bf16 v[136:139], a[56:59], a[88:91], v[136:139]\n"
"ds_read_b64_tr_b16 a[20:21], v3 offset:17408\n"
"v_mfma_f32_16x16x32_bf16 v[140:143], a[60:63], a[88:91], v[140:143]\n"
"s_waitcnt vmcnt(14)\n"
"ds_read_b64_tr_b16 a[22:23], v3 offset:25600\n"
"buffer_load_dwordx4 a[88:91], v15, s[16:19], s83 offen offset:0 sc1\n"
"v_mfma_f32_16x16x32_bf16 v[160:163], a[48:51], a[92:95], v[160:163]\n"
"ds_read_b64_tr_b16 a[24:25], v4 offset:17408\n"
"v_mfma_f32_16x16x32_bf16 v[164:167], a[52:55], a[92:95], v[164:167]\n"
"ds_read_b64_tr_b16 a[26:27], v4 offset:25600\n"
"v_mfma_f32_16x16x32_bf16 v[168:171], a[56:59], a[92:95], v[168:171]\n"
"ds_read_b64_tr_b16 a[28:29], v5 offset:17408\n"
"v_mfma_f32_16x16x32_bf16 v[172:175], a[60:63], a[92:95], v[172:175]\n"
"s_waitcnt vmcnt(14) lgkmcnt(7)\n"
"ds_read_b64_tr_b16 a[30:31], v5 offset:25600\n"
"buffer_load_dwordx4 a[92:95], v15, s[16:19], s83 offen offset:1024 sc1\n"
"v_mfma_f32_16x16x32_bf16 v[128:131], a[0:3], a[96:99], v[128:131]\n"
"s_add_u32 s83, s83, s54\n"
"ds_read_b64_tr_b16 a[32:33], v2 offset:18432\n"
"v_mfma_f32_16x16x32_bf16 v[132:135], a[4:7], a[96:99], v[132:135]\n"
"ds_read_b64_tr_b16 a[34:35], v2 offset:26624\n"
"v_mfma_f32_16x16x32_bf16 v[136:139], a[8:11], a[96:99], v[136:139]\n"
"ds_read_b64_tr_b16 a[36:37], v3 offset:18432\n"
"v_mfma_f32_16x16x32_bf16 v[140:143], a[12:15], a[96:99], v[140:143]\n"
"s_waitcnt vmcnt(14)\n"
"ds_read_b64_tr_b16 a[38:39], v3 offset:26624\n"
"buffer_load_dwordx4 a[96:99], v14, s[16:19], s83 offen offset:0 sc1\n"
"v_mfma_f32_16x16x32_bf16 v[160:163], a[0:3], a[100:103], v[160:163]\n"
"ds_read_b64_tr_b16 a[40:41], v4 offset:18432\n"
"v_mfma_f32_16x16x32_bf16 v[164:167], a[4:7], a[100:103], v[164:167]\n"
"ds_read_b64_tr_b16 a[42:43], v4 offset:26624\n"
"v_mfma_f32_16x16x32_bf16 v[168:171], a[8:11], a[100:103], v[168:171]\n"
"ds_read_b64_tr_b16 a[44:45], v5 offset:18432\n"
"v_mfma_f32_16x16x32_bf16 v[172:175], a[12:15], a[100:103], v[172:175]\n"
"s_waitcnt vmcnt(14) lgkmcnt(7)\n"
"ds_read_b64_tr_b16 a[46:47], v5 offset:26624\n"
"buffer_load_dwordx4 a[100:103], v14, s[16:19], s83 offen offset:1024 sc1\n"
"v_mfma_f32_16x16x32_bf16 v[128:131], a[16:19], a[104:107], v[128:131]\n"
"ds_read_b64_tr_b16 a[48:49], v2 offset:19456\n"
"v_mfma_f32_16x16x32_bf16 v[132:135], a[20:23], a[104:107], v[132:135]\n"
"ds_read_b64_tr_b16 a[50:51], v2 offset:27648\n"
"v_mfma_f32_16x16x32_bf16 v[136:139], a[24:27], a[104:107], v[136:139]\n"
"ds_read_b64_tr_b16 a[52:53], v3 offset:19456\n"
"v_add_u32 v2, 32768, v6\n"
"v_mfma_f32_16x16x32_bf16 v[140:143], a[28:31], a[104:107], v[140:143]\n"
"s_waitcnt vmcnt(14)\n"
"ds_read_b64_tr_b16 a[54:55], v3 offset:27648\n"
"buffer_load_dwordx4 a[104:107], v15, s[16:19], s83 offen offset:0 sc1\n"
"v_mfma_f32_16x16x32_bf16 v[160:163], a[16:19], a[108:111], v[160:163]\n"
"ds_read_b64_tr_b16 a[56:57], v4 offset:19456\n"
"v_add_u32 v3, 32768, v7\n"
"v_mfma_f32_16x16x32_bf16 v[164:167], a[20:23], a[108:111], v[164:167]\n"
"ds_read_b64_tr_b16 a[58:59], v4 offset:27648\n"
"v_mfma_f32_16x16x32_bf16 v[168:171], a[24:27], a[108:111], v[168:171]\n"
"ds_read_b64_tr_b16 a[60:61], v5 offset:19456\n"
"v_add_u32 v4, 32768, v8\n"
"v_mfma_f32_16x16x32_bf16 v[172:175], a[28:31], a[108:111], v[172:175]\n"
"s_waitcnt vmcnt(14) lgkmcnt(7)\n"
"ds_read_b64_tr_b16 a[62:63], v5 offset:27648\n"
"buffer_load_dwordx4 a[108:111], v15, s[16:19], s83 offen offset:1024 sc1\n"
"v_mfma_f32_16x16x32_bf16 v[128:131], a[32:35], a[112:115], v[128:131]\n"
"s_add_u32 s83, s83, s54\n"
"ds_read_b64_tr_b16 a[0:1], v2 offset:0\n"
"v_add_u32 v5, 32768, v9\n"
"v_mfma_f32_16x16x32_bf16 v[132:135], a[36:39], a[112:115], v[132:135]\n"
"ds_read_b64_tr_b16 a[2:3], v2 offset:8192\n"
"v_mfma_f32_16x16x32_bf16 v[136:139], a[40:43], a[112:115], v[136:139]\n"
"ds_read_b64_tr_b16 a[4:5], v3 offset:0\n"
"v_mfma_f32_16x16x32_bf16 v[140:143], a[44:47], a[112:115], v[140:143]\n"
"s_waitcnt vmcnt(14)\n"
"ds_read_b64_tr_b16 a[6:7], v3 offset:8192\n"
"buffer_load_dwordx4 a[112:115], v14, s[16:19], s83 offen offset:0 sc1\n"
"v_mfma_f32_16x16x32_bf16 v[160:163], a[32:35], a[116:119], v[160:163]\n"
"ds_read_b64_tr_b16 a[8:9], v4 offset:0\n"
"v_mfma_f32_16x16x32_bf16 v[164:167], a[36:39], a[116:119], v[164:167]\n"
"ds_read_b64_tr_b16 a[10:11], v4 offset:8192\n"
"v_mfma_f32_16x16x32_bf16 v[168:171], a[40:43], a[116:119], v[168:171]\n"
"ds_read_b64_tr_b16 a[12:13], v5 offset:0\n"
"v_mfma_f32_16x16x32_bf16 v[172:175], a[44:47], a[116:119], v[172:175]\n"
"s_waitcnt vmcnt(14) lgkmcnt(7)\n"
"ds_read_b64_tr_b16 a[14:15], v5 offset:8192\n"
"buffer_load_dwordx4 a[116:119], v14, s[16:19], s83 offen offset:1024 sc1\n"
"v_mfma_f32_16x16x32_bf16 v[128:131], a[48:51], a[120:123], v[128:131]\n"
"ds_read_b64_tr_b16 a[16:17], v2 offset:1024\n"
"v_mfma_f32_16x16x32_bf16 v[132:135], a[52:55], a[120:123], v[132:135]\n"
"ds_read_b64_tr_b16 a[18:19], v2 offset:9216\n"
"v_mfma_f32_16x16x32_bf16 v[136:139], a[56:59], a[120:123], v[136:139]\n"
"ds_read_b64_tr_b16 a[20:21], v3 offset:1024\n"
"v_mfma_f32_16x16x32_bf16 v[140:143], a[60:63], a[120:123], v[140:143]\n"
"s_waitcnt vmcnt(14)\n"
"ds_read_b64_tr_b16 a[22:23], v3 offset:9216\n"
"buffer_load_dwordx4 a[120:123], v15, s[16:19], s83 offen offset:0 sc1\n"
"v_mfma_f32_16x16x32_bf16 v[160:163], a[48:51], a[124:127], v[160:163]\n"
"ds_read_b64_tr_b16 a[24:25], v4 offset:1024\n"
"v_mfma_f32_16x16x32_bf16 v[164:167], a[52:55], a[124:127], v[164:167]\n"
"ds_read_b64_tr_b16 a[26:27], v4 offset:9216\n"
"v_mfma_f32_16x16x32_bf16 v[168:171], a[56:59], a[124:127], v[168:171]\n"
"ds_read_b64_tr_b16 a[28:29], v5 offset:1024\n"
"v_mfma_f32_16x16x32_bf16 v[172:175], a[60:63], a[124:127], v[172:175]\n"
"s_waitcnt vmcnt(14) lgkmcnt(7)\n"
"ds_read_b64_tr_b16 a[30:31], v5 offset:9216\n"
"buffer_load_dwordx4 a[124:127], v15, s[16:19], s83 offen offset:1024 sc1\n"
"v_mfma_f32_16x16x32_bf16 v[128:131], a[0:3], a[64:67], v[128:131]\n"
"s_add_u32 s83, s83, s54\n"
"ds_read_b64_tr_b16 a[32:33], v2 offset:2048\n"
"v_mfma_f32_16x16x32_bf16 v[132:135], a[4:7], a[64:67], v[132:135]\n"
"ds_read_b64_tr_b16 a[34:35], v2 offset:10240\n"
"v_mfma_f32_16x16x32_bf16 v[136:139], a[8:11], a[64:67], v[136:139]\n"
"ds_read_b64_tr_b16 a[36:37], v3 offset:2048\n"
"v_mfma_f32_16x16x32_bf16 v[140:143], a[12:15], a[64:67], v[140:143]\n"
"s_waitcnt vmcnt(14)\n"
"ds_read_b64_tr_b16 a[38:39], v3 offset:10240\n"
"buffer_load_dwordx4 a[64:67], v14, s[16:19], s83 offen offset:0 sc1\n"
"v_mfma_f32_16x16x32_bf16 v[160:163], a[0:3], a[68:71], v[160:163]\n"
"ds_read_b64_tr_b16 a[40:41], v4 offset:2048\n"
"v_mfma_f32_16x16x32_bf16 v[164:167], a[4:7], a[68:71], v[164:167]\n"
"ds_read_b64_tr_b16 a[42:43], v4 offset:10240\n"
"v_mfma_f32_16x16x32_bf16 v[168:171], a[8:11], a[68:71], v[168:171]\n"
"ds_read_b64_tr_b16 a[44:45], v5 offset:2048\n"
"v_mfma_f32_16x16x32_bf16 v[172:175], a[12:15], a[68:71], v[172:175]\n"
"s_waitcnt vmcnt(14) lgkmcnt(7)\n"
"ds_read_b64_tr_b16 a[46:47], v5 offset:10240\n"
"buffer_load_dwordx4 a[68:71], v14, s[16:19], s83 offen offset:1024 sc1\n"
"v_mfma_f32_16x16x32_bf16 v[128:131], a[16:19], a[72:75], v[128:131]\n"
"ds_read_b64_tr_b16 a[48:49], v2 offset:3072\n"
"v_mfma_f32_16x16x32_bf16 v[132:135], a[20:23], a[72:75], v[132:135]\n"
"ds_read_b64_tr_b16 a[50:51], v2 offset:11264\n"
"v_mfma_f32_16x16x32_bf16 v[136:139], a[24:27], a[72:75], v[136:139]\n"
"ds_read_b64_tr_b16 a[52:53], v3 offset:3072\n"
"v_mfma_f32_16x16x32_bf16 v[140:143], a[28:31], a[72:75], v[140:143]\n"
"s_waitcnt vmcnt(14)\n"
"ds_read_b64_tr_b16 a[54:55], v3 offset:11264\n"
"buffer_load_dwordx4 a[72:75], v15, s[16:19], s83 offen offset:0 sc1\n"
"v_mfma_f32_16x16x32_bf16 v[160:163], a[16:19], a[76:79], v[160:163]\n"
"ds_read_b64_tr_b16 a[56:57], v4 offset:3072\n"
"v_mfma_f32_16x16x32_bf16 v[164:167], a[20:23], a[76:79], v[164:167]\n"
"ds_read_b64_tr_b16 a[58:59], v4 offset:11264\n"
"v_mfma_f32_16x16x32_bf16 v[168:171], a[24:27], a[76:79], v[168:171]\n"
"ds_read_b64_tr_b16 a[60:61], v5 offset:3072\n"
"v_mfma_f32_16x16x32_bf16 v[172:175], a[28:31], a[76:79], v[172:175]\n"
"s_waitcnt vmcnt(14) lgkmcnt(7)\n"
"ds_read_b64_tr_b16 a[62:63], v5 offset:11264\n"
"buffer_load_dwordx4 a[76:79], v15, s[16:19], s83 offen offset:1024 sc1\n"
"v_mfma_f32_16x16x32_bf16 v[128:131], a[32:35], a[80:83], v[128:131]\n"
"s_add_u32 s83, s83, s54\n"
"ds_read_b64_tr_b16 a[0:1], v2 offset:16384\n"
"v_mfma_f32_16x16x32_bf16 v[132:135], a[36:39], a[80:83], v[132:135]\n"
"ds_read_b64_tr_b16 a[2:3], v2 offset:24576\n"
"v_mfma_f32_16x16x32_bf16 v[136:139], a[40:43], a[80:83], v[136:139]\n"
"ds_read_b64_tr_b16 a[4:5], v3 offset:16384\n"
"v_mfma_f32_16x16x32_bf16 v[140:143], a[44:47], a[80:83], v[140:143]\n"
"s_waitcnt vmcnt(14)\n"
"ds_read_b64_tr_b16 a[6:7], v3 offset:24576\n"
"buffer_load_dwordx4 a[80:83], v14, s[16:19], s83 offen offset:0 sc1\n"
"v_mfma_f32_16x16x32_bf16 v[160:163], a[32:35], a[84:87], v[160:163]\n"
"ds_read_b64_tr_b16 a[8:9], v4 offset:16384\n"
"v_mfma_f32_16x16x32_bf16 v[164:167], a[36:39], a[84:87], v[164:167]\n"
"ds_read_b64_tr_b16 a[10:11], v4 offset:24576\n"
"v_mfma_f32_16x16x32_bf16 v[168:171], a[40:43], a[84:87], v[168:171]\n"
"ds_read_b64_tr_b16 a[12:13], v5 offset:16384\n"
"v_mfma_f32_16x16x32_bf16 v[172:175], a[44:47], a[84:87], v[172:175]\n"
"s_waitcnt vmcnt(14) lgkmcnt(7)\n"
"ds_read_b64_tr_b16 a[14:15], v5 offset:24576\n"
"buffer_load_dwordx4 a[84:87], v14, s[16:19], s83 offen offset:1024 sc1\n"
"v_mfma_f32_16x16x32_bf16 v[128:131], a[48:51], a[88:91], v[128:131]\n"
"ds_read_b64_tr_b16 a[16:17], v2 offset:17408\n"
"v_mfma_f32_16x16x32_bf16 v[132:135], a[52:55], a[88:91], v[132:135]\n"
"ds_read_b64_tr_b16 a[18:19], v2 offset:25600\n"
"v_mfma_f32_16x16x32_bf16 v[136:139], a[56:59], a[88:91], v[136:139]\n"
"ds_read_b64_tr_b16 a[20:21], v3 offset:17408\n"
"v_mfma_f32_16x16x32_bf16 v[140:143], a[60:63], a[88:91], v[140:143]\n"
"s_waitcnt vmcnt(14)\n"
"ds_read_b64_tr_b16 a[22:23], v3 offset:25600\n"
"buffer_load_dwordx4 a[88:91], v15, s[16:19], s83 offen offset:0 sc1\n"
"v_mfma_f32_16x16x32_bf16 v[160:163], a[48:51], a[92:95], v[160:163]\n"
"ds_read_b64_tr_b16 a[24:25], v4 offset:17408\n"
"v_mfma_f32_16x16x32_bf16 v[164:167], a[52:55], a[92:95], v[164:167]\n"
"ds_read_b64_tr_b16 a[26:27], v4 offset:25600\n"
"v_mfma_f32_16x16x32_bf16 v[168:171], a[56:59], a[92:95], v[168:171]\n"
"ds_read_b64_tr_b16 a[28:29], v5 offset:17408\n"
"v_mfma_f32_16x16x32_bf16 v[172:175], a[60:63], a[92:95], v[172:175]\n"
"s_waitcnt vmcnt(14) lgkmcnt(7)\n"
"ds_read_b64_tr_b16 a[30:31], v5 offset:25600\n"
"buffer_load_dwordx4 a[92:95], v15, s[16:19], s83 offen offset:1024 sc1\n"
"v_mfma_f32_16x16x32_bf16 v[128:131], a[0:3], a[96:99], v[128:131]\n"
"s_add_u32 s83, s83, s54\n"
"ds_read_b64_tr_b16 a[32:33], v2 offset:18432\n"
"v_mfma_f32_16x16x32_bf16 v[132:135], a[4:7], a[96:99], v[132:135]\n"
"ds_read_b64_tr_b16 a[34:35], v2 offset:26624\n"
"v_mfma_f32_16x16x32_bf16 v[136:139], a[8:11], a[96:99], v[136:139]\n"
"ds_read_b64_tr_b16 a[36:37], v3 offset:18432\n"
"v_mfma_f32_16x16x32_bf16 v[140:143], a[12:15], a[96:99], v[140:143]\n"
"s_waitcnt vmcnt(14)\n"
"ds_read_b64_tr_b16 a[38:39], v3 offset:26624\n"
"buffer_load_dwordx4 a[96:99], v14, s[16:19], s83 offen offset:0 sc1\n"
"v_mfma_f32_16x16x32_bf16 v[160:163], a[0:3], a[100:103], v[160:163]\n"
"ds_read_b64_tr_b16 a[40:41], v4 offset:18432\n"
"v_mfma_f32_16x16x32_bf16 v[164:167], a[4:7], a[100:103], v[164:167]\n"
"ds_read_b64_tr_b16 a[42:43], v4 offset:26624\n"
"v_mfma_f32_16x16x32_bf16 v[168:171], a[8:11], a[100:103], v[168:171]\n"
"ds_read_b64_tr_b16 a[44:45], v5 offset:18432\n"
"v_mfma_f32_16x16x32_bf16 v[172:175], a[12:15], a[100:103], v[172:175]\n"
"s_waitcnt vmcnt(14) lgkmcnt(7)\n"
"ds_read_b64_tr_b16 a[46:47], v5 offset:26624\n"
"buffer_load_dwordx4 a[100:103], v14, s[16:19], s83 offen offset:1024 sc1\n"
"v_mfma_f32_16x16x32_bf16 v[128:131], a[16:19], a[104:107], v[128:131]\n"
"ds_read_b64_tr_b16 a[48:49], v2 offset:19456\n"
"v_mfma_f32_16x16x32_bf16 v[132:135], a[20:23], a[104:107], v[132:135]\n"
"ds_read_b64_tr_b16 a[50:51], v2 offset:27648\n"
"v_mfma_f32_16x16x32_bf16 v[136:139], a[24:27], a[104:107], v[136:139]\n"
"ds_read_b64_tr_b16 a[52:53], v3 offset:19456\n"
"v_add_u32 v2, 65536, v6\n"
"v_mfma_f32_16x16x32_bf16 v[140:143], a[28:31], a[104:107], v[140:143]\n"
"s_waitcnt vmcnt(14)\n"
"ds_read_b64_tr_b16 a[54:55], v3 offset:27648\n"
"buffer_load_dwordx4 a[104:107], v15, s[16:19], s83 offen offset:0 sc1\n"
"v_mfma_f32_16x16x32_bf16 v[160:163], a[16:19], a[108:111], v[160:163]\n"
"ds_read_b64_tr_b16 a[56:57], v4 offset:19456\n"
"v_add_u32 v3, 65536, v7\n"
"v_mfma_f32_16x16x32_bf16 v[164:167], a[20:23], a[108:111], v[164:167]\n"
"ds_read_b64_tr_b16 a[58:59], v4 offset:27648\n"
"v_mfma_f32_16x16x32_bf16 v[168:171], a[24:27], a[108:111], v[168:171]\n"
"ds_read_b64_tr_b16 a[60:61], v5 offset:19456\n"
"v_add_u32 v4, 65536, v8\n"
"v_mfma_f32_16x16x32_bf16 v[172:175], a[28:31], a[108:111], v[172:175]\n"
"s_waitcnt vmcnt(14) lgkmcnt(7)\n"
"ds_read_b64_tr_b16 a[62:63], v5 offset:27648\n"
"buffer_load_dwordx4 a[108:111], v15, s[16:19], s83 offen offset:1024 sc1\n"
"v_mfma_f32_16x16x32_bf16 v[128:131], a[32:35], a[112:115], v[128:131]\n"
"s_add_u32 s83, s83, s54\n"
"ds_read_b64_tr_b16 a[0:1], v2 offset:0\n"
"v_add_u32 v5, 65536, v9\n"
"v_mfma_f32_16x16x32_bf16 v[132:135], a[36:39], a[112:115], v[132:135]\n"
"ds_read_b64_tr_b16 a[2:3], v2 offset:8192\n"
"v_mfma_f32_16x16x32_bf16 v[136:139], a[40:43], a[112:115], v[136:139]\n"
"ds_read_b64_tr_b16 a[4:5], v3 offset:0\n"
"v_mfma_f32_16x16x32_bf16 v[140:143], a[44:47], a[112:115], v[140:143]\n"
"s_waitcnt vmcnt(14)\n"
"ds_read_b64_tr_b16 a[6:7], v3 offset:8192\n"
"buffer_load_dwordx4 a[112:115], v14, s[16:19], s83 offen offset:0 sc1\n"
"v_mfma_f32_16x16x32_bf16 v[160:163], a[32:35], a[116:119], v[160:163]\n"
"ds_read_b64_tr_b16 a[8:9], v4 offset:0\n"
"v_mfma_f32_16x16x32_bf16 v[164:167], a[36:39], a[116:119], v[164:167]\n"
"ds_read_b64_tr_b16 a[10:11], v4 offset:8192\n"
"v_mfma_f32_16x16x32_bf16 v[168:171], a[40:43], a[116:119], v[168:171]\n"
"ds_read_b64_tr_b16 a[12:13], v5 offset:0\n"
"v_mfma_f32_16x16x32_bf16 v[172:175], a[44:47], a[116:119], v[172:175]\n"
"s_waitcnt vmcnt(14) lgkmcnt(7)\n"
"ds_read_b64_tr_b16 a[14:15], v5 offset:8192\n"
"buffer_load_dwordx4 a[116:119], v14, s[16:19], s83 offen offset:1024 sc1\n"
"v_mfma_f32_16x16x32_bf16 v[128:131], a[48:51], a[120:123], v[128:131]\n"
"ds_read_b64_tr_b16 a[16:17], v2 offset:1024\n"
"v_mfma_f32_16x16x32_bf16 v[132:135], a[52:55], a[120:123], v[132:135]\n"
"ds_read_b64_tr_b16 a[18:19], v2 offset:9216\n"
"v_mfma_f32_16x16x32_bf16 v[136:139], a[56:59], a[120:123], v[136:139]\n"
"ds_read_b64_tr_b16 a[20:21], v3 offset:1024\n"
"v_mfma_f32_16x16x32_bf16 v[140:143], a[60:63], a[120:123], v[140:143]\n"
"s_waitcnt vmcnt(14)\n"
"ds_read_b64_tr_b16 a[22:23], v3 offset:9216\n"
"buffer_load_dwordx4 a[120:123], v15, s[16:19], s83 offen offset:0 sc1\n"
"v_mfma_f32_16x16x32_bf16 v[160:163], a[48:51], a[124:127], v[160:163]\n"
"ds_read_b64_tr_b16 a[24:25], v4 offset:1024\n"
"v_mfma_f32_16x16x32_bf16 v[164:167], a[52:55], a[124:127], v[164:167]\n"
"ds_read_b64_tr_b16 a[26:27], v4 offset:9216\n"
"v_mfma_f32_16x16x32_bf16 v[168:171], a[56:59], a[124:127], v[168:171]\n"
"ds_read_b64_tr_b16 a[28:29], v5 offset:1024\n"
"v_mfma_f32_16x16x32_bf16 v[172:175], a[60:63], a[124:127], v[172:175]\n"
"s_waitcnt vmcnt(14) lgkmcnt(7)\n"
"ds_read_b64_tr_b16 a[30:31], v5 offset:9216\n"
"buffer_load_dwordx4 a[124:127], v15, s[16:19], s83 offen offset:1024 sc1\n"
"v_mfma_f32_16x16x32_bf16 v[128:131], a[0:3], a[64:67], v[128:131]\n"
"s_add_u32 s83, s83, s54\n"
"ds_read_b64_tr_b16 a[32:33], v2 offset:2048\n"
"v_mfma_f32_16x16x32_bf16 v[132:135], a[4:7], a[64:67], v[132:135]\n"
"ds_read_b64_tr_b16 a[34:35], v2 offset:10240\n"
"v_mfma_f32_16x16x32_bf16 v[136:139], a[8:11], a[64:67], v[136:139]\n"
"ds_read_b64_tr_b16 a[36:37], v3 offset:2048\n"
"v_mfma_f32_16x16x32_bf16 v[140:143], a[12:15], a[64:67], v[140:143]\n"
"s_waitcnt vmcnt(14)\n"
"ds_read_b64_tr_b16 a[38:39], v3 offset:10240\n"
"v_mfma_f32_16x16x32_bf16 v[160:163], a[0:3], a[68:71], v[160:163]\n"
"ds_read_b64_tr_b16 a[40:41], v4 offset:2048\n"
"v_mfma_f32_16x16x32_bf16 v[164:167], a[4:7], a[68:71], v[164:167]\n"
"ds_read_b64_tr_b16 a[42:43], v4 offset:10240\n"
"v_mfma_f32_16x16x32_bf16 v[168:171], a[8:11], a[68:71], v[168:171]\n"
"ds_read_b64_tr_b16 a[44:45], v5 offset:2048\n"
"v_mfma_f32_16x16x32_bf16 v[172:175], a[12:15], a[68:71], v[172:175]\n"
"s_waitcnt vmcnt(13) lgkmcnt(7)\n"
"ds_read_b64_tr_b16 a[46:47], v5 offset:10240\n"
"v_mfma_f32_16x16x32_bf16 v[128:131], a[16:19], a[72:75], v[128:131]\n"
"ds_read_b64_tr_b16 a[48:49], v2 offset:3072\n"
"v_mfma_f32_16x16x32_bf16 v[132:135], a[20:23], a[72:75], v[132:135]\n"
"ds_read_b64_tr_b16 a[50:51], v2 offset:11264\n"
"v_mfma_f32_16x16x32_bf16 v[136:139], a[24:27], a[72:75], v[136:139]\n"
"ds_read_b64_tr_b16 a[52:53], v3 offset:3072\n"
"v_mfma_f32_16x16x32_bf16 v[140:143], a[28:31], a[72:75], v[140:143]\n"
"s_waitcnt vmcnt(12)\n"
"ds_read_b64_tr_b16 a[54:55], v3 offset:11264\n"
"v_mfma_f32_16x16x32_bf16 v[160:163], a[16:19], a[76:79], v[160:163]\n"
"ds_read_b64_tr_b16 a[56:57], v4 offset:3072\n"
"v_mfma_f32_16x16x32_bf16 v[164:167], a[20:23], a[76:79], v[164:167]\n"
"ds_read_b64_tr_b16 a[58:59], v4 offset:11264\n"
"v_mfma_f32_16x16x32_bf16 v[168:171], a[24:27], a[76:79], v[168:171]\n"
"ds_read_b64_tr_b16 a[60:61], v5 offset:3072\n"
"v_mfma_f32_16x16x32_bf16 v[172:175], a[28:31], a[76:79], v[172:175]\n"
"s_waitcnt vmcnt(11) lgkmcnt(7)\n"
"ds_read_b64_tr_b16 a[62:63], v5 offset:11264\n"
"v_mfma_f32_16x16x32_bf16 v[128:131], a[32:35], a[80:83], v[128:131]\n"
"ds_read_b64_tr_b16 a[0:1], v2 offset:16384\n"
"v_mfma_f32_16x16x32_bf16 v[132:135], a[36:39], a[80:83], v[132:135]\n"
"ds_read_b64_tr_b16 a[2:3], v2 offset:24576\n"
"v_mfma_f32_16x16x32_bf16 v[136:139], a[40:43], a[80:83], v[136:139]\n"
"ds_read_b64_tr_b16 a[4:5], v3 offset:16384\n"
"v_mfma_f32_16x16x32_bf16 v[140:143], a[44:47], a[80:83], v[140:143]\n"
"s_waitcnt vmcnt(10)\n"
"ds_read_b64_tr_b16 a[6:7], v3 offset:24576\n"
"v_mfma_f32_16x16x32_bf16 v[160:163], a[32:35], a[84:87], v[160:163]\n"
"ds_read_b64_tr_b16 a[8:9], v4 offset:16384\n"
"v_mfma_f32_16x16x32_bf16 v[164:167], a[36:39], a[84:87], v[164:167]\n"
"ds_read_b64_tr_b16 a[10:11], v4 offset:24576\n"
"v_mfma_f32_16x16x32_bf16 v[168:171], a[40:43], a[84:87], v[168:171]\n"
"ds_read_b64_tr_b16 a[12:13], v5 offset:16384\n"
"v_mfma_f32_16x16x32_bf16 v[172:175], a[44:47], a[84:87], v[172:175]\n"
"s_waitcnt vmcnt(9) lgkmcnt(7)\n"
"ds_read_b64_tr_b16 a[14:15], v5 offset:24576\n"
"v_mfma_f32_16x16x32_bf16 v[128:131], a[48:51], a[88:91], v[128:131]\n"
"ds_read_b64_tr_b16 a[16:17], v2 offset:17408\n"
"v_mfma_f32_16x16x32_bf16 v[132:135], a[52:55], a[88:91], v[132:135]\n"
"ds_read_b64_tr_b16 a[18:19], v2 offset:25600\n"
"v_mfma_f32_16x16x32_bf16 v[136:139], a[56:59], a[88:91], v[136:139]\n"
"ds_read_b64_tr_b16 a[20:21], v3 offset:17408\n"
"v_mfma_f32_16x16x32_bf16 v[140:143], a[60:63], a[88:91], v[140:143]\n"
"s_waitcnt vmcnt(8)\n"
"ds_read_b64_tr_b16 a[22:23], v3 offset:25600\n"
"v_mfma_f32_16x16x32_bf16 v[160:163], a[48:51], a[92:95], v[160:163]\n"
"ds_read_b64_tr_b16 a[24:25], v4 offset:17408\n"
"v_mfma_f32_16x16x32_bf16 v[164:167], a[52:55], a[92:95], v[164:167]\n"
"ds_read_b64_tr_b16 a[26:27], v4 offset:25600\n"
"v_mfma_f32_16x16x32_bf16 v[168:171], a[56:59], a[92:95], v[168:171]\n"
"ds_read_b64_tr_b16 a[28:29], v5 offset:17408\n"
"v_mfma_f32_16x16x32_bf16 v[172:175], a[60:63], a[92:95], v[172:175]\n"
"s_waitcnt vmcnt(7) lgkmcnt(7)\n"
"ds_read_b64_tr_b16 a[30:31], v5 offset:25600\n"
"v_mfma_f32_16x16x32_bf16 v[128:131], a[0:3], a[96:99], v[128:131]\n"
"ds_read_b64_tr_b16 a[32:33], v2 offset:18432\n"
"v_mfma_f32_16x16x32_bf16 v[132:135], a[4:7], a[96:99], v[132:135]\n"
"ds_read_b64_tr_b16 a[34:35], v2 offset:26624\n"
"v_mfma_f32_16x16x32_bf16 v[136:139], a[8:11], a[96:99], v[136:139]\n"
"ds_read_b64_tr_b16 a[36:37], v3 offset:18432\n"
"v_mfma_f32_16x16x32_bf16 v[140:143], a[12:15], a[96:99], v[140:143]\n"
"s_waitcnt vmcnt(6)\n"
"ds_read_b64_tr_b16 a[38:39], v3 offset:26624\n"
"v_mfma_f32_16x16x32_bf16 v[160:163], a[0:3], a[100:103], v[160:163]\n"
"ds_read_b64_tr_b16 a[40:41], v4 offset:18432\n"
"v_mfma_f32_16x16x32_bf16 v[164:167], a[4:7], a[100:103], v[164:167]\n"
"ds_read_b64_tr_b16 a[42:43], v4 offset:26624\n"
"v_mfma_f32_16x16x32_bf16 v[168:171], a[8:11], a[100:103], v[168:171]\n"
"ds_read_b64_tr_b16 a[44:45], v5 offset:18432\n"
"v_mfma_f32_16x16x32_bf16 v[172:175], a[12:15], a[100:103], v[172:175]\n"
"s_waitcnt vmcnt(5) lgkmcnt(7)\n"
"ds_read_b64_tr_b16 a[46:47], v5 offset:26624\n"
"v_mfma_f32_16x16x32_bf16 v[128:131], a[16:19], a[104:107], v[128:131]\n"
"ds_read_b64_tr_b16 a[48:49], v2 offset:19456\n"
"v_mfma_f32_16x16x32_bf16 v[132:135], a[20:23], a[104:107], v[132:135]\n"
"ds_read_b64_tr_b16 a[50:51], v2 offset:27648\n"
"v_mfma_f32_16x16x32_bf16 v[136:139], a[24:27], a[104:107], v[136:139]\n"
"ds_read_b64_tr_b16 a[52:53], v3 offset:19456\n"
"v_mfma_f32_16x16x32_bf16 v[140:143], a[28:31], a[104:107], v[140:143]\n"
"s_waitcnt vmcnt(4)\n"
"ds_read_b64_tr_b16 a[54:55], v3 offset:27648\n"
"v_mfma_f32_16x16x32_bf16 v[160:163], a[16:19], a[108:111], v[160:163]\n"
"ds_read_b64_tr_b16 a[56:57], v4 offset:19456\n"
"v_mfma_f32_16x16x32_bf16 v[164:167], a[20:23], a[108:111], v[164:167]\n"
"ds_read_b64_tr_b16 a[58:59], v4 offset:27648\n"
"v_mfma_f32_16x16x32_bf16 v[168:171], a[24:27], a[108:111], v[168:171]\n"
"ds_read_b64_tr_b16 a[60:61], v5 offset:19456\n"
"v_mfma_f32_16x16x32_bf16 v[172:175], a[28:31], a[108:111], v[172:175]\n"
"s_waitcnt vmcnt(3) lgkmcnt(7)\n"
"ds_read_b64_tr_b16 a[62:63], v5 offset:27648\n"
"v_mfma_f32_16x16x32_bf16 v[128:131], a[32:35], a[112:115], v[128:131]\n"
"v_mfma_f32_16x16x32_bf16 v[132:135], a[36:39], a[112:115], v[132:135]\n"
"v_mfma_f32_16x16x32_bf16 v[136:139], a[40:43], a[112:115], v[136:139]\n"
"v_mfma_f32_16x16x32_bf16 v[140:143], a[44:47], a[112:115], v[140:143]\n"
"s_waitcnt vmcnt(1) lgkmcnt(6)\n"
"v_mfma_f32_16x16x32_bf16 v[128:131], a[48:51], a[120:123], v[128:131]\n"
"s_waitcnt lgkmcnt(4)\n"
"v_mfma_f32_16x16x32_bf16 v[132:135], a[52:55], a[120:123], v[132:135]\n"
"s_waitcnt lgkmcnt(2)\n"
"v_mfma_f32_16x16x32_bf16 v[136:139], a[56:59], a[120:123], v[136:139]\n"
"s_waitcnt lgkmcnt(0)\n"
"v_mfma_f32_16x16x32_bf16 v[140:143], a[60:63], a[120:123], v[140:143]\n"
"s_mov_b32 s88, 16384\n"
"v_mfma_f32_16x16x32_bf16 v[160:163], a[32:35], a[116:119], v[160:163]\n"
"s_mov_b32 s89, 32768\n"
"v_mfma_f32_16x16x32_bf16 v[164:167], a[36:39], a[116:119], v[164:167]\n"
"s_mov_b32 s90, 49152\n"
"v_mfma_f32_16x16x32_bf16 v[168:171], a[40:43], a[116:119], v[168:171]\n"
"v_cvt_pk_bf16_f32 v48, v128, v129\n"
"v_cvt_pk_bf16_f32 v49, v130, v131\n"
"v_mfma_f32_16x16x32_bf16 v[172:175], a[44:47], a[116:119], v[172:175]\n"
"s_waitcnt vmcnt(0)\n"
"v_cvt_pk_bf16_f32 v50, v132, v133\n"
"v_cvt_pk_bf16_f32 v51, v134, v135\n"
"v_mfma_f32_16x16x32_bf16 v[160:163], a[48:51], a[124:127], v[160:163]\n"
"v_cvt_pk_bf16_f32 v52, v136, v137\n"
"v_cvt_pk_bf16_f32 v53, v138, v139\n"
"v_mfma_f32_16x16x32_bf16 v[164:167], a[52:55], a[124:127], v[164:167]\n"
"v_cvt_pk_bf16_f32 v54, v140, v141\n"
"v_cvt_pk_bf16_f32 v55, v142, v143\n"
"v_mfma_f32_16x16x32_bf16 v[168:171], a[56:59], a[124:127], v[168:171]\n"
"v_permlane16_swap_b32 v48, v50\n"
"v_permlane16_swap_b32 v49, v51\n"
"v_mfma_f32_16x16x32_bf16 v[172:175], a[60:63], a[124:127], v[172:175]\n"
"ds_write_b128 v26, v[48:51] offset:0\n"
"v_permlane16_swap_b32 v52, v54\n"
"v_permlane16_swap_b32 v53, v55\n"
"ds_write_b128 v27, v[52:55] offset:0\n"
"ds_read_b128 v[64:67], v24 offset:0\n"
"ds_read_b128 v[68:71], v24 offset:1024\n"
"v_cvt_pk_bf16_f32 v56, v160, v161\n"
"v_cvt_pk_bf16_f32 v57, v162, v163\n"
"v_cvt_pk_bf16_f32 v58, v164, v165\n"
"v_cvt_pk_bf16_f32 v59, v166, v167\n"
"v_cvt_pk_bf16_f32 v60, v168, v169\n"
"v_cvt_pk_bf16_f32 v61, v170, v171\n"
"v_cvt_pk_bf16_f32 v62, v172, v173\n"
"v_cvt_pk_bf16_f32 v63, v174, v175\n"
"s_nop 0\n"
"v_permlane16_swap_b32 v56, v58\n"
"v_permlane16_swap_b32 v57, v59\n"
"ds_write_b128 v26, v[56:59] offset:2048\n"
"v_permlane16_swap_b32 v60, v62\n"
"v_permlane16_swap_b32 v61, v63\n"
"ds_write_b128 v27, v[60:63] offset:2048\n"
"ds_read_b128 v[72:75], v24 offset:2048\n"
"ds_read_b128 v[76:79], v24 offset:3072\n"
"s_waitcnt lgkmcnt(5)\n"
"buffer_store_dwordx4 v[64:67], v16, s[44:47], 0 offen nt\n"
"s_waitcnt lgkmcnt(4)\n"
"buffer_store_dwordx4 v[68:71], v16, s[44:47], s88 offen nt\n"
"s_waitcnt lgkmcnt(1)\n"
"buffer_store_dwordx4 v[72:75], v16, s[44:47], s89 offen nt\n"
"s_waitcnt lgkmcnt(0)\n"
"buffer_store_dwordx4 v[76:79], v16, s[44:47], s90 offen nt\n"
"s_endpgm\n"
"Lkend0:\n"
"Lslow_%=:\n"
"s_waitcnt vmcnt(0)\n"
"s_nop 11\n"
"v_mov_b32 v96, 0\n"
"v_mov_b32 v97, 0\n"
"v_mov_b32 v98, 0\n"
"v_mov_b32 v99, 0\n"
"v_mov_b32 v100, 0\n"
"v_mov_b32 v101, 0\n"
"v_mov_b32 v102, 0\n"
"v_mov_b32 v103, 0\n"
"v_mov_b32 v104, 0\n"
"v_mov_b32 v105, 0\n"
"v_mov_b32 v106, 0\n"
"v_mov_b32 v107, 0\n"
"v_mov_b32 v108, 0\n"
"v_mov_b32 v109, 0\n"
"v_mov_b32 v110, 0\n"
"v_mov_b32 v111, 0\n"
"v_mov_b32 v112, 0\n"
"v_mov_b32 v113, 0\n"
"v_mov_b32 v114, 0\n"
"v_mov_b32 v115, 0\n"
"v_mov_b32 v116, 0\n"
"v_mov_b32 v117, 0\n"
"v_mov_b32 v118, 0\n"
"v_mov_b32 v119, 0\n"
"v_mov_b32 v120, 0\n"
"v_mov_b32 v121, 0\n"
"v_mov_b32 v122, 0\n"
"v_mov_b32 v123, 0\n"
"v_mov_b32 v124, 0\n"
"v_mov_b32 v125, 0\n"
"v_mov_b32 v126, 0\n"
"v_mov_b32 v127, 0\n"
"v_mov_b32 v64, 0\n"
"v_mov_b32 v65, 0\n"
"v_mov_b32 v66, 0\n"
"v_mov_b32 v67, 0\n"
"v_mov_b32 v68, 0\n"
"v_mov_b32 v69, 0\n"
"v_mov_b32 v70, 0\n"
"v_mov_b32 v71, 0\n"
"v_mov_b32 v72, 0xff7fffff\n"
"v_mov_b32 v73, 0xff7fffff\n"
"s_mov_b32 s84, 0\n"
"Lsl_%=:\n"
"s_add_u32 s90, s84, 63\n"
"s_and_b32 s90, s90, 63\n"
"s_lshl_b32 s90, s90, 12\n"
"s_add_u32 s90, s90, 4194304\n"
"s_lshl_b32 s91, s84, 12\n"
"s_add_u32 s91, s91, 4194304\n"
"buffer_load_dwordx4 a[128:131], v18, s[16:19], s90 offen offset:0 sc1\n"
"buffer_load_dwordx4 a[132:135], v18, s[16:19], s90 offen offset:1024 sc1\n"
"buffer_load_dwordx4 a[136:139], v18, s[16:19], s91 offen offset:2048 sc1\n"
"buffer_load_dwordx4 a[140:143], v18, s[16:19], s91 offen offset:3072 sc1\n"
"s_waitcnt vmcnt(0)\n"
"v_mfma_f32_32x32x16_bf16 v[32:47], a[128:131], a[176:179], 0\n"
"v_mfma_f32_32x32x16_bf16 v[32:47], a[132:135], a[180:183], v[32:47]\n"
"s_nop 11\n"
"v_max3_f32 v74, v32, v33, v72\n"
"v_max3_f32 v74, v34, v35, v74\n"
"v_max3_f32 v74, v36, v37, v74\n"
"v_max3_f32 v74, v38, v39, v74\n"
"v_max3_f32 v74, v40, v41, v74\n"
"v_max3_f32 v74, v42, v43, v74\n"
"v_max3_f32 v74, v44, v45, v74\n"
"v_max3_f32 v74, v46, v47, v74\n"
"v_mov_b32 v75, v74\n"
"s_nop 1\n"
"v_permlane32_swap_b32 v74, v75\n"
"v_max_f32 v74, v74, v75\n"
"v_sub_f32 v75, v72, v74\n"
"v_exp_f32 v75, v75\n"
"v_mov_b32 v72, v74\n"
"v_sub_f32 v32, v32, v74\n"
"v_exp_f32 v32, v32\n"
"v_mul_f32 v96, v96, v75\n"
"v_sub_f32 v33, v33, v74\n"
"v_exp_f32 v33, v33\n"
"v_mul_f32 v97, v97, v75\n"
"v_sub_f32 v34, v34, v74\n"
"v_exp_f32 v34, v34\n"
"v_mul_f32 v98, v98, v75\n"
"v_sub_f32 v35, v35, v74\n"
"v_exp_f32 v35, v35\n"
"v_mul_f32 v99, v99, v75\n"
"v_sub_f32 v36, v36, v74\n"
"v_exp_f32 v36, v36\n"
"v_mul_f32 v100, v100, v75\n"
"v_sub_f32 v37, v37, v74\n"
"v_exp_f32 v37, v37\n"
"v_mul_f32 v101, v101, v75\n"
"v_sub_f32 v38, v38, v74\n"
"v_exp_f32 v38, v38\n"
"v_mul_f32 v102, v102, v75\n"
"v_sub_f32 v39, v39, v74\n"
"v_exp_f32 v39, v39\n"
"v_mul_f32 v103, v103, v75\n"
"v_sub_f32 v40, v40, v74\n"
"v_exp_f32 v40, v40\n"
"v_mul_f32 v104, v104, v75\n"
"v_sub_f32 v41, v41, v74\n"
"v_exp_f32 v41, v41\n"
"v_mul_f32 v105, v105, v75\n"
"v_sub_f32 v42, v42, v74\n"
"v_exp_f32 v42, v42\n"
"v_mul_f32 v106, v106, v75\n"
"v_sub_f32 v43, v43, v74\n"
"v_exp_f32 v43, v43\n"
"v_mul_f32 v107, v107, v75\n"
"v_sub_f32 v44, v44, v74\n"
"v_exp_f32 v44, v44\n"
"v_mul_f32 v108, v108, v75\n"
"v_sub_f32 v45, v45, v74\n"
"v_exp_f32 v45, v45\n"
"v_mul_f32 v109, v109, v75\n"
"v_sub_f32 v46, v46, v74\n"
"v_exp_f32 v46, v46\n"
"v_mul_f32 v110, v110, v75\n"
"v_sub_f32 v47, v47, v74\n"
"v_exp_f32 v47, v47\n"
"v_mul_f32 v111, v111, v75\n"
"v_mul_f32 v64, v64, v75\n"
"v_mul_f32 v65, v65, v75\n"
"s_sub_u32 s90, s84, 23\n"
"s_cmp_lt_u32 s90, 25\n"
"s_cbranch_scc0 Lsr0_%=\n"
"v_cvt_pk_bf16_f32 v80, v32, v33\n"
"v_cvt_pk_bf16_f32 v81, v34, v35\n"
"v_cvt_pk_bf16_f32 v82, v36, v37\n"
"v_cvt_pk_bf16_f32 v83, v38, v39\n"
"v_cvt_pk_bf16_f32 v84, v40, v41\n"
"v_cvt_pk_bf16_f32 v85, v42, v43\n"
"v_cvt_pk_bf16_f32 v86, v44, v45\n"
"v_cvt_pk_bf16_f32 v87, v46, v47\n"
"s_nop 1\n"
"v_mfma_f32_32x32x16_bf16 v[96:111], a[136:139], v[80:83], v[96:111]\n"
"v_mfma_f32_32x32x16_bf16 v[96:111], a[140:143], v[84:87], v[96:111]\n"
"v_smfmac_f32_16x16x64_bf16 v[64:67], a[252:255], v[80:87], v30\n"
"s_branch Lsj0_%=\n"
"Lsr0_%=:\n"
"v_smfmac_f32_32x32x32_bf16 v[96:111], a[136:139], v[32:39], v17\n"
"v_smfmac_f32_16x16x64_bf16 v[64:67], a[240:243], v[32:39], v17\n"
"v_smfmac_f32_32x32x32_bf16 v[96:111], a[140:143], v[40:47], v17\n"
"v_smfmac_f32_16x16x64_bf16 v[64:67], a[240:243], v[40:47], v17\n"
"Lsj0_%=:\n"
"v_mfma_f32_32x32x16_bf16 v[48:63], a[128:131], a[184:187], 0\n"
"v_mfma_f32_32x32x16_bf16 v[48:63], a[132:135], a[188:191], v[48:63]\n"
"s_nop 11\n"
"v_max3_f32 v74, v48, v49, v73\n"
"v_max3_f32 v74, v50, v51, v74\n"
"v_max3_f32 v74, v52, v53, v74\n"
"v_max3_f32 v74, v54, v55, v74\n"
"v_max3_f32 v74, v56, v57, v74\n"
"v_max3_f32 v74, v58, v59, v74\n"
"v_max3_f32 v74, v60, v61, v74\n"
"v_max3_f32 v74, v62, v63, v74\n"
"v_mov_b32 v75, v74\n"
"s_nop 1\n"
"v_permlane32_swap_b32 v74, v75\n"
"v_max_f32 v74, v74, v75\n"
"v_sub_f32 v75, v73, v74\n"
"v_exp_f32 v75, v75\n"
"v_mov_b32 v73, v74\n"
"v_sub_f32 v48, v48, v74\n"
"v_exp_f32 v48, v48\n"
"v_mul_f32 v112, v112, v75\n"
"v_sub_f32 v49, v49, v74\n"
"v_exp_f32 v49, v49\n"
"v_mul_f32 v113, v113, v75\n"
"v_sub_f32 v50, v50, v74\n"
"v_exp_f32 v50, v50\n"
"v_mul_f32 v114, v114, v75\n"
"v_sub_f32 v51, v51, v74\n"
"v_exp_f32 v51, v51\n"
"v_mul_f32 v115, v115, v75\n"
"v_sub_f32 v52, v52, v74\n"
"v_exp_f32 v52, v52\n"
"v_mul_f32 v116, v116, v75\n"
"v_sub_f32 v53, v53, v74\n"
"v_exp_f32 v53, v53\n"
"v_mul_f32 v117, v117, v75\n"
"v_sub_f32 v54, v54, v74\n"
"v_exp_f32 v54, v54\n"
"v_mul_f32 v118, v118, v75\n"
"v_sub_f32 v55, v55, v74\n"
"v_exp_f32 v55, v55\n"
"v_mul_f32 v119, v119, v75\n"
"v_sub_f32 v56, v56, v74\n"
"v_exp_f32 v56, v56\n"
"v_mul_f32 v120, v120, v75\n"
"v_sub_f32 v57, v57, v74\n"
"v_exp_f32 v57, v57\n"
"v_mul_f32 v121, v121, v75\n"
"v_sub_f32 v58, v58, v74\n"
"v_exp_f32 v58, v58\n"
"v_mul_f32 v122, v122, v75\n"
"v_sub_f32 v59, v59, v74\n"
"v_exp_f32 v59, v59\n"
"v_mul_f32 v123, v123, v75\n"
"v_sub_f32 v60, v60, v74\n"
"v_exp_f32 v60, v60\n"
"v_mul_f32 v124, v124, v75\n"
"v_sub_f32 v61, v61, v74\n"
"v_exp_f32 v61, v61\n"
"v_mul_f32 v125, v125, v75\n"
"v_sub_f32 v62, v62, v74\n"
"v_exp_f32 v62, v62\n"
"v_mul_f32 v126, v126, v75\n"
"v_sub_f32 v63, v63, v74\n"
"v_exp_f32 v63, v63\n"
"v_mul_f32 v127, v127, v75\n"
"v_mul_f32 v68, v68, v75\n"
"v_mul_f32 v69, v69, v75\n"
"s_sub_u32 s90, s84, 23\n"
"s_cmp_lt_u32 s90, 25\n"
"s_cbranch_scc0 Lsr1_%=\n"
"v_cvt_pk_bf16_f32 v80, v48, v49\n"
"v_cvt_pk_bf16_f32 v81, v50, v51\n"
"v_cvt_pk_bf16_f32 v82, v52, v53\n"
"v_cvt_pk_bf16_f32 v83, v54, v55\n"
"v_cvt_pk_bf16_f32 v84, v56, v57\n"
"v_cvt_pk_bf16_f32 v85, v58, v59\n"
"v_cvt_pk_bf16_f32 v86, v60, v61\n"
"v_cvt_pk_bf16_f32 v87, v62, v63\n"
"s_nop 1\n"
"v_mfma_f32_32x32x16_bf16 v[112:127], a[136:139], v[80:83], v[112:127]\n"
"v_mfma_f32_32x32x16_bf16 v[112:127], a[140:143], v[84:87], v[112:127]\n"
"v_smfmac_f32_16x16x64_bf16 v[68:71], a[252:255], v[80:87], v30\n"
"s_branch Lsj1_%=\n"
"Lsr1_%=:\n"
"v_smfmac_f32_32x32x32_bf16 v[112:127], a[136:139], v[48:55], v17\n"
"v_smfmac_f32_16x16x64_bf16 v[68:71], a[240:243], v[48:55], v17\n"
"v_smfmac_f32_32x32x32_bf16 v[112:127], a[140:143], v[56:63], v17\n"
"v_smfmac_f32_16x16x64_bf16 v[68:71], a[240:243], v[56:63], v17\n"
"Lsj1_%=:\n"
"s_nop 11\n"
"s_add_u32 s84, s84, 1\n"
"s_cmp_lt_u32 s84, 64\n"
"s_cbranch_scc1 Lsl_%=\n"
"s_mov_b32 s83, 0\n"
"v_add_f32 v64, v64, v65\n"
"v_rcp_f32 v64, v64\n"
"v_accvgpr_read_b32 v21, a248\n"
"v_pk_mul_f32 v[96:97], v[96:97], v[64:65] op_sel_hi:[1,0]\n"
"v_pk_mul_f32 v[98:99], v[98:99], v[64:65] op_sel_hi:[1,0]\n"
"v_pk_mul_f32 v[104:105], v[104:105], v[64:65] op_sel_hi:[1,0]\n"
"v_pk_mul_f32 v[106:107], v[106:107], v[64:65] op_sel_hi:[1,0]\n"
"v_cvt_pk_bf16_f32 v48, v96, v97\n"
"v_cvt_pk_bf16_f32 v49, v98, v99\n"
"v_cvt_pk_bf16_f32 v50, v104, v105\n"
"v_cvt_pk_bf16_f32 v51, v106, v107\n"
"buffer_store_dwordx4 v[48:51], v21, s[16:19], s76 offen offset:0 sc1\n"
"v_pk_mul_f32 v[100:101], v[100:101], v[64:65] op_sel_hi:[1,0]\n"
"v_pk_mul_f32 v[102:103], v[102:103], v[64:65] op_sel_hi:[1,0]\n"
"v_pk_mul_f32 v[108:109], v[108:109], v[64:65] op_sel_hi:[1,0]\n"
"v_pk_mul_f32 v[110:111], v[110:111], v[64:65] op_sel_hi:[1,0]\n"
"v_cvt_pk_bf16_f32 v52, v100, v101\n"
"v_cvt_pk_bf16_f32 v53, v102, v103\n"
"v_cvt_pk_bf16_f32 v54, v108, v109\n"
"v_cvt_pk_bf16_f32 v55, v110, v111\n"
"buffer_store_dwordx4 v[52:55], v21, s[16:19], s76 offen offset:512 sc1\n"
"s_branch Lrest_%=\n"
"Lf2s_%=:\n"
"s_mov_b32 s94, 200000\n"
"Lf2c_%=:\n"
"s_sleep 2\n"
"buffer_load_dwordx4 v[20:23], v18, s[16:19], s88 offen sc1\n"
"buffer_load_dwordx4 v[24:27], v18, s[16:19], s88 offen offset:1024 sc1\n"
"s_waitcnt vmcnt(0)\n"
"v_cmp_ne_u64 vcc, s[80:81], v[20:21]\n"
"v_cmp_ne_u64 s[92:93], s[80:81], v[22:23]\n"
"s_or_b64 vcc, vcc, s[92:93]\n"
"v_cmp_ne_u64 s[92:93], s[80:81], v[24:25]\n"
"s_or_b64 vcc, vcc, s[92:93]\n"
"v_cmp_ne_u64 s[92:93], s[80:81], v[26:27]\n"
"s_or_b64 vcc, vcc, s[92:93]\n"
"s_cbranch_vccz Lf2ok_%=\n"
"s_sub_u32 s94, s94, 1\n"
"s_cbranch_scc0 Lf2c_%=\n"
"s_branch Lf2ok_%=\n"
"Lf3s_%=:\n"
"s_mov_b32 s94, 200000\n"
"Lf3c_%=:\n"
"s_sleep 1\n"
"buffer_load_dwordx2 v[28:29], v25, s[16:19], s64 offen sc1\n"
"s_waitcnt vmcnt(0)\n"
"v_cmp_ne_u64 vcc, s[80:81], v[28:29]\n"
"s_cbranch_vccz Lf3ok_%=\n"
"s_sub_u32 s94, s94, 1\n"
"s_cbranch_scc0 Lf3c_%=\n"
"s_branch Lf3ok_%=\n"
"Lf5as_%=:\n"
"s_mov_b32 s94, 200000\n"
"Lf5ac_%=:\n"
"s_sleep 1\n"
"s_xor_b32 s88, s75, 24\n"
"s_add_u32 s88, s88, 262144\n"
"buffer_load_dwordx2 v[28:29], off, s[16:19], s88 sc1\n"
"s_waitcnt vmcnt(0)\n"
"v_cmp_ne_u64 vcc, s[80:81], v[28:29]\n"
"s_cbranch_vccz Lf5aok_%=\n"
"s_sub_u32 s94, s94, 1\n"
"s_cbranch_scc0 Lf5ac_%=\n"
"s_branch Lf5aok_%=\n"
"Lf5bs_%=:\n"
"s_mov_b32 s94, 200000\n"
"Lf5bc_%=:\n"
"s_sleep 1\n"
"s_xor_b32 s88, s75, 16\n"
"s_add_u32 s88, s88, 131072\n"
"buffer_load_dwordx2 v[28:29], off, s[16:19], s88 sc1\n"
"s_waitcnt vmcnt(0)\n"
"v_cmp_ne_u64 vcc, s[80:81], v[28:29]\n"
"s_cbranch_vccz Lf5bok_%=\n"
"s_sub_u32 s94, s94, 1\n"
"s_cbranch_scc0 Lf5bc_%=\n"
"s_branch Lf5bok_%=\n"
"Lf5cs_%=:\n"
"s_mov_b32 s94, 200000\n"
"Lf5cc_%=:\n"
"s_sleep 1\n"
"s_xor_b32 s88, s75, 8\n"
"s_add_u32 s88, s88, 0\n"
"buffer_load_dwordx2 v[28:29], off, s[16:19], s88 sc1\n"
"s_waitcnt vmcnt(0)\n"
"v_cmp_ne_u64 vcc, s[80:81], v[28:29]\n"
"s_cbranch_vccz Lf5cok_%=\n"
"s_sub_u32 s94, s94, 1\n"
"s_cbranch_scc0 Lf5cc_%=\n"
"s_branch Lf5cok_%=\n"
"Lf4s_%=:\n"
"s_mov_b32 s94, 200000\n"
"Lf4c_%=:\n"
"s_sleep 1\n"
"buffer_load_dwordx2 v[22:23], v25, s[16:19], s65 offen sc1\n"
"s_waitcnt vmcnt(0)\n"
"v_cmp_ne_u64 vcc, s[80:81], v[22:23]\n"
"s_cbranch_vccz Lf4ok_%=\n"
"s_sub_u32 s94, s94, 1\n"
"s_cbranch_scc0 Lf4c_%=\n"
"s_branch Lf4ok_%=\n"
      :
      : "{s[0:1]}"(__builtin_amdgcn_kernarg_segment_ptr()), "{s2}"(blockIdx.x), "{v0}"(threadIdx.x + (unsigned)(size_t)lds)
      : "memory", "scc", "vcc", "v1","v2","v3","v4","v5","v6","v7","v8","v9","v10","v11","v12","v13","v14","v15","v16","v17","v18","v19","v20","v21","v22","v23","v24","v25","v26","v27","v28","v29","v30","v31","v32","v33","v34","v35","v36","v37","v38","v39","v40","v41","v42","v43","v44","v45","v46","v47","v48","v49","v50","v51","v52","v53","v54","v55","v56","v57","v58","v59","v60","v61","v62","v63","v64","v65","v66","v67","v68","v69","v70","v71","v72","v73","v74","v75","v76","v77","v78","v79","v80","v81","v82","v83","v84","v85","v86","v87","v88","v89","v90","v91","v92","v93","v94","v95","v96","v97","v98","v99","v100","v101","v102","v103","v104","v105","v106","v107","v108","v109","v110","v111","v112","v113","v114","v115","v116","v117","v118","v119","v120","v121","v122","v123","v124","v125","v126","v127","v128","v129","v130","v131","v132","v133","v134","v135","v136","v137","v138","v139","v140","v141","v142","v143","v144","v145","v146","v147","v148","v149","v150","v151","v152","v153","v154","v155","v156","v157","v158","v159","v160","v161","v162","v163","v164","v165","v166","v167","v168","v169","v170","v171","v172","v173","v174","v175","v176","v177","v178","v179","v180","v181","v182","v183","v184","v185","v186","v187","v188","v189","v190","v191","v192","v193","v194","v195","v196","v197","v198","v199","v200","v201","v202","v203","v204","v205","v206","v207","v208","v209","v210","v211","v212","v213","v214","v215","v216","v217","v218","v219","v220","v221","v222","v223","v224","v225","v226","v227","v228","v229","v230","v231","v232","v233","v234","v235","v236","v237","v238","v239","v240","v241","v242","v243","v244","v245","v246","v247","v248","v249","v250","v251","v252","v253","v254","v255","a0","a1","a2","a3","a4","a5","a6","a7","a8","a9","a10","a11","a12","a13","a14","a15","a16","a17","a18","a19","a20","a21","a22","a23","a24","a25","a26","a27","a28","a29","a30","a31","a32","a33","a34","a35","a36","a37","a38","a39","a40","a41","a42","a43","a44","a45","a46","a47","a48","a49","a50","a51","a52","a53","a54","a55","a56","a57","a58","a59","a60","a61","a62","a63","a64","a65","a66","a67","a68","a69","a70","a71","a72","a73","a74","a75","a76","a77","a78","a79","a80","a81","a82","a83","a84","a85","a86","a87","a88","a89","a90","a91","a92","a93","a94","a95","a96","a97","a98","a99","a100","a101","a102","a103","a104","a105","a106","a107","a108","a109","a110","a111","a112","a113","a114","a115","a116","a117","a118","a119","a120","a121","a122","a123","a124","a125","a126","a127","a128","a129","a130","a131","a132","a133","a134","a135","a136","a137","a138","a139","a140","a141","a142","a143","a144","a145","a146","a147","a148","a149","a150","a151","a152","a153","a154","a155","a156","a157","a158","a159","a160","a161","a162","a163","a164","a165","a166","a167","a168","a169","a170","a171","a172","a173","a174","a175","a176","a177","a178","a179","a180","a181","a182","a183","a184","a185","a186","a187","a188","a189","a190","a191","a192","a193","a194","a195","a196","a197","a198","a199","a200","a201","a202","a203","a204","a205","a206","a207","a208","a209","a210","a211","a212","a213","a214","a215","a216","a217","a218","a219","a220","a221","a222","a223","a224","a225","a226","a227","a228","a229","a230","a231","a232","a233","a234","a235","a236","a237","a238","a239","a240","a241","a242","a243","a244","a245","a246","a247","a248","a249","a250","a251","a252","a253","a254","a255","s3","s4","s5","s6","s7","s8","s9","s10","s11","s12","s13","s14","s15","s16","s17","s18","s19","s20","s21","s22","s23","s24","s25","s26","s27","s28","s29","s30","s31","s33","s34","s35","s36","s37","s38","s39","s40","s41","s42","s43","s44","s45","s46","s47","s48","s49","s50","s51","s52","s53","s54","s55","s56","s57","s58","s59","s60","s61","s62","s63","s64","s65","s66","s67","s68","s69","s70","s71","s72","s73","s74","s75","s76","s77","s78","s79","s80","s81","s82","s83","s84","s85","s86","s87","s88","s89","s90","s91","s92","s93","s94","s95","s96","s97","s98","s99");
}

extern "C" void kernel_launch(void* const* d_in, const int* in_sizes, int n_in, void* d_out, int out_size, void* d_ws, size_t ws_size, hipStream_t stream) {
  fused_block<<<256, 256, 0, stream>>>((const u16*)d_in[0], (const u16*)d_in[1], (const u16*)d_in[2], (const u16*)d_in[3], (const u16*)d_in[4],
                                       (const u16*)d_in[6], (const u16*)d_in[7], (const u16*)d_in[8], (char*)d_ws, (u16*)d_out);
}
```
